```python
import torch

def _softmax(x, axis=-1):
    return torch.softmax(x, dim=axis)
def _gelu(x):
    return torch.nn.functional.gelu(x, approximate="tanh")
def _silu(x):
    return torch.nn.functional.silu(x)
def _sigmoid(x):
    return torch.sigmoid(x)
def _layernorm(x, axis=-1, eps=1e-5):
    m = x.mean(dim=axis, keepdim=True)
    v = x.var(dim=axis, keepdim=True, unbiased=False)
    return (x - m) / torch.sqrt(v + eps)
def _scatter_add(tgt, idx, src, axis=0):
    return tgt.clone().index_add_(axis, idx, src)
def _segment_sum(src, seg, n):
    out = torch.zeros((n,) + src.shape[1:], dtype=src.dtype)
    return out.index_add_(0, seg, src)
def _pad(x, pw):
    flat = tuple(v for pair in reversed(pw) for v in pair)
    return torch.nn.functional.pad(x, flat)

kernel_name = 'gated_mix__linear_attn_block__ssm_conv_s4d__prompt'

def setup_inputs():

    _w = (lambda a: (lambda t: t.to(torch.bfloat16) if t.is_floating_point() else t)(torch.as_tensor(a)))
    def _A_setup():
        B, S, D = 1, 2048, 512
        return {
            "x": _w((lambda *s: torch.randn(*s, dtype=torch.bfloat16) * 0.05)(B, S, D)),
            "wqkv": _w((lambda *s: torch.randn(*s, dtype=torch.bfloat16) * 0.8)(D, 3 * D)),
            "wo": _w((lambda *s: torch.randn(*s, dtype=torch.bfloat16) * 0.05)(D, D)),
        }
    _A_in = _A_setup()

    def _B_setup():
        Bsz, L, D, N = 1, 2048, 512, 128
        return {
            "u": _w((lambda *s: torch.randn(*s, dtype=torch.bfloat16) * 0.05)(Bsz, L, D)),
            "log_A_real": _w((lambda *s: torch.randn(*s, dtype=torch.bfloat16) * 6.4)(D, N) * 0.5),
            "A_imag": _w((lambda *s: torch.randn(*s, dtype=torch.bfloat16) * 0.05)(D, N) * 3.14),
            "C_real": _w((lambda *s: torch.randn(*s, dtype=torch.bfloat16) * 0.2)(D, N) / N ** 0.5),
            "C_imag": _w((lambda *s: torch.randn(*s, dtype=torch.bfloat16) * 0.2)(D, N) / N ** 0.5),
            "log_dt": _w((lambda *s: torch.randn(*s, dtype=torch.bfloat16) * 0.05)(D) * 0.5 - 4.0),
            "D_skip": _w((lambda *s: torch.randn(*s, dtype=torch.bfloat16) * 0.05)(D)),
        }
    _B_in = _B_setup()

    return {
        "A_x": _A_in["x"],
        "A_wqkv": _A_in["wqkv"],
        "A_wo": _A_in["wo"],
        "B_u": _B_in["u"],
        "B_log_A_real": _B_in["log_A_real"],
        "B_A_imag": _B_in["A_imag"],
        "B_C_real": _B_in["C_real"],
        "B_C_imag": _B_in["C_imag"],
        "B_log_dt": _B_in["log_dt"],
        "B_D_skip": _B_in["D_skip"],
        "gate_w": _w((lambda *s: torch.randn(*s, dtype=torch.bfloat16) * 0.05)(512) * 16.0),
    }

def reference(A_x, A_wqkv, A_wo, B_u, B_log_A_real, B_A_imag, B_C_real, B_C_imag, B_log_dt, B_D_skip, gate_w):
    def _A_ref(x, wqkv, wo):
        B, S, D = x.shape
        H = 32; Dh = D // H
        qkv = (lambda x, *d: x.permute(*d))((x @ wqkv).reshape(B, S, 3, H, Dh), 2, 0, 3, 1, 4)
        q, k, v = qkv[0], qkv[1], qkv[2]
        phi = lambda t: torch.where(t > 0, t + 1, torch.exp(t)) + 1e-6
        pq, pk = phi(q), phi(k)
        kv = torch.einsum("bhsd,bhse->bhde", pk, v)
        z = torch.einsum("bhsd->bhd", pk)
        num = torch.einsum("bhqd,bhde->bhqe", pq, kv)
        den = torch.einsum("bhqd,bhd->bhq", pq, z)[..., None] + 1e-6
        o = num / den
        return (lambda x, *d: x.permute(*d))(o, 0, 2, 1, 3).reshape(B, S, D) @ wo

    def _B_ref(u, log_A_real, A_imag, C_real, C_imag, log_dt, D_skip):
        L = u.shape[1]
        dt = torch.exp(log_dt)
        A = -torch.exp(log_A_real) + 1j * A_imag
        C = (C_real + 1j * C_imag) * dt[:, None]
        pos = (lambda x, dt: x.to(dt))(torch.arange(L), log_dt.dtype)
        P = torch.exp((A * dt[:, None])[:, :, None] * pos[None, None, :])
        K = 2.0 * torch.einsum("dn,dnl->dl", C, P).real
        L2 = 2 * L
        u_t = (lambda x, *d: x.permute(*d))(u, 0, 2, 1)
        Kf = (lambda x, *a, **k: torch.fft.rfft(x.float(), *a, **k))(K, L2)
        Uf = (lambda x, *a, **k: torch.fft.rfft(x.float(), *a, **k))(u_t, L2)
        y = (lambda x, *a, **k: torch.fft.irfft(x, *a, **k).to(torch.bfloat16))(Uf * Kf[None, :, :], L2)[:, :, :L]
        y = (lambda x, *d: x.permute(*d))(y, 0, 2, 1)
        return y + u * D_skip[None, None, :]

    x = A_x
    g = _sigmoid((x * gate_w).sum(-1, keepdims=True))
    a = _A_ref(x, A_wqkv, A_wo)
    b = _B_ref(x, B_log_A_real, B_A_imag, B_C_real, B_C_imag, B_log_dt, B_D_skip)
    return g * a + (1 - g) * b
```

```cpp
#include <hip/hip_runtime.h>
#include <hip/hip_bf16.h>
#include <stdint.h>
typedef __hip_bfloat16 bf16;
static constexpr int S = 2048, D = 512, H = 32, DH = 16, NM = 128;
__device__ __forceinline__ float b2f(bf16 v) { return __bfloat162float(v); }
__device__ __forceinline__ float rb(float v) { return __bfloat162float(__float2bfloat16(v)); }
__device__ __forceinline__ float phi(float t) { float r = t > 0.f ? rb(t + 1.f) : rb(expf(t)); return rb(r + 1e-6f); }

__global__ void k_qkv(const bf16* x, const bf16* w, float* qkv) {
  int n = blockIdx.x * 64 + (threadIdx.x & 63);
  int s = blockIdx.y * 4 + (threadIdx.x >> 6);
  float acc = 0.f;
  for (int k = 0; k < D; ++k) acc += b2f(x[s * D + k]) * b2f(w[k * 3 * D + n]);
  acc = rb(acc);
  if (n < 2 * D) acc = phi(acc);
  qkv[s * 3 * D + n] = acc;
}
__global__ void k_kv(const float* qkv, float* kv, float* z) {
  int h = blockIdx.x; int d = threadIdx.x >> 4, e = threadIdx.x & 15;
  float acc = 0.f, zz = 0.f;
  for (int s = 0; s < S; ++s) { float pk = qkv[s * 3 * D + D + h * 16 + d]; acc += pk * qkv[s * 3 * D + 2 * D + h * 16 + e]; zz += pk; }
  kv[h * 256 + d * 16 + e] = rb(acc);
  if (e == 0) z[h * 16 + d] = rb(zz);
}
__global__ void k_o(const float* qkv, const float* kv, const float* z, float* o) {
  int s = blockIdx.x; int c = threadIdx.x; int h = c >> 4, e = c & 15;
  float num = 0.f, den = 0.f;
  for (int d = 0; d < 16; ++d) { float pq = qkv[s * 3 * D + h * 16 + d]; num += pq * kv[h * 256 + d * 16 + e]; den += pq * z[h * 16 + d]; }
  num = rb(num); den = rb(rb(den) + 1e-6f);
  o[s * D + c] = rb(num / den);
}
__global__ void k_kgen(const bf16* lar, const bf16* aim, const bf16* cr, const bf16* ci, const bf16* ldt, float* K) {
  int d = blockIdx.y; int l = blockIdx.x * 256 + threadIdx.x;
  float dt = rb(expf(b2f(ldt[d])));
  float pos = rb((float)l);
  float acc = 0.f;
  for (int n = 0; n < NM; ++n) {
    float ar = -rb(expf(b2f(lar[d * NM + n]))) * dt, ai = b2f(aim[d * NM + n]) * dt;
    float c_r = b2f(cr[d * NM + n]) * dt, c_i = b2f(ci[d * NM + n]) * dt;
    float e = expf(ar * pos); float sn, cs; sincosf(ai * pos, &sn, &cs);
    acc += c_r * e * cs - c_i * e * sn;
  }
  K[d * S + l] = 2.f * acc;
}
__global__ void k_conv(const bf16* x, const float* K, const bf16* dskip, float* b) {
  int d = blockIdx.x * 64 + (threadIdx.x & 63); int t = blockIdx.y * 4 + (threadIdx.x >> 6);
  float acc = 0.f;
  for (int l = 0; l <= t; ++l) acc += K[d * S + l] * b2f(x[(t - l) * D + d]);
  b[t * D + d] = rb(rb(acc) + rb(b2f(x[t * D + d]) * b2f(dskip[d])));
}
__global__ void k_final(const bf16* x, const float* o, const bf16* wo, const float* b, const bf16* gw, bf16* out) {
  int s = blockIdx.x; int c = threadIdx.x;
  __shared__ float red[512];
  red[c] = rb(b2f(x[s * D + c]) * b2f(gw[c]));
  __syncthreads();
  for (int st = 256; st > 0; st >>= 1) { if (c < st) red[c] += red[c + st]; __syncthreads(); }
  float g = rb(1.f / (1.f + expf(-rb(red[0]))));
  float acc = 0.f;
  for (int k = 0; k < D; ++k) acc += o[s * D + k] * b2f(wo[k * D + c]);
  float a = rb(acc);
  out[s * D + c] = __float2bfloat16(rb(g * a) + rb(rb(1.f - g) * b[s * D + c]));
}

extern "C" void kernel_launch(void* const* d_in, const int* in_sizes, int n_in,
                              void* d_out, int out_size, void* d_ws, size_t ws_size,
                              hipStream_t stream) {
  const bf16* x = (const bf16*)d_in[0]; const bf16* wqkv = (const bf16*)d_in[1]; const bf16* wo = (const bf16*)d_in[2];
  const bf16* lar = (const bf16*)d_in[4]; const bf16* aim = (const bf16*)d_in[5]; const bf16* cr = (const bf16*)d_in[6];
  const bf16* ci = (const bf16*)d_in[7]; const bf16* ldt = (const bf16*)d_in[8]; const bf16* dsk = (const bf16*)d_in[9];
  const bf16* gw = (const bf16*)d_in[10];
  float* ws = (float*)d_ws;
  float* qkv = ws; float* kv = qkv + S * 3 * D; float* z = kv + H * 256; float* o = z + 512; float* K = o + S * D; float* b = K + D * S;
  k_qkv<<<dim3(24, S / 4), 256, 0, stream>>>(x, wqkv, qkv);
  k_kv<<<H, 256, 0, stream>>>(qkv, kv, z);
  k_o<<<S, 512, 0, stream>>>(qkv, kv, z, o);
  k_kgen<<<dim3(S / 256, D), 256, 0, stream>>>(lar, aim, cr, ci, ldt, K);
  k_conv<<<dim3(D / 64, S / 4), 256, 0, stream>>>(x, K, dsk, b);
  k_final<<<S, 512, 0, stream>>>(x, o, wo, b, gw, (bf16*)d_out);
}
```

```cpp
#include <hip/hip_runtime.h>
#include <hip/hip_bf16.h>
#include <stdint.h>

typedef unsigned int u32;
typedef unsigned short u16;
typedef __attribute__((ext_vector_type(8))) short s16x8;
typedef __attribute__((ext_vector_type(4))) short s16x4;
typedef __attribute__((ext_vector_type(8))) _Float16 h16x8;
typedef __attribute__((ext_vector_type(2))) _Float16 h16x2;
typedef __attribute__((ext_vector_type(2))) __bf16 b16x2;
typedef __attribute__((ext_vector_type(2))) float f32x2;
typedef __attribute__((ext_vector_type(4))) float f32x4;
typedef __attribute__((ext_vector_type(16))) float f32x16;
typedef __attribute__((ext_vector_type(2))) u32 u32x2;
typedef __attribute__((ext_vector_type(4))) u32 u32x4;
#define LDS_AS __attribute__((address_space(3)))
#define DI __device__ __forceinline__

static constexpr int SEQ = 2048, DM = 512;
static constexpr size_t WS_KV = 0;
static constexpr size_t WS_G = 65536;
static constexpr size_t WS_PQ = 1u << 20;
static constexpr size_t WS_BT = 4u << 20;
static constexpr int KV_BYTES = 32 * 272 * 4;

#define WAIT_VM(n) asm volatile("s_waitcnt vmcnt(" #n ")" ::: "memory")
#define WAIT_LGKM0() asm volatile("s_waitcnt lgkmcnt(0)" ::: "memory")
#define RAW_BAR() __builtin_amdgcn_s_barrier()

DI u32 pk_bf16(float a, float b) { return __builtin_bit_cast(u32, __builtin_convertvector((f32x2){a, b}, b16x2)); }
DI u32 pk_f16(f32x2 v) { return __builtin_bit_cast(u32, __builtin_convertvector(v, h16x2)); }
DI float bf_lo(u32 v) { return __builtin_bit_cast(float, v << 16); }
DI float bf_hi(u32 v) { return __builtin_bit_cast(float, v & 0xffff0000u); }
DI float rbf(float v) { return bf_lo(pk_bf16(v, v)); }
DI float phi_f(float t) {
  float e = __builtin_amdgcn_exp2f(fminf(t, 0.f) * 1.44269504088896341f);
  return (fmaxf(t, 0.f) + 1e-6f) + e;
}
DI s16x4 tr_read(const char* p) { return __builtin_amdgcn_ds_read_tr16_b64_v4i16((LDS_AS s16x4*)p); }
DI s16x8 cat8(s16x4 a, s16x4 b) { return __builtin_shufflevector(a, b, 0, 1, 2, 3, 4, 5, 6, 7); }
DI void glds16(const void* g, char* l) {
  __builtin_amdgcn_global_load_lds((const __attribute__((address_space(1))) void*)g, (LDS_AS void*)l, 16, 0, 0);
}
DI f32x2 cmul(f32x2 a, f32x2 w) {
  f32x2 wr = {-w.y, w.x};
  return (f32x2){a.x, a.x} * w + (f32x2){a.y, a.y} * wr;
}
DI f32x2 cexp_p(float alpha, float theta, float p) {
  float m = __builtin_amdgcn_exp2f(alpha * p);
  float f = __builtin_amdgcn_fractf(theta * p);
  return (f32x2){m * __builtin_amdgcn_cosf(f), m * __builtin_amdgcn_sinf(f)};
}
#define MFMA16(a, b, c) __builtin_amdgcn_mfma_f32_16x16x32_bf16((a), (b), (c), 0, 0, 0)
#define MFMA16H(a, b, c) __builtin_amdgcn_mfma_f32_16x16x32_f16((a), (b), (c), 0, 0, 0)
#define MFMA32(a, b, c) __builtin_amdgcn_mfma_f32_32x32x16_bf16((a), (b), (c), 0, 0, 0)

static constexpr int STAGE = 40960;

DI void gemm_role(const u16* __restrict__ x, const u16* __restrict__ wqkv, char* __restrict__ ws, int tt, int hg, char* smem) {
  const int tid = threadIdx.x, lane = tid & 63;
  const int w = __builtin_amdgcn_readfirstlane(tid >> 6);
  const int wm = w >> 2, wh = w & 3, g = lane >> 4, l15 = lane & 15;
  const int sr = lane >> 3, sp = lane & 7;
  const u16* xs = x + (size_t)(tt * 128 + 16 * w + sr) * DM + ((sp ^ sr) << 3);
  const int fw = ((sr >> 1) & 1) | ((w & 1) << 1);
  const u16* wsrc = wqkv + (size_t)(8 * w + sr) * 1536 + hg * 64 + ((sp ^ (fw << 1)) << 3);
#define ISSUE(s) do { char* sb = smem + ((s) & 3) * STAGE; \
    glds16(xs + (s) * 64, sb + w * 2048); glds16(xs + (s) * 64 + 8 * DM, sb + w * 2048 + 1024); \
    glds16(wsrc + (size_t)(s) * 64 * 1536, sb + 16384 + w * 1024); \
    glds16(wsrc + (size_t)(s) * 64 * 1536 + 512, sb + 16384 + 8192 + w * 1024); \
    glds16(wsrc + (size_t)(s) * 64 * 1536 + 1024, sb + 16384 + 16384 + w * 1024); } while (0)
  ISSUE(0); ISSUE(1); ISSUE(2); ISSUE(3);
  const int xo = (64 * wm + l15) * 128 + ((g ^ (lane & 7)) << 4);
  const int q4 = l15 >> 2, p4 = lane & 3;
  const int wo_ = 16384 + (8 * g + q4) * 128 + ((wh ^ (((q4 >> 1) & 1) | ((g & 1) << 1))) << 5) + 8 * p4;
  f32x4 aq[4], ak[4], av[4];
#pragma unroll
  for (int i = 0; i < 4; ++i) { aq[i] = (f32x4){0, 0, 0, 0}; ak[i] = aq[i]; av[i] = aq[i]; }
#define COMPUTE(s) do { const char* sb = smem + ((s) & 3) * STAGE; \
    _Pragma("unroll") for (int ks = 0; ks < 2; ++ks) { \
      s16x8 xf[4], wf[3]; \
      _Pragma("unroll") for (int st = 0; st < 3; ++st) \
        wf[st] = cat8(tr_read(sb + wo_ + st * 8192 + ks * 4096), tr_read(sb + wo_ + st * 8192 + ks * 4096 + 512)); \
      _Pragma("unroll") for (int mt = 0; mt < 4; ++mt) xf[mt] = *(const s16x8*)(sb + ((xo ^ (ks * 64)) + mt * 2048)); \
      _Pragma("unroll") for (int mt = 0; mt < 4; ++mt) { \
        aq[mt] = MFMA16(wf[0], xf[mt], aq[mt]); ak[mt] = MFMA16(xf[mt], wf[1], ak[mt]); av[mt] = MFMA16(xf[mt], wf[2], av[mt]); } \
    } } while (0)
  WAIT_VM(15); RAW_BAR(); COMPUTE(0);
  WAIT_VM(10); WAIT_LGKM0(); RAW_BAR(); ISSUE(4); COMPUTE(1);
  WAIT_VM(10); WAIT_LGKM0(); RAW_BAR(); ISSUE(5); COMPUTE(2);
  WAIT_VM(10); WAIT_LGKM0(); RAW_BAR(); ISSUE(6); COMPUTE(3);
  WAIT_VM(10); WAIT_LGKM0(); RAW_BAR(); ISSUE(7); COMPUTE(4);
  WAIT_VM(10); WAIT_LGKM0(); RAW_BAR(); COMPUTE(5);
  WAIT_VM(5); WAIT_LGKM0(); RAW_BAR(); COMPUTE(6);
  WAIT_VM(0); WAIT_LGKM0(); RAW_BAR(); COMPUTE(7);
#undef ISSUE
#undef COMPUTE
  const int h = 4 * hg + wh;
  f32x4 kvt = {0, 0, 0, 0}, zz = {0, 0, 0, 0};
  const u32x4 ones_u = {0x3f803f80u, 0x3f803f80u, 0x3f803f80u, 0x3f803f80u};
#pragma unroll
  for (int pr = 0; pr < 2; ++pr) {
    u32x4 pk, vv;
#pragma unroll
    for (int m = 0; m < 2; ++m) {
      const f32x4 kk = ak[2 * pr + m], v4 = av[2 * pr + m];
      pk[2 * m] = pk_bf16(phi_f(kk[0]), phi_f(kk[1])); pk[2 * m + 1] = pk_bf16(phi_f(kk[2]), phi_f(kk[3]));
      vv[2 * m] = pk_bf16(v4[0], v4[1]); vv[2 * m + 1] = pk_bf16(v4[2], v4[3]);
    }
    kvt = MFMA16(__builtin_bit_cast(s16x8, vv), __builtin_bit_cast(s16x8, pk), kvt);
    zz = MFMA16(__builtin_bit_cast(s16x8, ones_u), __builtin_bit_cast(s16x8, pk), zz);
  }
  float* red = (float*)smem + (wm * 4 + wh) * 272;
#pragma unroll
  for (int r = 0; r < 4; ++r) red[(4 * g + r) * 16 + l15] = kvt[r];
  if (g == 0) red[256 + l15] = zz[0];
  char* pqb = ws + WS_PQ + ((size_t)((tt * 4 + 2 * wm) * 32 + h) * 2 + (g >> 1)) * 512 + l15 * 16 + (g & 1) * 8;
#pragma unroll
  for (int mt = 0; mt < 4; ++mt) {
    const f32x4 qq = aq[mt];
    u32x2 o = {pk_bf16(phi_f(qq[0]), phi_f(qq[1])), pk_bf16(phi_f(qq[2]), phi_f(qq[3]))};
    *(u32x2*)(pqb + (mt >> 1) * 32768 + (mt & 1) * 256) = o;
  }
  __syncthreads();
  float* kvacc = (float*)(ws + WS_KV) + hg * 1088;
  const float* rs = (const float*)smem;
  atomicAdd(kvacc + tid, rs[tid] + rs[1088 + tid]);
  atomicAdd(kvacc + 512 + tid, rs[512 + tid] + rs[1088 + 512 + tid]);
  if (tid < 64) atomicAdd(kvacc + 1024 + tid, rs[1024 + tid] + rs[1088 + 1024 + tid]);
}

static constexpr int TB_ROW = 544, TB_CH = 63 * TB_ROW;
static constexpr int U_OFF = 4 * TB_CH, U_CH = (2048 + 240) * 2;
static constexpr int KR_CH = 4160;
static constexpr int EX_OFF = 4 * KR_CH;

DI void conv_role(const u16* __restrict__ x, const u16* __restrict__ lar, const u16* __restrict__ aim, const u16* __restrict__ cre,
                  const u16* __restrict__ cim, const u16* __restrict__ ldt, const u16* __restrict__ dsk, const u16* __restrict__ gw,
                  char* __restrict__ ws, int cg, char* smem) {
  const int tid = threadIdx.x, lane = tid & 63;
  const int w = __builtin_amdgcn_readfirstlane(tid >> 6);
  const int ch = w >> 1, half = w & 1, g = lane >> 4, l15 = lane & 15;
  const int c = 4 * cg + ch;
  const u32 lar2 = *(const u32*)(lar + c * 128 + 2 * lane), aim2 = *(const u32*)(aim + c * 128 + 2 * lane);
  const u32 cre2 = *(const u32*)(cre + c * 128 + 2 * lane), cim2 = *(const u32*)(cim + c * 128 + 2 * lane);
  const float ldt_f = bf_lo(ldt[c]), dsk_f = bf_lo(dsk[c]);
  u32x2 xr[4];
#pragma unroll
  for (int r = 0; r < 4; ++r) xr[r] = *(const u32x2*)(x + (size_t)(4 * tid + r) * DM + 4 * cg);
  const u32x4 gx0 = *(const u32x4*)(x + (size_t)(16 * cg + 2 * w) * DM + 8 * lane);
  const u32x4 gx1 = *(const u32x4*)(x + (size_t)(16 * cg + 2 * w + 1) * DM + 8 * lane);
  const u32x4 gwv = *(const u32x4*)(gw + 8 * lane);
  const float dt = rbf(expf(ldt_f));
  float al[2], th[2];
  al[0] = -rbf(expf(bf_lo(lar2))) * dt * 1.44269504088896341f; al[1] = -rbf(expf(bf_hi(lar2))) * dt * 1.44269504088896341f;
  th[0] = bf_lo(aim2) * dt * 0.15915494309189535f; th[1] = bf_hi(aim2) * dt * 0.15915494309189535f;
  float cm = fmaxf(fmaxf(fabsf(bf_lo(cre2)), fabsf(bf_hi(cre2))), fmaxf(fabsf(bf_lo(cim2)), fabsf(bf_hi(cim2))));
#pragma unroll
  for (int o = 32; o >= 1; o >>= 1) cm = fmaxf(cm, __shfl_xor(cm, o));
  int ce = (int)((__builtin_bit_cast(u32, cm) >> 23) & 0xff);
  ce = ce < 1 ? 1 : (ce > 253 ? 253 : ce);
  const float cinv = __builtin_bit_cast(float, (u32)(254 - ce) << 23);
  const float kscale = 2.f * dt * __builtin_bit_cast(float, (u32)ce << 23);
  char* tb = smem + ch * TB_CH + lane * 8;
#define PUT(e, v0, v1) *(u32x2*)(tb + (e) * TB_ROW) = (u32x2){pk_f16(v0), pk_f16(v1)}
#define CONJ(v) ((f32x2){(v).x, -(v).y})
  if (half == 0) {
    f32x2 w16[2], xc[2], w1[2], yc[2];
#pragma unroll
    for (int m = 0; m < 2; ++m) { w16[m] = cexp_p(al[m], th[m], 16.f); w1[m] = CONJ(cexp_p(al[m], th[m], 1.f)); }
    xc[0] = (f32x2){bf_lo(cre2) * cinv, bf_lo(cim2) * cinv}; xc[1] = (f32x2){bf_hi(cre2) * cinv, bf_hi(cim2) * cinv};
    PUT(0, xc[0], xc[1]);
    PUT(16, ((f32x2){1.f, 0.f}), ((f32x2){1.f, 0.f}));
    PUT(17, w1[0], w1[1]);
    yc[0] = w1[0]; yc[1] = w1[1];
#pragma unroll
    for (int i = 1; i < 16; ++i) {
      xc[0] = cmul(xc[0], w16[0]); xc[1] = cmul(xc[1], w16[1]);
      PUT(i, xc[0], xc[1]);
      if (i >= 2) { yc[0] = cmul(yc[0], w1[0]); yc[1] = cmul(yc[1], w1[1]); PUT(16 + i, yc[0], yc[1]); }
    }
  } else {
    f32x2 w2[2], w4[2], w8[2], wB[2], a[2], t[2];
#pragma unroll
    for (int m = 0; m < 2; ++m) {
      w2[m] = CONJ(cexp_p(al[m], th[m], 2.f)); wB[m] = CONJ(cexp_p(al[m], th[m], 256.f));
      w4[m] = cmul(w2[m], w2[m]); w8[m] = cmul(w4[m], w4[m]);
    }
    a[0] = wB[0]; a[1] = wB[1]; t[0] = a[0]; t[1] = a[1];
    PUT(32, t[0], t[1]);
#pragma unroll
    for (int m = 1; m < 9; ++m) { t[0] = cmul(t[0], w2[0]); t[1] = cmul(t[1], w2[1]); PUT(32 + m, t[0], t[1]); }
#pragma unroll
    for (int s = 2; s < 4; ++s) {
      a[0] = cmul(a[0], wB[0]); a[1] = cmul(a[1], wB[1]); t[0] = a[0]; t[1] = a[1];
      PUT(41 + 5 * (s - 2), t[0], t[1]);
#pragma unroll
      for (int m = 1; m < 5; ++m) { t[0] = cmul(t[0], w4[0]); t[1] = cmul(t[1], w4[1]); PUT(41 + 5 * (s - 2) + m, t[0], t[1]); }
    }
#pragma unroll
    for (int s = 4; s < 8; ++s) {
      a[0] = cmul(a[0], wB[0]); a[1] = cmul(a[1], wB[1]); t[0] = a[0]; t[1] = a[1];
      PUT(51 + 3 * (s - 4), t[0], t[1]);
#pragma unroll
      for (int m = 1; m < 3; ++m) { t[0] = cmul(t[0], w8[0]); t[1] = cmul(t[1], w8[1]); PUT(51 + 3 * (s - 4) + m, t[0], t[1]); }
    }
  }
#undef PUT
  {
    char* ub = smem + U_OFF + 480 + tid * 8;
    *(u32x2*)(ub + 0 * U_CH) = (u32x2){__builtin_amdgcn_perm(xr[1].x, xr[0].x, 0x05040100u), __builtin_amdgcn_perm(xr[3].x, xr[2].x, 0x05040100u)};
    *(u32x2*)(ub + 1 * U_CH) = (u32x2){__builtin_amdgcn_perm(xr[1].x, xr[0].x, 0x07060302u), __builtin_amdgcn_perm(xr[3].x, xr[2].x, 0x07060302u)};
    *(u32x2*)(ub + 2 * U_CH) = (u32x2){__builtin_amdgcn_perm(xr[1].y, xr[0].y, 0x05040100u), __builtin_amdgcn_perm(xr[3].y, xr[2].y, 0x05040100u)};
    *(u32x2*)(ub + 3 * U_CH) = (u32x2){__builtin_amdgcn_perm(xr[1].y, xr[0].y, 0x07060302u), __builtin_amdgcn_perm(xr[3].y, xr[2].y, 0x07060302u)};
    if (tid < 240) *(u32x2*)(smem + U_OFF + (tid / 60) * U_CH + (tid % 60) * 8) = (u32x2){0u, 0u};
  }
  {
    float s0 = 0.f, s1 = 0.f;
#pragma unroll
    for (int i = 0; i < 4; ++i) {
      const float wl = bf_lo(gwv[i]), wh_ = bf_hi(gwv[i]);
      const u32 p0 = pk_bf16(bf_lo(gx0[i]) * wl, bf_hi(gx0[i]) * wh_), p1 = pk_bf16(bf_lo(gx1[i]) * wl, bf_hi(gx1[i]) * wh_);
      s0 += bf_lo(p0) + bf_hi(p0); s1 += bf_lo(p1) + bf_hi(p1);
    }
#pragma unroll
    for (int o = 32; o >= 1; o >>= 1) { s0 += __shfl_xor(s0, o); s1 += __shfl_xor(s1, o); }
    if (lane < 2) {
      const float s = rbf(lane ? s1 : s0);
      ((float*)(ws + WS_G))[16 * cg + 2 * w + lane] = rbf(1.f / (1.f + expf(-s)));
    }
  }
  __syncthreads();
  f32x4 kd[2] = {{0, 0, 0, 0}, {0, 0, 0, 0}};
  {
    int e0, e1;
    if (half == 0) {
      e0 = 16 + l15;
      e1 = 32 + ((0x8766654443222100ull >> (4 * l15)) & 15);
    } else {
      const int gq = l15 >> 2, r = min(l15 & 3, 2);
      e0 = 41 + 5 * (gq >> 1) + 2 * (gq & 1) + r;
      e1 = 51 + 3 * gq + r;
    }
    const char* tch = smem + ch * TB_CH + 16 * g;
    const char* pa0 = tch + e0 * TB_ROW; const char* pa1 = tch + e1 * TB_ROW; const char* pb = tch + l15 * TB_ROW;
#pragma unroll
    for (int ks = 0; ks < 8; ++ks) {
      const h16x8 b = *(const h16x8*)(pb + 64 * ks);
      kd[0] = MFMA16H(*(const h16x8*)(pa0 + 64 * ks), b, kd[0]);
      kd[1] = MFMA16H(*(const h16x8*)(pa1 + 64 * ks), b, kd[1]);
    }
  }
  __syncthreads();
  {
    char* kr = smem + ch * KR_CH;
    const f32x4 k0 = kd[0] * kscale, k1 = kd[1] * kscale;
    if (half == 0) {
      const int y = 2044 - 16 * l15 - 4 * g;
      *(u32x2*)(kr + 2 * y) = (u32x2){pk_bf16(k0[3], k0[2]), pk_bf16(k0[1], k0[0])};
      *(u32x2*)(kr + 2 * (y - 256)) = (u32x2){pk_bf16(k1[3], k1[2]), pk_bf16(k1[1], k1[0])};
      if (lane < 4) *(u32x4*)(kr + 4096 + 16 * lane) = (u32x4){0u, 0u, 0u, 0u};
    } else {
      const int y2 = 2040 - (512 + 256 * (g >> 1) + 16 * l15 + 8 * (g & 1));
      *(u32x4*)(kr + 2 * y2) = (u32x4){pk_bf16(k0[2], k0[2]), pk_bf16(k0[1], k0[1]), pk_bf16(k0[1], k0[0]), pk_bf16(k0[0], k0[0])};
      const int y3 = 2032 - (1024 + 256 * g + 16 * l15);
      const u32 a2 = pk_bf16(k1[2], k1[2]), a1 = pk_bf16(k1[1], k1[1]), a0 = pk_bf16(k1[0], k1[0]);
      *(u32x4*)(kr + 2 * y3) = (u32x4){a2, a2, a1, a1};
      *(u32x4*)(kr + 2 * y3 + 16) = (u32x4){a1, pk_bf16(k1[1], k1[0]), a0, a0};
    }
  }
  __syncthreads();
  f32x4 acc[8];
#pragma unroll
  for (int i = 0; i < 8; ++i) acc[i] = (f32x4){0, 0, 0, 0};
  {
    const int yb = 2031 - l15 + 8 * g - 128 * half;
    const u32 sh = (yb & 1) * 16;
    const char* ka = smem + ch * KR_CH + (yb >> 1) * 4;
    const char* ua = smem + U_OFF + ch * U_CH + (8 * g + 16 * l15 + 224 - 128 * half) * 2;
#pragma unroll
    for (int rr = 0; rr < 4; ++rr) {
      s16x8 A[8], B[8];
#pragma unroll
      for (int n = 0; n < 8; ++n) {
        const u32* kp = (const u32*)(ka - 64 * rr - 512 * n);
        const u32 d0 = kp[0], d1 = kp[1], d2 = kp[2], d3 = kp[3], d4 = kp[4];
        A[n] = __builtin_bit_cast(s16x8, (u32x4){__builtin_amdgcn_alignbit(d1, d0, sh), __builtin_amdgcn_alignbit(d2, d1, sh),
                                                 __builtin_amdgcn_alignbit(d3, d2, sh), __builtin_amdgcn_alignbit(d4, d3, sh)});
        B[n] = *(const s16x8*)(ua - 64 * rr + 512 * n);
      }
#pragma unroll
      for (int nu = 0; nu < 8; ++nu)
#pragma unroll
        for (int mu = 0; mu < 8 - nu; ++mu) acc[nu + mu] = MFMA16(A[nu], B[mu], acc[nu + mu]);
    }
  }
  {
    char* ex = smem + EX_OFF + (ch * 2 + half) * 4096 + lane * 16;
    f32x4 keep[4];
    if (half) {
#pragma unroll
      for (int i = 0; i < 4; ++i) { *(f32x4*)(ex + 1024 * i) = acc[i]; keep[i] = acc[4 + i]; }
    } else {
#pragma unroll
      for (int i = 0; i < 4; ++i) { *(f32x4*)(ex + 1024 * i) = acc[4 + i]; keep[i] = acc[i]; }
    }
    __syncthreads();
    const char* ei = smem + EX_OFF + (ch * 2 + (half ^ 1)) * 4096 + lane * 16;
    const int t0 = 1024 * half + 16 * l15 + 4 * g;
    u16* bt = (u16*)(ws + WS_BT) + (size_t)c * SEQ + t0;
    const char* up = smem + U_OFF + ch * U_CH + 480 + 2 * t0;
#pragma unroll
    for (int i = 0; i < 4; ++i) {
      const f32x4 y = keep[i] + *(const f32x4*)(ei + 1024 * i);
      const u32x2 uu = *(const u32x2*)(up + 512 * i);
      const u32 y01 = pk_bf16(y[0], y[1]), y23 = pk_bf16(y[2], y[3]);
      const u32 s01 = pk_bf16(bf_lo(uu.x) * dsk_f, bf_hi(uu.x) * dsk_f), s23 = pk_bf16(bf_lo(uu.y) * dsk_f, bf_hi(uu.y) * dsk_f);
      *(u32x2*)(bt + 256 * i) = (u32x2){pk_bf16(bf_lo(y01) + bf_lo(s01), bf_hi(y01) + bf_hi(s01)),
                                        pk_bf16(bf_lo(y23) + bf_lo(s23), bf_hi(y23) + bf_hi(s23))};
    }
  }
}

__global__ __launch_bounds__(512, 2) void k1(const u16* __restrict__ x, const u16* __restrict__ wqkv, const u16* __restrict__ lar,
                                             const u16* __restrict__ aim, const u16* __restrict__ cre, const u16* __restrict__ cim,
                                             const u16* __restrict__ ldt, const u16* __restrict__ dsk, const u16* __restrict__ gw,
                                             char* __restrict__ ws) {
  extern __shared__ __attribute__((aligned(16))) char smem[];
  const int b = blockIdx.x, xcd = b & 7, slot = b >> 3, idx = slot >> 1;
  if (slot & 1) conv_role(x, lar, aim, cre, cim, ldt, dsk, gw, ws, xcd * 16 + idx, smem);
  else gemm_role(x, wqkv, ws, 4 * (xcd >> 1) + (idx & 3), 4 * (xcd & 1) + (idx >> 2), smem);
}

static constexpr int K2_WO = 0, K2_BT = 65536, K2_KV = 65536 + 8192, K2_EX = K2_KV + 17408, K2_LDS = K2_EX + 16384;

__global__ __launch_bounds__(256, 1) void k2(const u16* __restrict__ wo, const char* __restrict__ ws, u16* __restrict__ out) {
  extern __shared__ __attribute__((aligned(16))) char smem[];
  const int tid = threadIdx.x, lane = tid & 63;
  const int w = __builtin_amdgcn_readfirstlane(tid >> 6);
  const int b = blockIdx.x, xcd = b & 7, idx = b >> 3;
  const int rt = 8 * (xcd >> 1) + (idx & 7), ct = 4 * (xcd & 1) + (idx >> 3);
  const int th = w & 1, kh = w >> 1, hh = lane >> 5, l31 = lane & 31;
  f32x4 kvr[9];
  const f32x4* kvs = (const f32x4*)(ws + WS_KV);
#pragma unroll
  for (int i = 0; i < 8; ++i) kvr[i] = kvs[tid + 256 * i];
  kvr[8] = kvs[2048 + (tid & 127)];
  s16x8 pf[16];
  const s16x8* pqs = (const s16x8*)(ws + WS_PQ + (size_t)((2 * rt + th) * 32 + 16 * kh) * 1024) + lane;
#pragma unroll
  for (int i = 0; i < 16; ++i) pf[i] = pqs[64 * i];
  const float gt = ((const float*)(ws + WS_G))[64 * rt + 32 * th + l31];
#pragma unroll
  for (int i = 0; i < 8; ++i) *(u32x2*)(smem + K2_KV + (tid + 256 * i) * 8) = (u32x2){pk_bf16(kvr[i][0], kvr[i][1]), pk_bf16(kvr[i][2], kvr[i][3])};
  if (tid < 128) *(u32x2*)(smem + K2_KV + (2048 + tid) * 8) = (u32x2){pk_bf16(kvr[8][0], kvr[8][1]), pk_bf16(kvr[8][2], kvr[8][3])};
  {
    const int sr = lane >> 3, sp = lane & 7;
    const u16* src = wo + (size_t)(128 * w + sr) * DM + 64 * ct + ((sp ^ (((sr >> 1) & 1) << 2)) << 3);
#pragma unroll
    for (int i = 0; i < 16; ++i) glds16(src + (size_t)i * 8 * DM, smem + K2_WO + (128 * w + 8 * i) * 128);
    const u16* bs = (const u16*)(ws + WS_BT) + (size_t)(64 * ct + 16 * w + sr) * SEQ + 64 * rt + ((sp ^ (((sr >> 1) & 1) << 2)) << 3);
    glds16(bs, smem + K2_BT + 16 * w * 128);
    glds16(bs + 8 * SEQ, smem + K2_BT + (16 * w + 8) * 128);
  }
  WAIT_VM(0);
  __syncthreads();
  f32x16 acc[2];
#pragma unroll
  for (int i = 0; i < 16; ++i) { acc[0][i] = 0.f; acc[1][i] = 0.f; }
  const f32x16 zero16 = acc[0];
  const char* kva = smem + K2_KV + 16 * kh * 544 + min(l31, 16) * 32 + hh * 16;
  const int blk = (lane >> 4) & 1, q4 = (lane & 15) >> 2, p4 = lane & 3;
  const int sw = ((q4 >> 1) & 1) << 6;
  const char* wa = smem + K2_WO + (256 * kh + 4 * hh + q4) * 128 + blk * 32 + p4 * 8;
#pragma unroll
  for (int i = 0; i < 16; ++i) {
    const f32x16 d = MFMA32(*(const s16x8*)(kva + i * 544), pf[i], zero16);
    const float inv = __builtin_amdgcn_rcpf(d[8] + 1e-6f);
    const u32x4 of = {pk_bf16(d[0] * inv, d[1] * inv), pk_bf16(d[2] * inv, d[3] * inv), pk_bf16(d[4] * inv, d[5] * inv), pk_bf16(d[6] * inv, d[7] * inv)};
#pragma unroll
    for (int c2 = 0; c2 < 2; ++c2) {
      const char* p = wa + i * 2048 + ((c2 * 64) ^ sw);
      acc[c2] = MFMA32(cat8(tr_read(p), tr_read(p + 1024)), __builtin_bit_cast(s16x8, of), acc[c2]);
    }
  }
  char* ex = smem + K2_EX + w * 4096 + lane * 16;
#pragma unroll
  for (int i = 0; i < 4; ++i) {
    const f32x16& s = kh ? acc[0] : acc[1];
    *(f32x4*)(ex + 1024 * i) = (f32x4){s[4 * i], s[4 * i + 1], s[4 * i + 2], s[4 * i + 3]};
  }
  __syncthreads();
  const char* ei = smem + K2_EX + (w ^ 2) * 4096 + lane * 16;
  const float g1 = rbf(1.f - gt);
  const char* ba = smem + K2_BT + (32 * kh + 4 * hh + q4) * 128 + (((th * 64) ^ sw) + blk * 32 + p4 * 8);
  u16* op = out + (size_t)(64 * rt + 32 * th + l31) * DM + 64 * ct + 32 * kh + 4 * hh;
#pragma unroll
  for (int i = 0; i < 4; ++i) {
    const f32x16& s = kh ? acc[1] : acc[0];
    const f32x4 o = *(const f32x4*)(ei + 1024 * i);
    const u32x2 bb = __builtin_bit_cast(u32x2, tr_read(ba + i * 1024));
    const float r0 = gt * (s[4 * i] + o[0]) + g1 * bf_lo(bb.x), r1 = gt * (s[4 * i + 1] + o[1]) + g1 * bf_hi(bb.x);
    const float r2 = gt * (s[4 * i + 2] + o[2]) + g1 * bf_lo(bb.y), r3 = gt * (s[4 * i + 3] + o[3]) + g1 * bf_hi(bb.y);
    *(u32x2*)(op + 8 * i) = (u32x2){pk_bf16(r0, r1), pk_bf16(r2, r3)};
  }
}

extern "C" void kernel_launch(void* const* d_in, const int* in_sizes, int n_in, void* d_out, int out_size, void* d_ws,
                              size_t ws_size, hipStream_t stream) {
  (void)in_sizes; (void)n_in; (void)out_size; (void)ws_size;
  (void)hipFuncSetAttribute((const void*)k1, hipFuncAttributeMaxDynamicSharedMemorySize, 163840);
  (void)hipFuncSetAttribute((const void*)k2, hipFuncAttributeMaxDynamicSharedMemorySize, K2_LDS);
  (void)hipMemsetAsync((char*)d_ws + WS_KV, 0, KV_BYTES, stream);
  k1<<<256, 512, 163840, stream>>>((const u16*)d_in[0], (const u16*)d_in[1], (const u16*)d_in[4], (const u16*)d_in[5],
                                   (const u16*)d_in[6], (const u16*)d_in[7], (const u16*)d_in[8], (const u16*)d_in[9],
                                   (const u16*)d_in[10], (char*)d_ws);
  k2<<<256, 256, K2_LDS, stream>>>((const u16*)d_in[2], (const char*)d_ws, (u16*)d_out);
}
```

```cpp
#include <hip/hip_runtime.h>
#include <hip/hip_bf16.h>
#include <stdint.h>

typedef unsigned int u32;
typedef unsigned short u16;
typedef __attribute__((ext_vector_type(8))) short s16x8;
typedef __attribute__((ext_vector_type(4))) short s16x4;
typedef __attribute__((ext_vector_type(8))) _Float16 h16x8;
typedef __attribute__((ext_vector_type(2))) _Float16 h16x2;
typedef __attribute__((ext_vector_type(2))) __bf16 b16x2;
typedef __attribute__((ext_vector_type(2))) float f32x2;
typedef __attribute__((ext_vector_type(4))) float f32x4;
typedef __attribute__((ext_vector_type(16))) float f32x16;
typedef __attribute__((ext_vector_type(2))) u32 u32x2;
typedef __attribute__((ext_vector_type(4))) u32 u32x4;
#define LDS_AS __attribute__((address_space(3)))
#define DI __device__ __forceinline__

static constexpr int SEQ = 2048, DM = 512;
static constexpr size_t WS_KV = 0;
static constexpr size_t WS_G = 65536;
static constexpr size_t WS_PQ = 1u << 20;
static constexpr size_t WS_BT = 4u << 20;
static constexpr int KV_BYTES = 32 * 272 * 4;

#define STAMP_DECL do {} while (0)
#define STAMP(i) do {} while (0)
#define STAMP_FLUSH(kern, wsp) do {} while (0)
#define WAIT_VM(n) asm volatile("s_waitcnt vmcnt(" #n ")" ::: "memory")
#define WAIT_LGKM0() asm volatile("s_waitcnt lgkmcnt(0)" ::: "memory")
#define RAW_BAR() __builtin_amdgcn_s_barrier()

DI u32 pk_bf16(float a, float b) { return __builtin_bit_cast(u32, __builtin_convertvector((f32x2){a, b}, b16x2)); }
DI u32 pk_f16(f32x2 v) { return __builtin_bit_cast(u32, __builtin_convertvector(v, h16x2)); }
DI float bf_lo(u32 v) { return __builtin_bit_cast(float, v << 16); }
DI float bf_hi(u32 v) { return __builtin_bit_cast(float, v & 0xffff0000u); }
DI float rbf(float v) { return bf_lo(pk_bf16(v, v)); }
DI float phi_f(float t) {
  float e = __builtin_amdgcn_exp2f(fminf(t, 0.f) * 1.44269504088896341f);
  return (fmaxf(t, 0.f) + 1e-6f) + e;
}
DI s16x4 tr_read(const char* p) { return __builtin_amdgcn_ds_read_tr16_b64_v4i16((LDS_AS s16x4*)p); }
DI s16x8 cat8(s16x4 a, s16x4 b) { return __builtin_shufflevector(a, b, 0, 1, 2, 3, 4, 5, 6, 7); }
DI void glds16(const void* g, char* l) {
  __builtin_amdgcn_global_load_lds((const __attribute__((address_space(1))) void*)g, (LDS_AS void*)l, 16, 0, 0);
}
DI f32x2 cmul(f32x2 a, f32x2 w) {
  f32x2 wr = {-w.y, w.x};
  return (f32x2){a.x, a.x} * w + (f32x2){a.y, a.y} * wr;
}
DI f32x2 cexp_p(float alpha, float theta, float p) {
  float m = __builtin_amdgcn_exp2f(alpha * p);
  float f = __builtin_amdgcn_fractf(theta * p);
  return (f32x2){m * __builtin_amdgcn_cosf(f), m * __builtin_amdgcn_sinf(f)};
}
#define MFMA16(a, b, c) __builtin_amdgcn_mfma_f32_16x16x32_bf16((a), (b), (c), 0, 0, 0)
#define MFMA16H(a, b, c) __builtin_amdgcn_mfma_f32_16x16x32_f16((a), (b), (c), 0, 0, 0)
#define MFMA32(a, b, c) __builtin_amdgcn_mfma_f32_32x32x16_bf16((a), (b), (c), 0, 0, 0)

static constexpr int STAGE = 40960;

DI void gemm_role(const u16* __restrict__ x, const u16* __restrict__ wqkv, char* __restrict__ ws, int tt, int hg, char* smem) {
  const int tid = threadIdx.x, lane = tid & 63;
  const int w = __builtin_amdgcn_readfirstlane(tid >> 6);
  const int wm = w >> 2, wh = w & 3, g = lane >> 4, l15 = lane & 15;
  STAMP_DECL; STAMP(0);
  const int sr = lane >> 3, sp = lane & 7;
  const u16* xs = x + (size_t)(tt * 128 + 16 * w + sr) * DM + ((sp ^ sr) << 3);
  const int fw = ((sr >> 1) & 1) | ((w & 1) << 1);
  const u16* wsrc = wqkv + (size_t)(8 * w + sr) * 1536 + hg * 64 + ((sp ^ (fw << 1)) << 3);
#define ISSUE(s) do { char* sb = smem + ((s) & 3) * STAGE; \
    glds16(xs + (s) * 64, sb + w * 2048); glds16(xs + (s) * 64 + 8 * DM, sb + w * 2048 + 1024); \
    glds16(wsrc + (size_t)(s) * 64 * 1536, sb + 16384 + w * 1024); \
    glds16(wsrc + (size_t)(s) * 64 * 1536 + 512, sb + 16384 + 8192 + w * 1024); \
    glds16(wsrc + (size_t)(s) * 64 * 1536 + 1024, sb + 16384 + 16384 + w * 1024); } while (0)
  ISSUE(0); ISSUE(1); ISSUE(2); ISSUE(3);
  const int xo = (64 * wm + l15) * 128 + ((g ^ (lane & 7)) << 4);
  const int q4 = l15 >> 2, p4 = lane & 3;
  const int wo_ = 16384 + (8 * g + q4) * 128 + ((wh ^ (((q4 >> 1) & 1) | ((g & 1) << 1))) << 5) + 8 * p4;
  f32x4 aq[4], ak[4], av[4];
#pragma unroll
  for (int i = 0; i < 4; ++i) { aq[i] = (f32x4){0, 0, 0, 0}; ak[i] = aq[i]; av[i] = aq[i]; }
#define COMPUTE(s) do { const char* sb = smem + ((s) & 3) * STAGE; \
    _Pragma("unroll") for (int ks = 0; ks < 2; ++ks) { \
      s16x8 xf[4], wf[3]; \
      _Pragma("unroll") for (int st = 0; st < 3; ++st) \
        wf[st] = cat8(tr_read(sb + wo_ + st * 8192 + ks * 4096), tr_read(sb + wo_ + st * 8192 + ks * 4096 + 512)); \
      _Pragma("unroll") for (int mt = 0; mt < 4; ++mt) xf[mt] = *(const s16x8*)(sb + ((xo ^ (ks * 64)) + mt * 2048)); \
      _Pragma("unroll") for (int mt = 0; mt < 4; ++mt) { \
        aq[mt] = MFMA16(wf[0], xf[mt], aq[mt]); ak[mt] = MFMA16(xf[mt], wf[1], ak[mt]); av[mt] = MFMA16(xf[mt], wf[2], av[mt]); } \
    } } while (0)
  STAMP(1);
  WAIT_VM(15); RAW_BAR(); STAMP(2); COMPUTE(0);
  WAIT_VM(10); WAIT_LGKM0(); RAW_BAR(); STAMP(3); ISSUE(4); COMPUTE(1);
  WAIT_VM(10); WAIT_LGKM0(); RAW_BAR(); ISSUE(5); COMPUTE(2);
  WAIT_VM(10); WAIT_LGKM0(); RAW_BAR(); ISSUE(6); COMPUTE(3);
  WAIT_VM(10); WAIT_LGKM0(); RAW_BAR(); STAMP(4); ISSUE(7); COMPUTE(4);
  WAIT_VM(10); WAIT_LGKM0(); RAW_BAR(); COMPUTE(5);
  WAIT_VM(5); WAIT_LGKM0(); RAW_BAR(); COMPUTE(6);
  WAIT_VM(0); WAIT_LGKM0(); RAW_BAR(); STAMP(5); COMPUTE(7);
  STAMP(6);
#undef ISSUE
#undef COMPUTE
  const int h = 4 * hg + wh;
  f32x4 kvt = {0, 0, 0, 0}, zz = {0, 0, 0, 0};
  const u32x4 ones_u = {0x3f803f80u, 0x3f803f80u, 0x3f803f80u, 0x3f803f80u};
#pragma unroll
  for (int pr = 0; pr < 2; ++pr) {
    u32x4 pk, vv;
#pragma unroll
    for (int m = 0; m < 2; ++m) {
      const f32x4 kk = ak[2 * pr + m], v4 = av[2 * pr + m];
      pk[2 * m] = pk_bf16(phi_f(kk[0]), phi_f(kk[1])); pk[2 * m + 1] = pk_bf16(phi_f(kk[2]), phi_f(kk[3]));
      vv[2 * m] = pk_bf16(v4[0], v4[1]); vv[2 * m + 1] = pk_bf16(v4[2], v4[3]);
    }
    kvt = MFMA16(__builtin_bit_cast(s16x8, vv), __builtin_bit_cast(s16x8, pk), kvt);
    zz = MFMA16(__builtin_bit_cast(s16x8, ones_u), __builtin_bit_cast(s16x8, pk), zz);
  }
  float* red = (float*)smem + (wm * 4 + wh) * 272;
#pragma unroll
  for (int r = 0; r < 4; ++r) red[(4 * g + r) * 16 + l15] = kvt[r];
  if (g == 0) red[256 + l15] = zz[0];
  char* pqb = ws + WS_PQ + ((size_t)((tt * 4 + 2 * wm) * 32 + h) * 2 + (g >> 1)) * 512 + l15 * 16 + (g & 1) * 8;
#pragma unroll
  for (int mt = 0; mt < 4; ++mt) {
    const f32x4 qq = aq[mt];
    u32x2 o = {pk_bf16(phi_f(qq[0]), phi_f(qq[1])), pk_bf16(phi_f(qq[2]), phi_f(qq[3]))};
    *(u32x2*)(pqb + (mt >> 1) * 32768 + (mt & 1) * 256) = o;
  }
  STAMP(7);
  __syncthreads();
  STAMP(8);
  float* kvacc = (float*)(ws + WS_KV) + hg * 1088;
  const float* rs = (const float*)smem;
  atomicAdd(kvacc + tid, rs[tid] + rs[1088 + tid]);
  atomicAdd(kvacc + 512 + tid, rs[512 + tid] + rs[1088 + 512 + tid]);
  if (tid < 64) atomicAdd(kvacc + 1024 + tid, rs[1024 + tid] + rs[1088 + 1024 + tid]);
  STAMP(9); WAIT_VM(0);
  STAMP_FLUSH(0, ws);
}

static constexpr int TB_ROW = 544, TB_CH = 63 * TB_ROW;
static constexpr int U_OFF = 4 * TB_CH, U_CH = (2048 + 240) * 2;
static constexpr int KR_CH = 4160;
static constexpr int EX_OFF = 4 * KR_CH;

DI void conv_role(const u16* __restrict__ x, const u16* __restrict__ lar, const u16* __restrict__ aim, const u16* __restrict__ cre,
                  const u16* __restrict__ cim, const u16* __restrict__ ldt, const u16* __restrict__ dsk, const u16* __restrict__ gw,
                  char* __restrict__ ws, int cg, char* smem) {
  const int tid = threadIdx.x, lane = tid & 63;
  const int w = __builtin_amdgcn_readfirstlane(tid >> 6);
  const int ch = w >> 1, half = w & 1, g = lane >> 4, l15 = lane & 15;
  const int c = 4 * cg + ch;
  STAMP_DECL; STAMP(0);
  const u32 lar2 = *(const u32*)(lar + c * 128 + 2 * lane), aim2 = *(const u32*)(aim + c * 128 + 2 * lane);
  const u32 cre2 = *(const u32*)(cre + c * 128 + 2 * lane), cim2 = *(const u32*)(cim + c * 128 + 2 * lane);
  const float ldt_f = bf_lo(ldt[c]), dsk_f = bf_lo(dsk[c]);
  u32x2 xr[4];
#pragma unroll
  for (int r = 0; r < 4; ++r) xr[r] = *(const u32x2*)(x + (size_t)(4 * tid + r) * DM + 4 * cg);
  const u32x4 gx0 = *(const u32x4*)(x + (size_t)(16 * cg + 2 * w) * DM + 8 * lane);
  const u32x4 gx1 = *(const u32x4*)(x + (size_t)(16 * cg + 2 * w + 1) * DM + 8 * lane);
  const u32x4 gwv = *(const u32x4*)(gw + 8 * lane);
  const float dt = rbf(expf(ldt_f));
  float al[2], th[2];
  al[0] = -rbf(expf(bf_lo(lar2))) * dt * 1.44269504088896341f; al[1] = -rbf(expf(bf_hi(lar2))) * dt * 1.44269504088896341f;
  th[0] = bf_lo(aim2) * dt * 0.15915494309189535f; th[1] = bf_hi(aim2) * dt * 0.15915494309189535f;
  STAMP(1);
  float cm = fmaxf(fmaxf(fabsf(bf_lo(cre2)), fabsf(bf_hi(cre2))), fmaxf(fabsf(bf_lo(cim2)), fabsf(bf_hi(cim2))));
#pragma unroll
  for (int o = 32; o >= 1; o >>= 1) cm = fmaxf(cm, __shfl_xor(cm, o));
  int ce = (int)((__builtin_bit_cast(u32, cm) >> 23) & 0xff);
  ce = ce < 1 ? 1 : (ce > 253 ? 253 : ce);
  const float cinv = __builtin_bit_cast(float, (u32)(254 - ce) << 23);
  const float kscale = 2.f * dt * __builtin_bit_cast(float, (u32)ce << 23);
  char* tb = smem + ch * TB_CH + lane * 8;
#define PUT(e, v0, v1) *(u32x2*)(tb + (e) * TB_ROW) = (u32x2){pk_f16(v0), pk_f16(v1)}
#define CONJ(v) ((f32x2){(v).x, -(v).y})
  if (half == 0) {
    f32x2 w16[2], xc[2], w1[2], yc[2];
#pragma unroll
    for (int m = 0; m < 2; ++m) { w16[m] = cexp_p(al[m], th[m], 16.f); w1[m] = CONJ(cexp_p(al[m], th[m], 1.f)); }
    xc[0] = (f32x2){bf_lo(cre2) * cinv, bf_lo(cim2) * cinv}; xc[1] = (f32x2){bf_hi(cre2) * cinv, bf_hi(cim2) * cinv};
    PUT(0, xc[0], xc[1]);
    PUT(16, ((f32x2){1.f, 0.f}), ((f32x2){1.f, 0.f}));
    PUT(17, w1[0], w1[1]);
    yc[0] = w1[0]; yc[1] = w1[1];
#pragma unroll
    for (int i = 1; i < 16; ++i) {
      xc[0] = cmul(xc[0], w16[0]); xc[1] = cmul(xc[1], w16[1]);
      PUT(i, xc[0], xc[1]);
      if (i >= 2) { yc[0] = cmul(yc[0], w1[0]); yc[1] = cmul(yc[1], w1[1]); PUT(16 + i, yc[0], yc[1]); }
    }
  } else {
    f32x2 w2[2], w4[2], w8[2], wB[2], a[2], t[2];
#pragma unroll
    for (int m = 0; m < 2; ++m) {
      w2[m] = CONJ(cexp_p(al[m], th[m], 2.f)); wB[m] = CONJ(cexp_p(al[m], th[m], 256.f));
      w4[m] = cmul(w2[m], w2[m]); w8[m] = cmul(w4[m], w4[m]);
    }
    a[0] = wB[0]; a[1] = wB[1]; t[0] = a[0]; t[1] = a[1];
    PUT(32, t[0], t[1]);
#pragma unroll
    for (int m = 1; m < 9; ++m) { t[0] = cmul(t[0], w2[0]); t[1] = cmul(t[1], w2[1]); PUT(32 + m, t[0], t[1]); }
#pragma unroll
    for (int s = 2; s < 4; ++s) {
      a[0] = cmul(a[0], wB[0]); a[1] = cmul(a[1], wB[1]); t[0] = a[0]; t[1] = a[1];
      PUT(41 + 5 * (s - 2), t[0], t[1]);
#pragma unroll
      for (int m = 1; m < 5; ++m) { t[0] = cmul(t[0], w4[0]); t[1] = cmul(t[1], w4[1]); PUT(41 + 5 * (s - 2) + m, t[0], t[1]); }
    }
#pragma unroll
    for (int s = 4; s < 8; ++s) {
      a[0] = cmul(a[0], wB[0]); a[1] = cmul(a[1], wB[1]); t[0] = a[0]; t[1] = a[1];
      PUT(51 + 3 * (s - 4), t[0], t[1]);
#pragma unroll
      for (int m = 1; m < 3; ++m) { t[0] = cmul(t[0], w8[0]); t[1] = cmul(t[1], w8[1]); PUT(51 + 3 * (s - 4) + m, t[0], t[1]); }
    }
  }
#undef PUT
  STAMP(2);
  {
    char* ub = smem + U_OFF + 480 + tid * 8;
    *(u32x2*)(ub + 0 * U_CH) = (u32x2){__builtin_amdgcn_perm(xr[1].x, xr[0].x, 0x05040100u), __builtin_amdgcn_perm(xr[3].x, xr[2].x, 0x05040100u)};
    *(u32x2*)(ub + 1 * U_CH) = (u32x2){__builtin_amdgcn_perm(xr[1].x, xr[0].x, 0x07060302u), __builtin_amdgcn_perm(xr[3].x, xr[2].x, 0x07060302u)};
    *(u32x2*)(ub + 2 * U_CH) = (u32x2){__builtin_amdgcn_perm(xr[1].y, xr[0].y, 0x05040100u), __builtin_amdgcn_perm(xr[3].y, xr[2].y, 0x05040100u)};
    *(u32x2*)(ub + 3 * U_CH) = (u32x2){__builtin_amdgcn_perm(xr[1].y, xr[0].y, 0x07060302u), __builtin_amdgcn_perm(xr[3].y, xr[2].y, 0x07060302u)};
    if (tid < 240) *(u32x2*)(smem + U_OFF + (tid / 60) * U_CH + (tid % 60) * 8) = (u32x2){0u, 0u};
  }
  STAMP(3);
  {
    float s0 = 0.f, s1 = 0.f;
#pragma unroll
    for (int i = 0; i < 4; ++i) {
      const float wl = bf_lo(gwv[i]), wh_ = bf_hi(gwv[i]);
      const u32 p0 = pk_bf16(bf_lo(gx0[i]) * wl, bf_hi(gx0[i]) * wh_), p1 = pk_bf16(bf_lo(gx1[i]) * wl, bf_hi(gx1[i]) * wh_);
      s0 += bf_lo(p0) + bf_hi(p0); s1 += bf_lo(p1) + bf_hi(p1);
    }
#pragma unroll
    for (int o = 32; o >= 1; o >>= 1) { s0 += __shfl_xor(s0, o); s1 += __shfl_xor(s1, o); }
    if (lane < 2) {
      const float s = rbf(lane ? s1 : s0);
      ((float*)(ws + WS_G))[16 * cg + 2 * w + lane] = rbf(1.f / (1.f + expf(-s)));
    }
  }
  STAMP(4);
  __syncthreads();
  STAMP(5);
  f32x4 kd[2] = {{0, 0, 0, 0}, {0, 0, 0, 0}};
  {
    int e0, e1;
    if (half == 0) {
      e0 = 16 + l15;
      e1 = 32 + ((0x8766654443222100ull >> (4 * l15)) & 15);
    } else {
      const int gq = l15 >> 2, r = min(l15 & 3, 2);
      e0 = 41 + 5 * (gq >> 1) + 2 * (gq & 1) + r;
      e1 = 51 + 3 * gq + r;
    }
    const char* tch = smem + ch * TB_CH + 16 * g;
    const char* pa0 = tch + e0 * TB_ROW; const char* pa1 = tch + e1 * TB_ROW; const char* pb = tch + l15 * TB_ROW;
#pragma unroll
    for (int ks = 0; ks < 8; ++ks) {
      const h16x8 b = *(const h16x8*)(pb + 64 * ks);
      kd[0] = MFMA16H(*(const h16x8*)(pa0 + 64 * ks), b, kd[0]);
      kd[1] = MFMA16H(*(const h16x8*)(pa1 + 64 * ks), b, kd[1]);
    }
  }
  STAMP(6);
  __syncthreads();
  {
    char* kr = smem + ch * KR_CH;
    const f32x4 k0 = kd[0] * kscale, k1 = kd[1] * kscale;
    if (half == 0) {
      const int y = 2044 - 16 * l15 - 4 * g;
      *(u32x2*)(kr + 2 * y) = (u32x2){pk_bf16(k0[3], k0[2]), pk_bf16(k0[1], k0[0])};
      *(u32x2*)(kr + 2 * (y - 256)) = (u32x2){pk_bf16(k1[3], k1[2]), pk_bf16(k1[1], k1[0])};
      if (lane < 4) *(u32x4*)(kr + 4096 + 16 * lane) = (u32x4){0u, 0u, 0u, 0u};
    } else {
      const int y2 = 2040 - (512 + 256 * (g >> 1) + 16 * l15 + 8 * (g & 1));
      *(u32x4*)(kr + 2 * y2) = (u32x4){pk_bf16(k0[2], k0[2]), pk_bf16(k0[1], k0[1]), pk_bf16(k0[1], k0[0]), pk_bf16(k0[0], k0[0])};
      const int y3 = 2032 - (1024 + 256 * g + 16 * l15);
      const u32 a2 = pk_bf16(k1[2], k1[2]), a1 = pk_bf16(k1[1], k1[1]), a0 = pk_bf16(k1[0], k1[0]);
      *(u32x4*)(kr + 2 * y3) = (u32x4){a2, a2, a1, a1};
      *(u32x4*)(kr + 2 * y3 + 16) = (u32x4){a1, pk_bf16(k1[1], k1[0]), a0, a0};
    }
  }
  __syncthreads();
  STAMP(7);
  f32x4 acc[8];
#pragma unroll
  for (int i = 0; i < 8; ++i) acc[i] = (f32x4){0, 0, 0, 0};
  {
    const int yb = 2031 - l15 + 8 * g - 128 * half;
    const u32 sh = (yb & 1) * 16;
    const char* ka = smem + ch * KR_CH + (yb >> 1) * 4;
    const char* ua = smem + U_OFF + ch * U_CH + (8 * g + 16 * l15 + 224 - 128 * half) * 2;
#pragma unroll
    for (int rr = 0; rr < 4; ++rr) {
      s16x8 A[8], B[8];
#pragma unroll
      for (int n = 0; n < 8; ++n) {
        const u32* kp = (const u32*)(ka - 64 * rr - 512 * n);
        const u32 d0 = kp[0], d1 = kp[1], d2 = kp[2], d3 = kp[3], d4 = kp[4];
        A[n] = __builtin_bit_cast(s16x8, (u32x4){__builtin_amdgcn_alignbit(d1, d0, sh), __builtin_amdgcn_alignbit(d2, d1, sh),
                                                 __builtin_amdgcn_alignbit(d3, d2, sh), __builtin_amdgcn_alignbit(d4, d3, sh)});
        B[n] = *(const s16x8*)(ua - 64 * rr + 512 * n);
      }
#pragma unroll
      for (int nu = 0; nu < 8; ++nu)
#pragma unroll
        for (int mu = 0; mu < 8 - nu; ++mu) acc[nu + mu] = MFMA16(A[nu], B[mu], acc[nu + mu]);
    }
  }
  STAMP(8);
  {
    char* ex = smem + EX_OFF + (ch * 2 + half) * 4096 + lane * 16;
    f32x4 keep[4];
    if (half) {
#pragma unroll
      for (int i = 0; i < 4; ++i) { *(f32x4*)(ex + 1024 * i) = acc[i]; keep[i] = acc[4 + i]; }
    } else {
#pragma unroll
      for (int i = 0; i < 4; ++i) { *(f32x4*)(ex + 1024 * i) = acc[4 + i]; keep[i] = acc[i]; }
    }
    __syncthreads();
    const char* ei = smem + EX_OFF + (ch * 2 + (half ^ 1)) * 4096 + lane * 16;
    const int t0 = 1024 * half + 16 * l15 + 4 * g;
    u16* bt = (u16*)(ws + WS_BT) + (size_t)c * SEQ + t0;
    const char* up = smem + U_OFF + ch * U_CH + 480 + 2 * t0;
#pragma unroll
    for (int i = 0; i < 4; ++i) {
      const f32x4 y = keep[i] + *(const f32x4*)(ei + 1024 * i);
      const u32x2 uu = *(const u32x2*)(up + 512 * i);
      const u32 y01 = pk_bf16(y[0], y[1]), y23 = pk_bf16(y[2], y[3]);
      const u32 s01 = pk_bf16(bf_lo(uu.x) * dsk_f, bf_hi(uu.x) * dsk_f), s23 = pk_bf16(bf_lo(uu.y) * dsk_f, bf_hi(uu.y) * dsk_f);
      *(u32x2*)(bt + 256 * i) = (u32x2){pk_bf16(bf_lo(y01) + bf_lo(s01), bf_hi(y01) + bf_hi(s01)),
                                        pk_bf16(bf_lo(y23) + bf_lo(s23), bf_hi(y23) + bf_hi(s23))};
    }
  }
  STAMP(9); WAIT_VM(0);
  STAMP_FLUSH(0, ws);
}

__global__ __launch_bounds__(512, 2) void k1(const u16* __restrict__ x, const u16* __restrict__ wqkv, const u16* __restrict__ lar,
                                             const u16* __restrict__ aim, const u16* __restrict__ cre, const u16* __restrict__ cim,
                                             const u16* __restrict__ ldt, const u16* __restrict__ dsk, const u16* __restrict__ gw,
                                             char* __restrict__ ws) {
  extern __shared__ __attribute__((aligned(16))) char smem[];
  const int b = blockIdx.x, xcd = b & 7, slot = b >> 3, idx = slot >> 1;
  if (slot & 1) conv_role(x, lar, aim, cre, cim, ldt, dsk, gw, ws, xcd * 16 + idx, smem);
  else gemm_role(x, wqkv, ws, 4 * (xcd >> 1) + (idx & 3), 4 * (xcd & 1) + (idx >> 2), smem);
}

static constexpr int K2_WO = 0, K2_BT = 65536, K2_KV = 65536 + 8192, K2_EX = K2_KV + 17408, K2_LDS = K2_EX + 16384;

__global__ __launch_bounds__(256, 1) void k2(const u16* __restrict__ wo, const char* __restrict__ ws, u16* __restrict__ out) {
  extern __shared__ __attribute__((aligned(16))) char smem[];
  const int tid = threadIdx.x, lane = tid & 63;
  const int w = __builtin_amdgcn_readfirstlane(tid >> 6);
  const int b = blockIdx.x, xcd = b & 7, idx = b >> 3;
  const int rt = 8 * (xcd >> 1) + (idx & 7), ct = 4 * (xcd & 1) + (idx >> 3);
  const int th = w & 1, kh = w >> 1, hh = lane >> 5, l31 = lane & 31;
  STAMP_DECL; STAMP(0);
  f32x4 kvr[9];
  const f32x4* kvs = (const f32x4*)(ws + WS_KV);
#pragma unroll
  for (int i = 0; i < 8; ++i) kvr[i] = kvs[tid + 256 * i];
  kvr[8] = kvs[2048 + (tid & 127)];
  s16x8 pf[16];
  const s16x8* pqs = (const s16x8*)(ws + WS_PQ + (size_t)((2 * rt + th) * 32 + 16 * kh) * 1024) + lane;
#pragma unroll
  for (int i = 0; i < 16; ++i) pf[i] = pqs[64 * i];
  const float gt = ((const float*)(ws + WS_G))[64 * rt + 32 * th + l31];
  STAMP(1);
#pragma unroll
  for (int i = 0; i < 8; ++i) *(u32x2*)(smem + K2_KV + (tid + 256 * i) * 8) = (u32x2){pk_bf16(kvr[i][0], kvr[i][1]), pk_bf16(kvr[i][2], kvr[i][3])};
  if (tid < 128) *(u32x2*)(smem + K2_KV + (2048 + tid) * 8) = (u32x2){pk_bf16(kvr[8][0], kvr[8][1]), pk_bf16(kvr[8][2], kvr[8][3])};
  STAMP(2);
  {
    const int sr = lane >> 3, sp = lane & 7;
    const u16* src = wo + (size_t)(128 * w + sr) * DM + 64 * ct + ((sp ^ (((sr >> 1) & 1) << 2)) << 3);
#pragma unroll
    for (int i = 0; i < 16; ++i) glds16(src + (size_t)i * 8 * DM, smem + K2_WO + (128 * w + 8 * i) * 128);
    const u16* bs = (const u16*)(ws + WS_BT) + (size_t)(64 * ct + 16 * w + sr) * SEQ + 64 * rt + ((sp ^ (((sr >> 1) & 1) << 2)) << 3);
    glds16(bs, smem + K2_BT + 16 * w * 128);
    glds16(bs + 8 * SEQ, smem + K2_BT + (16 * w + 8) * 128);
  }
  STAMP(3);
  WAIT_VM(0);
  STAMP(4);
  __syncthreads();
  STAMP(5);
  f32x16 acc[2];
#pragma unroll
  for (int i = 0; i < 16; ++i) { acc[0][i] = 0.f; acc[1][i] = 0.f; }
  const f32x16 zero16 = acc[0];
  const char* kva = smem + K2_KV + 16 * kh * 544 + min(l31, 16) * 32 + hh * 16;
  const int blk = (lane >> 4) & 1, q4 = (lane & 15) >> 2, p4 = lane & 3;
  const int sw = ((q4 >> 1) & 1) << 6;
  const char* wa = smem + K2_WO + (256 * kh + 4 * hh + q4) * 128 + blk * 32 + p4 * 8;
#pragma unroll
  for (int i = 0; i < 16; ++i) {
    const f32x16 d = MFMA32(*(const s16x8*)(kva + i * 544), pf[i], zero16);
    const float inv = __builtin_amdgcn_rcpf(d[8] + 1e-6f);
    const u32x4 of = {pk_bf16(d[0] * inv, d[1] * inv), pk_bf16(d[2] * inv, d[3] * inv), pk_bf16(d[4] * inv, d[5] * inv), pk_bf16(d[6] * inv, d[7] * inv)};
#pragma unroll
    for (int c2 = 0; c2 < 2; ++c2) {
      const char* p = wa + i * 2048 + ((c2 * 64) ^ sw);
      acc[c2] = MFMA32(cat8(tr_read(p), tr_read(p + 1024)), __builtin_bit_cast(s16x8, of), acc[c2]);
    }
  }
  STAMP(6);
  char* ex = smem + K2_EX + w * 4096 + lane * 16;
#pragma unroll
  for (int i = 0; i < 4; ++i) {
    const f32x16& s = kh ? acc[0] : acc[1];
    *(f32x4*)(ex + 1024 * i) = (f32x4){s[4 * i], s[4 * i + 1], s[4 * i + 2], s[4 * i + 3]};
  }
  __syncthreads();
  const char* ei = smem + K2_EX + (w ^ 2) * 4096 + lane * 16;
  const float g1 = rbf(1.f - gt);
  const char* ba = smem + K2_BT + (32 * kh + 4 * hh + q4) * 128 + (((th * 64) ^ sw) + blk * 32 + p4 * 8);
  u16* op = out + (size_t)(64 * rt + 32 * th + l31) * DM + 64 * ct + 32 * kh + 4 * hh;
#pragma unroll
  for (int i = 0; i < 4; ++i) {
    const f32x16& s = kh ? acc[1] : acc[0];
    const f32x4 o = *(const f32x4*)(ei + 1024 * i);
    const u32x2 bb = __builtin_bit_cast(u32x2, tr_read(ba + i * 1024));
    const float r0 = gt * (s[4 * i] + o[0]) + g1 * bf_lo(bb.x), r1 = gt * (s[4 * i + 1] + o[1]) + g1 * bf_hi(bb.x);
    const float r2 = gt * (s[4 * i + 2] + o[2]) + g1 * bf_lo(bb.y), r3 = gt * (s[4 * i + 3] + o[3]) + g1 * bf_hi(bb.y);
    *(u32x2*)(op + 8 * i) = (u32x2){pk_bf16(r0, r1), pk_bf16(r2, r3)};
  }
  STAMP(9); WAIT_VM(0);
  STAMP_FLUSH(1, ws);
}

extern "C" void kernel_launch(void* const* d_in, const int* in_sizes, int n_in, void* d_out, int out_size, void* d_ws,
                              size_t ws_size, hipStream_t stream) {
  (void)in_sizes; (void)n_in; (void)out_size; (void)ws_size;
  (void)hipFuncSetAttribute((const void*)k1, hipFuncAttributeMaxDynamicSharedMemorySize, 163840);
  (void)hipFuncSetAttribute((const void*)k2, hipFuncAttributeMaxDynamicSharedMemorySize, K2_LDS);
  (void)hipMemsetAsync((char*)d_ws + WS_KV, 0, KV_BYTES, stream);
  k1<<<256, 512, 163840, stream>>>((const u16*)d_in[0], (const u16*)d_in[1], (const u16*)d_in[4], (const u16*)d_in[5],
                                   (const u16*)d_in[6], (const u16*)d_in[7], (const u16*)d_in[8], (const u16*)d_in[9],
                                   (const u16*)d_in[10], (char*)d_ws);
  k2<<<256, 256, K2_LDS, stream>>>((const u16*)d_in[2], (const char*)d_ws, (u16*)d_out);
}
```

```cpp
#include <hip/hip_runtime.h>
#include <hip/hip_bf16.h>
#include <stdint.h>

typedef unsigned int u32;
typedef unsigned short u16;
typedef __attribute__((ext_vector_type(8))) short s16x8;
typedef __attribute__((ext_vector_type(4))) short s16x4;
typedef __attribute__((ext_vector_type(8))) _Float16 h16x8;
typedef __attribute__((ext_vector_type(2))) _Float16 h16x2;
typedef __attribute__((ext_vector_type(2))) __bf16 b16x2;
typedef __attribute__((ext_vector_type(2))) float f32x2;
typedef __attribute__((ext_vector_type(4))) float f32x4;
typedef __attribute__((ext_vector_type(16))) float f32x16;
typedef __attribute__((ext_vector_type(2))) u32 u32x2;
typedef __attribute__((ext_vector_type(4))) u32 u32x4;
#define LDS_AS __attribute__((address_space(3)))
#define DI __device__ __forceinline__

static constexpr int SEQ = 2048, DM = 512;
static constexpr size_t WS_KV = 0;
static constexpr size_t WS_G = 65536;
static constexpr size_t WS_PQ = 1u << 20;
static constexpr size_t WS_BT = 4u << 20;
static constexpr int KV_BYTES = 32 * 272 * 4;

#define STAMP_DECL do {} while (0)
#define STAMP(i) do {} while (0)
#define STAMP_FLUSH(kern, wsp) do {} while (0)
#define WAIT_VM(n) asm volatile("s_waitcnt vmcnt(" #n ")" ::: "memory")
#define WAIT_L0() __builtin_amdgcn_s_waitcnt(0xC07F)
#define RAW_BAR() __builtin_amdgcn_s_barrier()
#define SCHED() __builtin_amdgcn_sched_barrier(0)
#define LBAR() do { WAIT_L0(); asm volatile("" ::: "memory"); RAW_BAR(); asm volatile("" ::: "memory"); } while (0)
typedef LDS_AS char* lptr;
#define LD(T, p) (*(const LDS_AS T*)(p))
#define ST(T, p) (*(LDS_AS T*)(p))

DI u32 pk_bf16(float a, float b) { return __builtin_bit_cast(u32, __builtin_convertvector((f32x2){a, b}, b16x2)); }
DI u32 pk_f16(f32x2 v) { return __builtin_bit_cast(u32, __builtin_convertvector(v, h16x2)); }
DI float bf_lo(u32 v) { return __builtin_bit_cast(float, v << 16); }
DI float bf_hi(u32 v) { return __builtin_bit_cast(float, v & 0xffff0000u); }
DI float u2f(u32 v) { return __builtin_bit_cast(float, v); }
DI b16x2 u2b(u32 v) { return __builtin_bit_cast(b16x2, v); }
DI float rbf(float v) { return bf_lo(pk_bf16(v, v)); }
static constexpr float LOG2E = 1.44269504088896341f, EPS = 1e-6f;
DI float phi_f(float t) {
  float lo, hi;
  asm("v_min_f32 %0, %1, %2" : "=v"(lo) : "s"(EPS), "v"(t));
  asm("v_max_f32 %0, %1, %2" : "=v"(hi) : "s"(EPS), "v"(t));
  return hi + __builtin_amdgcn_exp2f(__builtin_fmaf(lo, LOG2E, -EPS * LOG2E));
}
DI float exp_f(float v) {
  const float p = v * LOG2E;
  const float r = __builtin_fmaf(v, 1.925963033500011e-8f, __builtin_fmaf(v, LOG2E, -p));
  return __builtin_amdgcn_exp2f(p) * __builtin_fmaf(r, 0.6931471805599453f, 1.f);
}
#define DPP_F(v, ctrl, rmask, bc) __builtin_bit_cast(float, __builtin_amdgcn_update_dpp(0, __builtin_bit_cast(int, v), ctrl, rmask, 0xf, bc))
DI float wave_sum(float r) {
  r += DPP_F(r, 0x111, 0xf, true); r += DPP_F(r, 0x112, 0xf, true); r += DPP_F(r, 0x114, 0xf, true); r += DPP_F(r, 0x118, 0xf, true);
  r += DPP_F(r, 0x142, 0xa, false); r += DPP_F(r, 0x143, 0xc, false);
  return r;
}
DI u32 wave_umax(u32 r) {
#define DPP_U(v, ctrl, rmask, bc) (u32)__builtin_amdgcn_update_dpp(0, (int)(v), ctrl, rmask, 0xf, bc)
  r = max(r, DPP_U(r, 0x111, 0xf, true)); r = max(r, DPP_U(r, 0x112, 0xf, true)); r = max(r, DPP_U(r, 0x114, 0xf, true));
  r = max(r, DPP_U(r, 0x118, 0xf, true)); r = max(r, DPP_U(r, 0x142, 0xa, false)); r = max(r, DPP_U(r, 0x143, 0xc, false));
  return (u32)__builtin_amdgcn_readlane((int)r, 63);
}
DI s16x4 tr_read(lptr p) { return __builtin_amdgcn_ds_read_tr16_b64_v4i16((LDS_AS s16x4*)p); }
DI s16x8 cat8(s16x4 a, s16x4 b) { return __builtin_shufflevector(a, b, 0, 1, 2, 3, 4, 5, 6, 7); }
typedef __amdgpu_buffer_rsrc_t rsrc_t;
DI rsrc_t mk_rsrc(const void* p, int bytes) { return __builtin_amdgcn_make_buffer_rsrc((void*)p, 0, bytes, 0x00020000); }
DI void blds16(rsrc_t r, lptr l, int voff, int soff) { __builtin_amdgcn_raw_ptr_buffer_load_lds(r, (LDS_AS void*)l, 16, voff, soff, 0, 0); }
DI f32x2 cmul(f32x2 a, f32x2 w) {
  f32x2 wr = {-w.y, w.x};
  return (f32x2){a.x, a.x} * w + (f32x2){a.y, a.y} * wr;
}
DI f32x2 cexp_p(float alpha, float theta, float p) {
  float m = __builtin_amdgcn_exp2f(alpha * p);
  float f = __builtin_amdgcn_fractf(theta * p);
  return (f32x2){m * __builtin_amdgcn_cosf(f), m * __builtin_amdgcn_sinf(f)};
}
#define MFMA16(a, b, c) __builtin_amdgcn_mfma_f32_16x16x32_bf16((a), (b), (c), 0, 0, 0)
#define MFMA16H(a, b, c) __builtin_amdgcn_mfma_f32_16x16x32_f16((a), (b), (c), 0, 0, 0)
#define MFMA32(a, b, c) __builtin_amdgcn_mfma_f32_32x32x16_bf16((a), (b), (c), 0, 0, 0)

static constexpr int STAGE = 40960;

DI void gemm_role(const u16* __restrict__ x, const u16* __restrict__ wqkv, char* __restrict__ ws, int tt, int hg, lptr smem) {
  const int tid = threadIdx.x, lane = tid & 63;
  const int w = __builtin_amdgcn_readfirstlane(tid >> 6);
  const int wm = w >> 2, wh = w & 3, g = lane >> 4, l15 = lane & 15;
  STAMP_DECL; STAMP(0);
  const int sr = lane >> 3, sp = lane & 7;
  const rsrc_t rx = mk_rsrc(x, SEQ * DM * 2), rw = mk_rsrc(wqkv, DM * 1536 * 2);
  const int xv = sr * 1024 + ((sp ^ sr) << 4), xs0 = (tt * 128 + 16 * w) * 1024;
  const int fw = ((sr >> 1) & 1) | ((w & 1) << 1);
  const int wv = sr * 3072 + ((sp ^ (fw << 1)) << 4), ws0 = 8 * w * 3072 + hg * 128;
#define ISS(s, i) do { lptr sb = smem + ((s) & 3) * STAGE; \
    if ((i) < 2) blds16(rx, sb + w * 2048 + (i) * 1024, xv, xs0 + (s) * 128 + (i) * 8192); \
    else blds16(rw, sb + 16384 + ((i) - 2) * 8192 + w * 1024, wv, ws0 + (s) * 64 * 3072 + ((i) - 2) * 1024); } while (0)
#define ISSUE(s) do { ISS(s, 0); ISS(s, 1); ISS(s, 2); ISS(s, 3); ISS(s, 4); } while (0)
  ISSUE(0); ISSUE(1);
  const int xo = (64 * wm + l15) * 128 + ((g ^ (lane & 7)) << 4);
  const int q4 = l15 >> 2, p4 = lane & 3;
  const int wo_ = 16384 + (8 * g + q4) * 128 + ((wh ^ (((q4 >> 1) & 1) | ((g & 1) << 1))) << 5) + 8 * p4;
  f32x4 aq[4], ak[4], av[4];
#pragma unroll
  for (int i = 0; i < 4; ++i) { aq[i] = (f32x4){EPS, EPS, EPS, EPS}; ak[i] = aq[i]; av[i] = (f32x4){0, 0, 0, 0}; }
  s16x8 xa[4], wa[3], xb[4], wb[3];
#define LOADF(xf, wf, s, ks) do { lptr sb = smem + ((s) & 3) * STAGE; \
    _Pragma("unroll") for (int st = 0; st < 3; ++st) \
      wf[st] = cat8(tr_read(sb + wo_ + st * 8192 + (ks) * 4096), tr_read(sb + wo_ + st * 8192 + (ks) * 4096 + 512)); \
    _Pragma("unroll") for (int mt = 0; mt < 4; ++mt) xf[mt] = LD(s16x8, sb + ((xo ^ ((ks) * 64)) + mt * 2048)); } while (0)
#define MM3(xf, wf, mt) do { aq[mt] = MFMA16(wf[0], xf[mt], aq[mt]); ak[mt] = MFMA16(xf[mt], wf[1], ak[mt]); av[mt] = MFMA16(xf[mt], wf[2], av[mt]); \
    asm volatile("" : "+v"(aq[mt]), "+v"(ak[mt]), "+v"(av[mt])); } while (0)
#define MMI(xf, wf, is) do { SCHED(); MM3(xf, wf, 0); SCHED(); if ((is) >= 0) { ISS(is, 0); ISS(is, 1); } SCHED(); MM3(xf, wf, 1); SCHED(); \
    if ((is) >= 0) { ISS(is, 2); ISS(is, 3); } SCHED(); MM3(xf, wf, 2); SCHED(); if ((is) >= 0) ISS(is, 4); SCHED(); MM3(xf, wf, 3); SCHED(); } while (0)
#define STEP(s, vm, i1, i2) do { WAIT_L0(); LOADF(xb, wb, s, 1); MMI(xa, wa, i1); \
    WAIT_L0(); WAIT_VM(vm); RAW_BAR(); LOADF(xa, wa, (s) + 1, 0); MMI(xb, wb, i2); } while (0)
  STAMP(1);
  WAIT_VM(5); RAW_BAR();
  STAMP(2);
  LOADF(xa, wa, 0, 0);
  STEP(0, 5, 2, 3);
  STAMP(3);
  STEP(1, 10, 4, 5);
  STEP(2, 10, -1, 6);
  STEP(3, 10, -1, 7);
  STAMP(4);
  STEP(4, 10, -1, -1);
  STEP(5, 5, -1, -1);
  STEP(6, 0, -1, -1);
  STAMP(5);
  WAIT_L0(); LOADF(xb, wb, 7, 1); MMI(xa, wa, -1); WAIT_L0(); MMI(xb, wb, -1);
  STAMP(6);
#undef ISS
#undef ISSUE
#undef LOADF
#undef MM3
#undef MMI
#undef STEP
  const int h = 4 * hg + wh;
  f32x4 kvt = {0, 0, 0, 0}, zz = {0, 0, 0, 0};
  const u32x4 ones_u = {0x3f803f80u, 0x3f803f80u, 0x3f803f80u, 0x3f803f80u};
#pragma unroll
  for (int pr = 0; pr < 2; ++pr) {
    u32x4 pk, vv;
#pragma unroll
    for (int m = 0; m < 2; ++m) {
      const f32x4 kk = ak[2 * pr + m], v4 = av[2 * pr + m];
      pk[2 * m] = pk_bf16(phi_f(kk[0]), phi_f(kk[1])); pk[2 * m + 1] = pk_bf16(phi_f(kk[2]), phi_f(kk[3]));
      vv[2 * m] = pk_bf16(v4[0], v4[1]); vv[2 * m + 1] = pk_bf16(v4[2], v4[3]);
    }
    kvt = MFMA16(__builtin_bit_cast(s16x8, vv), __builtin_bit_cast(s16x8, pk), kvt);
    zz = MFMA16(__builtin_bit_cast(s16x8, ones_u), __builtin_bit_cast(s16x8, pk), zz);
  }
  lptr red = smem + (wm * 4 + wh) * 1088 + l15 * 4;
#pragma unroll
  for (int r = 0; r < 4; ++r) ST(float, red + (4 * g + r) * 64) = kvt[r];
  if (g == 0) ST(float, red + 1024) = zz[0];
  STAMP(7);
  LBAR();
  STAMP(8);
  float* kvacc = (float*)(ws + WS_KV) + hg * 1088;
  atomicAdd(kvacc + tid, LD(float, smem + tid * 4) + LD(float, smem + 4352 + tid * 4));
  atomicAdd(kvacc + 512 + tid, LD(float, smem + 2048 + tid * 4) + LD(float, smem + 4352 + 2048 + tid * 4));
  if (tid < 64) atomicAdd(kvacc + 1024 + tid, LD(float, smem + 4096 + tid * 4) + LD(float, smem + 4352 + 4096 + tid * 4));
  char* pqb = ws + WS_PQ + ((size_t)((tt * 4 + 2 * wm) * 32 + h) * 2 + (g >> 1)) * 512 + l15 * 16 + (g & 1) * 256;
#pragma unroll
  for (int pr = 0; pr < 2; ++pr) {
    const f32x4 q0 = aq[2 * pr], q1 = aq[2 * pr + 1];
    const auto s0 = __builtin_amdgcn_permlane16_swap(pk_bf16(phi_f(q0[0]), phi_f(q0[1])), pk_bf16(phi_f(q1[0]), phi_f(q1[1])), false, false);
    const auto s1 = __builtin_amdgcn_permlane16_swap(pk_bf16(phi_f(q0[2]), phi_f(q0[3])), pk_bf16(phi_f(q1[2]), phi_f(q1[3])), false, false);
    *(u32x4*)(pqb + pr * 32768) = (u32x4){s0[0], s1[0], s0[1], s1[1]};
  }
  STAMP(9);
  STAMP_FLUSH(0, ws);
}

static constexpr int TB_ROW = 544, TB_CH = 63 * TB_ROW;
static constexpr int U_OFF = 4 * TB_CH, U_CH = (2048 + 240) * 2;
static constexpr int KR_CP = 4160, KR_CH = 2 * KR_CP;
static constexpr int EX_OFF = 4 * KR_CH;

DI void conv_role(const u16* __restrict__ x, const u16* __restrict__ lar, const u16* __restrict__ aim, const u16* __restrict__ cre,
                  const u16* __restrict__ cim, const u16* __restrict__ ldt, const u16* __restrict__ dsk, const u16* __restrict__ gw,
                  char* __restrict__ ws, int cg, lptr smem) {
  const int tid = threadIdx.x, lane = tid & 63;
  const int w = __builtin_amdgcn_readfirstlane(tid >> 6);
  const int ch = w >> 1, half = w & 1, g = lane >> 4, l15 = lane & 15;
  const int c = 4 * cg + ch;
  STAMP_DECL; STAMP(0);
  const u32 lar2 = *(const u32*)(lar + c * 128 + 2 * lane), aim2 = *(const u32*)(aim + c * 128 + 2 * lane);
  const u32 cre2 = *(const u32*)(cre + c * 128 + 2 * lane), cim2 = *(const u32*)(cim + c * 128 + 2 * lane);
  const u32 ldt_u = ldt[c], dsk_u = dsk[c];
  SCHED(); RAW_BAR(); SCHED();
  const rsrc_t rx = mk_rsrc(x, SEQ * DM * 2);
  u32x2 xr[4];
#pragma unroll
  for (int r = 0; r < 4; ++r) xr[r] = __builtin_amdgcn_raw_buffer_load_b64(rx, tid * 4096 + r * 1024, 8 * cg, 0);
  const u32x4 gx0 = __builtin_amdgcn_raw_buffer_load_b128(rx, lane * 16, (16 * cg + 2 * w) * 1024, 0);
  const u32x4 gx1 = __builtin_amdgcn_raw_buffer_load_b128(rx, lane * 16 + 1024, (16 * cg + 2 * w) * 1024, 0);
  const u32x4 gwv = *(const u32x4*)(gw + 8 * lane);
  SCHED();
  const float dt = rbf(exp_f(bf_lo(ldt_u))), dsk_f = bf_lo(dsk_u);
  float al[2], th[2];
  al[0] = -rbf(exp_f(bf_lo(lar2))) * dt * LOG2E; al[1] = -rbf(exp_f(bf_hi(lar2))) * dt * LOG2E;
  th[0] = bf_lo(aim2) * dt * 0.15915494309189535f; th[1] = bf_hi(aim2) * dt * 0.15915494309189535f;
  STAMP(1);
  u32 ce = wave_umax(max(max(cre2 & 0x7f80u, (cre2 >> 16) & 0x7f80u), max(cim2 & 0x7f80u, (cim2 >> 16) & 0x7f80u))) >> 7;
  ce = ce < 1 ? 1 : (ce > 253 ? 253 : ce);
  const float cinv = __builtin_bit_cast(float, (254u - ce) << 23);
  const float kscale = 2.f * dt * __builtin_bit_cast(float, ce << 23);
  lptr tb = smem + ch * TB_CH + lane * 8;
#define PUT(e, v0, v1) ST(u32x2, tb + (e) * TB_ROW) = (u32x2){pk_f16(v0), pk_f16(v1)}
#define CONJ(v) ((f32x2){(v).x, -(v).y})
  if (half == 0) {
    f32x2 w16[2], xc[2], w1[2], yc[2];
#pragma unroll
    for (int m = 0; m < 2; ++m) { w16[m] = cexp_p(al[m], th[m], 16.f); w1[m] = CONJ(cexp_p(al[m], th[m], 1.f)); }
    xc[0] = (f32x2){bf_lo(cre2) * cinv, bf_lo(cim2) * cinv}; xc[1] = (f32x2){bf_hi(cre2) * cinv, bf_hi(cim2) * cinv};
    PUT(0, xc[0], xc[1]);
    PUT(16, ((f32x2){1.f, 0.f}), ((f32x2){1.f, 0.f}));
    PUT(17, w1[0], w1[1]);
    yc[0] = w1[0]; yc[1] = w1[1];
#pragma unroll
    for (int i = 1; i < 16; ++i) {
      xc[0] = cmul(xc[0], w16[0]); xc[1] = cmul(xc[1], w16[1]);
      PUT(i, xc[0], xc[1]);
      if (i >= 2) { yc[0] = cmul(yc[0], w1[0]); yc[1] = cmul(yc[1], w1[1]); PUT(16 + i, yc[0], yc[1]); }
    }
  } else {
    f32x2 w2[2], w4[2], w8[2], wB[2], a[2], t[2];
#pragma unroll
    for (int m = 0; m < 2; ++m) {
      w2[m] = CONJ(cexp_p(al[m], th[m], 2.f)); wB[m] = CONJ(cexp_p(al[m], th[m], 256.f));
      w4[m] = cmul(w2[m], w2[m]); w8[m] = cmul(w4[m], w4[m]);
    }
    a[0] = wB[0]; a[1] = wB[1]; t[0] = a[0]; t[1] = a[1];
    PUT(32, t[0], t[1]);
#pragma unroll
    for (int m = 1; m < 9; ++m) { t[0] = cmul(t[0], w2[0]); t[1] = cmul(t[1], w2[1]); PUT(32 + m, t[0], t[1]); }
#pragma unroll
    for (int s = 2; s < 4; ++s) {
      a[0] = cmul(a[0], wB[0]); a[1] = cmul(a[1], wB[1]); t[0] = a[0]; t[1] = a[1];
      PUT(41 + 5 * (s - 2), t[0], t[1]);
#pragma unroll
      for (int m = 1; m < 5; ++m) { t[0] = cmul(t[0], w4[0]); t[1] = cmul(t[1], w4[1]); PUT(41 + 5 * (s - 2) + m, t[0], t[1]); }
    }
#pragma unroll
    for (int s = 4; s < 8; ++s) {
      a[0] = cmul(a[0], wB[0]); a[1] = cmul(a[1], wB[1]); t[0] = a[0]; t[1] = a[1];
      PUT(51 + 3 * (s - 4), t[0], t[1]);
#pragma unroll
      for (int m = 1; m < 3; ++m) { t[0] = cmul(t[0], w8[0]); t[1] = cmul(t[1], w8[1]); PUT(51 + 3 * (s - 4) + m, t[0], t[1]); }
    }
  }
#undef PUT
  STAMP(2);
  LBAR();
  STAMP(3);
  f32x4 kd[2] = {{0, 0, 0, 0}, {0, 0, 0, 0}};
  {
    int e0, e1;
    if (half == 0) {
      e0 = 16 + l15;
      e1 = 32 + ((0x8766654443222100ull >> (4 * l15)) & 15);
    } else {
      const int gq = l15 >> 2, r = min(l15 & 3, 2);
      e0 = 41 + 5 * (gq >> 1) + 2 * (gq & 1) + r;
      e1 = 51 + 3 * gq + r;
    }
    lptr tch = smem + ch * TB_CH + 16 * g;
    lptr pa0 = tch + e0 * TB_ROW, pa1 = tch + e1 * TB_ROW, pb = tch + l15 * TB_ROW;
    h16x8 fb[8], f0[8], f1[8];
#pragma unroll
    for (int ks = 0; ks < 8; ++ks) { fb[ks] = LD(h16x8, pb + 64 * ks); f0[ks] = LD(h16x8, pa0 + 64 * ks); f1[ks] = LD(h16x8, pa1 + 64 * ks); }
    SCHED();
    lptr ub = smem + U_OFF + 480 + tid * 8;
    ST(u32x2, ub + 0 * U_CH) = (u32x2){__builtin_amdgcn_perm(xr[1].x, xr[0].x, 0x05040100u), __builtin_amdgcn_perm(xr[3].x, xr[2].x, 0x05040100u)};
    ST(u32x2, ub + 1 * U_CH) = (u32x2){__builtin_amdgcn_perm(xr[1].x, xr[0].x, 0x07060302u), __builtin_amdgcn_perm(xr[3].x, xr[2].x, 0x07060302u)};
    ST(u32x2, ub + 2 * U_CH) = (u32x2){__builtin_amdgcn_perm(xr[1].y, xr[0].y, 0x05040100u), __builtin_amdgcn_perm(xr[3].y, xr[2].y, 0x05040100u)};
    ST(u32x2, ub + 3 * U_CH) = (u32x2){__builtin_amdgcn_perm(xr[1].y, xr[0].y, 0x07060302u), __builtin_amdgcn_perm(xr[3].y, xr[2].y, 0x07060302u)};
    if (lane < 30) ST(u32x4, smem + U_OFF + ch * U_CH + lane * 16) = (u32x4){0u, 0u, 0u, 0u};
    SCHED();
#pragma unroll
    for (int ks = 0; ks < 8; ++ks) { kd[0] = MFMA16H(f0[ks], fb[ks], kd[0]); kd[1] = MFMA16H(f1[ks], fb[ks], kd[1]); }
  }
  STAMP(4);
  LBAR();
  STAMP(5);
  {
    lptr kr = smem + ch * KR_CH;
    const f32x4 k0 = kd[0] * kscale, k1 = kd[1] * kscale;
    if (half == 0) {
      if (lane < 8) ST(u32x4, kr + (lane >> 2) * KR_CP + 4096 + 16 * (lane & 3)) = (u32x4){0u, 0u, 0u, 0u};
      const int y = 2044 - 16 * l15 - 4 * g;
      const u32 p32 = pk_bf16(k0[3], k0[2]), p10 = pk_bf16(k0[1], k0[0]);
      ST(u32x2, kr + 2 * y) = (u32x2){p32, p10};
      ST(u16, kr + KR_CP + 2 * y + 2) = (u16)p32;
      ST(u32, kr + KR_CP + 2 * y + 4) = pk_bf16(k0[2], k0[1]);
      ST(u16, kr + KR_CP + 2 * y + 8) = (u16)(p10 >> 16);
      const u32 q10 = pk_bf16(k1[1], k1[0]);
      ST(u32x2, kr + 2 * (y - 256)) = (u32x2){pk_bf16(k1[3], k1[2]), q10};
      ST(u32x2, kr + KR_CP + 2 * (y - 256)) = (u32x2){pk_bf16(k1[3], k1[3]), pk_bf16(k1[2], k1[1])};
      if (lane == 0) ST(u16, kr + KR_CP + 2 * 1792) = (u16)q10;
    } else {
      const int y2 = 2040 - (512 + 256 * (g >> 1) + 16 * l15 + 8 * (g & 1));
      const u32 b2 = pk_bf16(k0[2], k0[2]), b1 = pk_bf16(k0[1], k0[1]), b0 = pk_bf16(k0[0], k0[0]);
      ST(u32x4, kr + 2 * y2) = (u32x4){b2, b1, pk_bf16(k0[1], k0[0]), b0};
      ST(u32x4, kr + KR_CP + 2 * y2) = (u32x4){b2, pk_bf16(k0[2], k0[1]), b1, b0};
      const int y3 = 2032 - (1024 + 256 * g + 16 * l15);
      const u32 a2 = pk_bf16(k1[2], k1[2]), a1 = pk_bf16(k1[1], k1[1]), a0 = pk_bf16(k1[0], k1[0]);
      ST(u32x4, kr + 2 * y3) = (u32x4){a2, a2, a1, a1};
      ST(u32x4, kr + 2 * y3 + 16) = (u32x4){a1, pk_bf16(k1[1], k1[0]), a0, a0};
      ST(u32x4, kr + KR_CP + 2 * y3) = (u32x4){a2, a2, pk_bf16(k1[2], k1[1]), a1};
      ST(u32x4, kr + KR_CP + 2 * y3 + 16) = (u32x4){a1, a1, a0, a0};
    }
  }
  {
    float s0 = 0.f, s1 = 0.f;
#pragma unroll
    for (int i = 0; i < 4; ++i) {
      s0 = __builtin_amdgcn_fdot2_f32_bf16(u2b(gx0[i]), u2b(gwv[i]), s0, false);
      s1 = __builtin_amdgcn_fdot2_f32_bf16(u2b(gx1[i]), u2b(gwv[i]), s1, false);
    }
    s0 = wave_sum(s0); s1 = wave_sum(s1);
    const float g0 = rbf(__builtin_amdgcn_rcpf(1.f + __builtin_amdgcn_exp2f(-LOG2E * rbf(s0))));
    const float g1 = rbf(__builtin_amdgcn_rcpf(1.f + __builtin_amdgcn_exp2f(-LOG2E * rbf(s1))));
    if (lane == 63) *(f32x2*)(ws + WS_G + (16 * cg + 2 * w) * 4) = (f32x2){g0, g1};
  }
  STAMP(6);
  LBAR();
  STAMP(7);
  f32x4 acc[8];
#pragma unroll
  for (int i = 0; i < 8; ++i) acc[i] = (f32x4){0, 0, 0, 0};
  {
    const int yb = 2031 - l15 + 8 * g - 128 * half - 96 - 1792;
    lptr ka = smem + ch * KR_CH + (yb & 1) * KR_CP + ((yb + 1) >> 1) * 4;
    lptr ua = smem + U_OFF + ch * U_CH + (8 * g + 16 * l15 + 224 - 128 * half - 96) * 2;
    u32x4 A0[8], A1[8]; s16x8 B0[8], B1[8];
#define LOADC(A, B, rr) do { _Pragma("unroll") for (int n = 0; n < 8; ++n) { \
      lptr kp = ka + 64 * (3 - (rr)) + 512 * (7 - n); \
      A[n] = (u32x4){LD(u32, kp), LD(u32, kp + 4), LD(u32, kp + 8), LD(u32, kp + 12)}; \
      B[n] = LD(s16x8, ua + 64 * (3 - (rr)) + 512 * n); } } while (0)
#define MMC(A, B) do { SCHED(); _Pragma("unroll") for (int nu = 0; nu < 8; ++nu) _Pragma("unroll") for (int mu = 0; mu < 8 - nu; ++mu) \
      acc[nu + mu] = MFMA16(__builtin_bit_cast(s16x8, A[nu]), B[mu], acc[nu + mu]); \
      asm volatile("" : "+v"(acc[0]), "+v"(acc[1]), "+v"(acc[2]), "+v"(acc[3]), "+v"(acc[4]), "+v"(acc[5]), "+v"(acc[6]), "+v"(acc[7])); SCHED(); } while (0)
    LOADC(A0, B0, 0);
    WAIT_L0(); LOADC(A1, B1, 1); MMC(A0, B0);
    WAIT_L0(); LOADC(A0, B0, 2); MMC(A1, B1);
    WAIT_L0(); LOADC(A1, B1, 3); MMC(A0, B0);
    WAIT_L0(); MMC(A1, B1);
#undef LOADC
#undef MMC
  }
  STAMP(8);
  {
    lptr ex = smem + EX_OFF + (ch * 2 + half) * 4096 + lane * 16;
    f32x4 keep[4];
    if (half) {
#pragma unroll
      for (int i = 0; i < 4; ++i) { ST(f32x4, ex + 1024 * i) = acc[i]; keep[i] = acc[4 + i]; }
    } else {
#pragma unroll
      for (int i = 0; i < 4; ++i) { ST(f32x4, ex + 1024 * i) = acc[4 + i]; keep[i] = acc[i]; }
    }
    LBAR();
    lptr ei = smem + EX_OFF + (ch * 2 + (half ^ 1)) * 4096 + lane * 16;
    const int t0 = 1024 * half + 16 * l15 + 4 * g;
    u16* bt = (u16*)(ws + WS_BT) + (size_t)c * SEQ + t0;
    lptr up = smem + U_OFF + ch * U_CH + 480 + 2 * t0;
#pragma unroll
    for (int i = 0; i < 4; ++i) {
      const f32x4 y = keep[i] + LD(f32x4, ei + 1024 * i);
      const u32x2 uu = LD(u32x2, up + 512 * i);
      const u32 y01 = pk_bf16(y[0], y[1]), y23 = pk_bf16(y[2], y[3]);
      const u32 s01 = pk_bf16(bf_lo(uu.x) * dsk_f, bf_hi(uu.x) * dsk_f), s23 = pk_bf16(bf_lo(uu.y) * dsk_f, bf_hi(uu.y) * dsk_f);
      *(u32x2*)(bt + 256 * i) = (u32x2){pk_bf16(bf_lo(y01) + bf_lo(s01), bf_hi(y01) + bf_hi(s01)),
                                        pk_bf16(bf_lo(y23) + bf_lo(s23), bf_hi(y23) + bf_hi(s23))};
    }
  }
  STAMP(9);
  STAMP_FLUSH(0, ws);
}

__global__ __launch_bounds__(512, 2) void k1(const u16* __restrict__ x, const u16* __restrict__ wqkv, const u16* __restrict__ lar,
                                             const u16* __restrict__ aim, const u16* __restrict__ cre, const u16* __restrict__ cim,
                                             const u16* __restrict__ ldt, const u16* __restrict__ dsk, const u16* __restrict__ gw,
                                             char* __restrict__ ws) {
  extern __shared__ __attribute__((aligned(16))) char smem_[];
  lptr smem = (lptr)smem_;
  const int b = blockIdx.x, xcd = b & 7, slot = b >> 3, idx = slot >> 1;
  if (slot & 1) conv_role(x, lar, aim, cre, cim, ldt, dsk, gw, ws, xcd * 16 + idx, smem);
  else gemm_role(x, wqkv, ws, 4 * (xcd >> 1) + (idx & 3), 4 * (xcd & 1) + (idx >> 2), smem);
}

static constexpr int K2_WO = 0, K2_BT = 65536, K2_KV = 65536 + 8192, K2_EX = K2_KV + 17408, K2_LDS = K2_EX + 16384;

__global__ __launch_bounds__(256, 2) void k2(const u16* __restrict__ wo, const char* __restrict__ ws, u16* __restrict__ out) {
  extern __shared__ __attribute__((aligned(16))) char smem_[];
  lptr smem = (lptr)smem_;
  const int tid = threadIdx.x, lane = tid & 63;
  const int w = __builtin_amdgcn_readfirstlane(tid >> 6);
  const int b = blockIdx.x, xcd = b & 7, idx = b >> 3;
  const int rt = 8 * (xcd >> 1) + (idx & 7), ct = 4 * (xcd & 1) + (idx >> 3);
  const int th = w & 1, kh = w >> 1, hh = lane >> 5, l31 = lane & 31;
  STAMP_DECL; STAMP(0);
  const rsrc_t rws = mk_rsrc(ws, 6 << 20), rwo = mk_rsrc(wo, DM * DM * 2);
  u32x4 kvr[9];
#pragma unroll
  for (int i = 0; i < 8; ++i) kvr[i] = __builtin_amdgcn_raw_buffer_load_b128(rws, tid * 16, (int)WS_KV + 4096 * i, 0);
  kvr[8] = __builtin_amdgcn_raw_buffer_load_b128(rws, (tid & 127) * 16, (int)WS_KV + 32768, 0);
  u32x4 pf[16];
  {
    const int sr = lane >> 3, sp = lane & 7, sc = (sp ^ (((sr >> 1) & 1) << 2)) << 4;
    const int pq0 = (int)WS_PQ + ((2 * rt + th) * 32 + 16 * kh) * 1024;
#pragma unroll
    for (int i = 0; i < 16; ++i) {
      pf[i] = __builtin_amdgcn_raw_buffer_load_b128(rws, lane * 16, pq0 + 1024 * i, 0);
      blds16(rwo, smem + K2_WO + (256 * kh + 16 * i + 8 * th) * 128, sr * 1024 + sc, (256 * kh + 16 * i + 8 * th) * 1024 + 128 * ct);
    }
    blds16(rws, smem + K2_BT + 16 * w * 128, sr * 4096 + sc, (int)WS_BT + (64 * ct + 16 * w) * 4096 + 128 * rt);
    blds16(rws, smem + K2_BT + (16 * w + 8) * 128, sr * 4096 + sc, (int)WS_BT + (64 * ct + 16 * w + 8) * 4096 + 128 * rt);
  }
  const float gt = __builtin_bit_cast(float, __builtin_amdgcn_raw_buffer_load_b32(rws, l31 * 4, (int)WS_G + (64 * rt + 32 * th) * 4, 0));
  STAMP(1);
#define F_(v) u2f(v)
#pragma unroll
  for (int i = 0; i < 8; ++i) ST(u32x2, smem + K2_KV + (tid + 256 * i) * 8) = (u32x2){pk_bf16(F_(kvr[i][0]), F_(kvr[i][1])), pk_bf16(F_(kvr[i][2]), F_(kvr[i][3]))};
  if (tid < 128) ST(u32x2, smem + K2_KV + (2048 + tid) * 8) = (u32x2){pk_bf16(F_(kvr[8][0]), F_(kvr[8][1])), pk_bf16(F_(kvr[8][2]), F_(kvr[8][3]))};
  STAMP(3);
  WAIT_VM(0);
  STAMP(4);
  LBAR();
  STAMP(5);
  f32x16 acc[2];
#pragma unroll
  for (int i = 0; i < 16; ++i) { acc[0][i] = 0.f; acc[1][i] = 0.f; }
  f32x16 cinit = acc[0];
  cinit[8] = EPS;
  lptr kva = smem + K2_KV + 16 * kh * 544 + min(l31, 16) * 32 + hh * 16;
  const int blk = (lane >> 4) & 1, q4 = (lane & 15) >> 2, p4 = lane & 3;
  const int sw = ((q4 >> 1) & 1) << 6;
  lptr wa0 = smem + K2_WO + (256 * kh + 4 * hh + q4) * 128 + blk * 32 + p4 * 8 + sw;
  lptr wa1 = smem + K2_WO + (256 * kh + 4 * hh + q4) * 128 + blk * 32 + p4 * 8 + (64 ^ sw);
  {
    f32x16 d[2]; s16x8 kf[2], wf[2][2];
    kf[0] = LD(s16x8, kva); kf[1] = LD(s16x8, kva + 544);
    d[0] = MFMA32(kf[0], __builtin_bit_cast(s16x8, pf[0]), cinit);
#pragma unroll
    for (int i = 0; i < 16; ++i) {
      SCHED();
      wf[0][0] = cat8(tr_read(wa0 + i * 2048), tr_read(wa0 + i * 2048 + 1024));
      wf[0][1] = cat8(tr_read(wa1 + i * 2048), tr_read(wa1 + i * 2048 + 1024));
      if (i + 1 < 16) d[(i + 1) & 1] = MFMA32(kf[(i + 1) & 1], __builtin_bit_cast(s16x8, pf[i + 1]), cinit);
      if (i + 1 < 16) asm volatile("" : "+v"(d[(i + 1) & 1]));
      if (i + 2 < 16) kf[i & 1] = LD(s16x8, kva + (i + 2) * 544);
      SCHED();
      const f32x16& dd = d[i & 1];
      const float inv = __builtin_amdgcn_rcpf(dd[8]);
      const u32x4 of = {pk_bf16(dd[0] * inv, dd[1] * inv), pk_bf16(dd[2] * inv, dd[3] * inv), pk_bf16(dd[4] * inv, dd[5] * inv), pk_bf16(dd[6] * inv, dd[7] * inv)};
      acc[0] = MFMA32(wf[0][0], __builtin_bit_cast(s16x8, of), acc[0]);
      acc[1] = MFMA32(wf[0][1], __builtin_bit_cast(s16x8, of), acc[1]);
      asm volatile("" : "+v"(acc[0]), "+v"(acc[1]));
    }
  }
  STAMP(6);
  lptr ex = smem + K2_EX + w * 4096 + lane * 16;
  f32x16 keep;
  if (kh) {
#pragma unroll
    for (int i = 0; i < 4; ++i) ST(f32x4, ex + 1024 * i) = (f32x4){acc[0][4 * i], acc[0][4 * i + 1], acc[0][4 * i + 2], acc[0][4 * i + 3]};
    keep = acc[1];
  } else {
#pragma unroll
    for (int i = 0; i < 4; ++i) ST(f32x4, ex + 1024 * i) = (f32x4){acc[1][4 * i], acc[1][4 * i + 1], acc[1][4 * i + 2], acc[1][4 * i + 3]};
    keep = acc[0];
  }
  LBAR();
  lptr ei = smem + K2_EX + (w ^ 2) * 4096 + lane * 16;
  const float g1 = rbf(1.f - gt);
  lptr ba = smem + K2_BT + (32 * kh + 4 * hh + q4) * 128 + (((th * 64) ^ sw) + blk * 32 + p4 * 8);
  u16* op = out + (size_t)(64 * rt + 32 * th + l31) * DM + 64 * ct + 32 * kh + 8 * hh;
  u32x2 res[4];
#pragma unroll
  for (int i = 0; i < 4; ++i) {
    const f32x4 o = LD(f32x4, ei + 1024 * i);
    const u32x2 bb = __builtin_bit_cast(u32x2, tr_read(ba + i * 1024));
    const float r0 = gt * (keep[4 * i] + o[0]) + g1 * bf_lo(bb.x), r1 = gt * (keep[4 * i + 1] + o[1]) + g1 * bf_hi(bb.x);
    const float r2 = gt * (keep[4 * i + 2] + o[2]) + g1 * bf_lo(bb.y), r3 = gt * (keep[4 * i + 3] + o[3]) + g1 * bf_hi(bb.y);
    res[i] = (u32x2){pk_bf16(r0, r1), pk_bf16(r2, r3)};
  }
#pragma unroll
  for (int i = 0; i < 2; ++i) {
    const auto sx = __builtin_amdgcn_permlane32_swap(res[2 * i].x, res[2 * i + 1].x, false, false);
    const auto sy = __builtin_amdgcn_permlane32_swap(res[2 * i].y, res[2 * i + 1].y, false, false);
    *(u32x4*)(op + 16 * i) = (u32x4){sx[0], sy[0], sx[1], sy[1]};
  }
  STAMP(9);
  STAMP_FLUSH(1, ws);
}

extern "C" void kernel_launch(void* const* d_in, const int* in_sizes, int n_in, void* d_out, int out_size, void* d_ws,
                              size_t ws_size, hipStream_t stream) {
  (void)in_sizes; (void)n_in; (void)out_size; (void)ws_size;
  (void)hipFuncSetAttribute((const void*)k1, hipFuncAttributeMaxDynamicSharedMemorySize, 163840);
  (void)hipFuncSetAttribute((const void*)k2, hipFuncAttributeMaxDynamicSharedMemorySize, K2_LDS);
  (void)hipMemsetAsync((char*)d_ws + WS_KV, 0, KV_BYTES, stream);
  k1<<<256, 512, 163840, stream>>>((const u16*)d_in[0], (const u16*)d_in[1], (const u16*)d_in[4], (const u16*)d_in[5],
                                   (const u16*)d_in[6], (const u16*)d_in[7], (const u16*)d_in[8], (const u16*)d_in[9],
                                   (const u16*)d_in[10], (char*)d_ws);
  k2<<<256, 256, K2_LDS, stream>>>((const u16*)d_in[2], (const char*)d_ws, (u16*)d_out);
}
```

```cpp
#include <hip/hip_runtime.h>
#include <hip/hip_bf16.h>
#include <stdint.h>

typedef unsigned int u32;
typedef unsigned short u16;
typedef __attribute__((ext_vector_type(8))) short s16x8;
typedef __attribute__((ext_vector_type(4))) short s16x4;
typedef __attribute__((ext_vector_type(8))) _Float16 h16x8;
typedef __attribute__((ext_vector_type(2))) _Float16 h16x2;
typedef __attribute__((ext_vector_type(2))) __bf16 b16x2;
typedef __attribute__((ext_vector_type(2))) float f32x2;
typedef __attribute__((ext_vector_type(4))) float f32x4;
typedef __attribute__((ext_vector_type(16))) float f32x16;
typedef __attribute__((ext_vector_type(2))) u32 u32x2;
typedef __attribute__((ext_vector_type(4))) u32 u32x4;
#define LDS_AS __attribute__((address_space(3)))
#define DI __device__ __forceinline__

static constexpr int SEQ = 2048, DM = 512;
static constexpr size_t WS_KV = 0;
static constexpr size_t WS_G = 65536;
static constexpr size_t WS_PQ = 1u << 20;
static constexpr size_t WS_BT = 4u << 20;
static constexpr size_t WS_SYNC = 49152;
typedef unsigned long long u64;
typedef __attribute__((address_space(1))) u64 gu64;
typedef __attribute__((address_space(1))) u32 gu32;
#define RLX_AGENT __ATOMIC_RELAXED, __HIP_MEMORY_SCOPE_AGENT
__device__ __forceinline__ u64 next_tag(u64 e) { return e * 6364136223846793005ull + 1442695040888963407ull; }

#define STAMP_DECL do {} while (0)
#define STAMP(i) do {} while (0)
#define STAMP_FLUSH(kern, wsp) do {} while (0)
#define ST_AUX 17
#define WAIT_VM(n) asm volatile("s_waitcnt vmcnt(" #n ")" ::: "memory")
#define WAIT_L0() __builtin_amdgcn_s_waitcnt(0xC07F)
#define RAW_BAR() __builtin_amdgcn_s_barrier()
#define SCHED() __builtin_amdgcn_sched_barrier(0)
#define LBAR() do { WAIT_L0(); asm volatile("" ::: "memory"); RAW_BAR(); asm volatile("" ::: "memory"); } while (0)
typedef LDS_AS char* lptr;
#define LD(T, p) (*(const LDS_AS T*)(p))
#define ST(T, p) (*(LDS_AS T*)(p))

DI u32 pk_bf16(float a, float b) { return __builtin_bit_cast(u32, __builtin_convertvector((f32x2){a, b}, b16x2)); }
DI u32 pk_f16(f32x2 v) { return __builtin_bit_cast(u32, __builtin_convertvector(v, h16x2)); }
DI float bf_lo(u32 v) { return __builtin_bit_cast(float, v << 16); }
DI float bf_hi(u32 v) { return __builtin_bit_cast(float, v & 0xffff0000u); }
DI float u2f(u32 v) { return __builtin_bit_cast(float, v); }
DI b16x2 u2b(u32 v) { return __builtin_bit_cast(b16x2, v); }
DI float rbf(float v) { return bf_lo(pk_bf16(v, v)); }
static constexpr float LOG2E = 1.44269504088896341f, EPS = 1e-6f;
DI float phi_f(float t) {
  float lo, hi;
  asm("v_min_f32 %0, %1, %2" : "=v"(lo) : "s"(EPS), "v"(t));
  asm("v_max_f32 %0, %1, %2" : "=v"(hi) : "s"(EPS), "v"(t));
  return hi + __builtin_amdgcn_exp2f(__builtin_fmaf(lo, LOG2E, -EPS * LOG2E));
}
DI float exp_f(float v) {
  const float p = v * LOG2E;
  const float r = __builtin_fmaf(v, 1.925963033500011e-8f, __builtin_fmaf(v, LOG2E, -p));
  return __builtin_amdgcn_exp2f(p) * __builtin_fmaf(r, 0.6931471805599453f, 1.f);
}
#define DPP_F(v, ctrl, rmask, bc) __builtin_bit_cast(float, __builtin_amdgcn_update_dpp(0, __builtin_bit_cast(int, v), ctrl, rmask, 0xf, bc))
DI float wave_sum(float r) {
  r += DPP_F(r, 0x111, 0xf, true); r += DPP_F(r, 0x112, 0xf, true); r += DPP_F(r, 0x114, 0xf, true); r += DPP_F(r, 0x118, 0xf, true);
  r += DPP_F(r, 0x142, 0xa, false); r += DPP_F(r, 0x143, 0xc, false);
  return r;
}
DI u32 wave_umax(u32 r) {
#define DPP_U(v, ctrl, rmask, bc) (u32)__builtin_amdgcn_update_dpp(0, (int)(v), ctrl, rmask, 0xf, bc)
  r = max(r, DPP_U(r, 0x111, 0xf, true)); r = max(r, DPP_U(r, 0x112, 0xf, true)); r = max(r, DPP_U(r, 0x114, 0xf, true));
  r = max(r, DPP_U(r, 0x118, 0xf, true)); r = max(r, DPP_U(r, 0x142, 0xa, false)); r = max(r, DPP_U(r, 0x143, 0xc, false));
  return (u32)__builtin_amdgcn_readlane((int)r, 63);
}
DI s16x4 tr_read(lptr p) { return __builtin_amdgcn_ds_read_tr16_b64_v4i16((LDS_AS s16x4*)p); }
DI s16x8 cat8(s16x4 a, s16x4 b) { return __builtin_shufflevector(a, b, 0, 1, 2, 3, 4, 5, 6, 7); }
typedef __amdgpu_buffer_rsrc_t rsrc_t;
DI rsrc_t mk_rsrc(const void* p, int bytes) { return __builtin_amdgcn_make_buffer_rsrc((void*)p, 0, bytes, 0x00020000); }
DI void blds16(rsrc_t r, lptr l, int voff, int soff) { __builtin_amdgcn_raw_ptr_buffer_load_lds(r, (LDS_AS void*)l, 16, voff, soff, 0, 0); }
DI f32x2 cmul(f32x2 a, f32x2 w) {
  f32x2 t, r;
  asm("v_pk_mul_f32 %0, %1, %2 op_sel:[1,1] op_sel_hi:[1,0]" : "=v"(t) : "v"(a), "v"(w));
  asm("v_pk_fma_f32 %0, %1, %2, %3 op_sel_hi:[0,1,1] neg_lo:[0,0,1]" : "=v"(r) : "v"(a), "v"(w), "v"(t));
  return r;
}
DI f32x2 cexp_p(float alpha, float theta, float p) {
  float m = __builtin_amdgcn_exp2f(alpha * p);
  float f = __builtin_amdgcn_fractf(theta * p);
  return (f32x2){m * __builtin_amdgcn_cosf(f), m * __builtin_amdgcn_sinf(f)};
}
#define MFMA16(a, b, c) __builtin_amdgcn_mfma_f32_16x16x32_bf16((a), (b), (c), 0, 0, 0)
#define MFMA16H(a, b, c) __builtin_amdgcn_mfma_f32_16x16x32_f16((a), (b), (c), 0, 0, 0)
#define MFMA32(a, b, c) __builtin_amdgcn_mfma_f32_32x32x16_bf16((a), (b), (c), 0, 0, 0)

static constexpr int STAGE = 40960;

DI void gemm_role(const u16* __restrict__ x, const u16* __restrict__ wqkv, char* __restrict__ ws, int tt, int hg, lptr smem) {
  const int tid = threadIdx.x, lane = tid & 63;
  const int w = __builtin_amdgcn_readfirstlane(tid >> 6);
  const int wm = w >> 2, wh = w & 3, g = lane >> 4, l15 = lane & 15;
  STAMP_DECL; STAMP(0);
  const int sr = lane >> 3, sp = lane & 7;
  const rsrc_t rx = mk_rsrc(x, SEQ * DM * 2), rw = mk_rsrc(wqkv, DM * 1536 * 2);
  const int xv = sr * 1024 + ((sp ^ sr) << 4), xs0 = (tt * 128 + 16 * w) * 1024;
  const int fw = ((sr >> 1) & 1) | ((w & 1) << 1);
  const int wv = sr * 3072 + ((sp ^ (fw << 1)) << 4), ws0 = 8 * w * 3072 + hg * 128;
#define ISS(s, i) do { lptr sb = smem + ((s) & 3) * STAGE; \
    if ((i) < 2) blds16(rx, sb + w * 2048 + (i) * 1024, xv, xs0 + (s) * 128 + (i) * 8192); \
    else blds16(rw, sb + 16384 + ((i) - 2) * 8192 + w * 1024, wv, ws0 + (s) * 64 * 3072 + ((i) - 2) * 1024); } while (0)
#define ISSUE(s) do { ISS(s, 0); ISS(s, 1); ISS(s, 2); ISS(s, 3); ISS(s, 4); } while (0)
  gu64* sync_e = (gu64*)(ws + WS_SYNC + hg * 256);
  const u64 epoch = __hip_atomic_load(sync_e, RLX_AGENT);
  ISSUE(0); ISSUE(1);
  const int xo = (64 * wm + l15) * 128 + ((g ^ (lane & 7)) << 4);
  const int q4 = l15 >> 2, p4 = lane & 3;
  const int wo_ = 16384 + (8 * g + q4) * 128 + ((wh ^ (((q4 >> 1) & 1) | ((g & 1) << 1))) << 5) + 8 * p4;
  f32x4 aq[4], ak[4], av[4];
#pragma unroll
  for (int i = 0; i < 4; ++i) { aq[i] = (f32x4){EPS, EPS, EPS, EPS}; ak[i] = aq[i]; av[i] = (f32x4){0, 0, 0, 0}; }
  s16x8 xa[4], wa[3], xb[4], wb[3];
#define LOADF(xf, wf, s, ks) do { lptr sb = smem + ((s) & 3) * STAGE; \
    _Pragma("unroll") for (int st = 0; st < 3; ++st) \
      wf[st] = cat8(tr_read(sb + wo_ + st * 8192 + (ks) * 4096), tr_read(sb + wo_ + st * 8192 + (ks) * 4096 + 512)); \
    _Pragma("unroll") for (int mt = 0; mt < 4; ++mt) xf[mt] = LD(s16x8, sb + ((xo ^ ((ks) * 64)) + mt * 2048)); } while (0)
#define MM3(xf, wf, mt) do { aq[mt] = MFMA16(wf[0], xf[mt], aq[mt]); ak[mt] = MFMA16(xf[mt], wf[1], ak[mt]); av[mt] = MFMA16(xf[mt], wf[2], av[mt]); \
    asm volatile("" : "+v"(aq[mt]), "+v"(ak[mt]), "+v"(av[mt])); } while (0)
#define MMI(xf, wf, is) do { SCHED(); MM3(xf, wf, 0); SCHED(); if ((is) >= 0) { ISS(is, 0); ISS(is, 1); } SCHED(); MM3(xf, wf, 1); SCHED(); \
    if ((is) >= 0) { ISS(is, 2); ISS(is, 3); } SCHED(); MM3(xf, wf, 2); SCHED(); if ((is) >= 0) ISS(is, 4); SCHED(); MM3(xf, wf, 3); SCHED(); } while (0)
#define STEP(s, vm, i1, i2) do { WAIT_L0(); LOADF(xb, wb, s, 1); MMI(xa, wa, i1); \
    WAIT_L0(); WAIT_VM(vm); RAW_BAR(); LOADF(xa, wa, (s) + 1, 0); MMI(xb, wb, i2); } while (0)
  STAMP(1);
  WAIT_VM(5); RAW_BAR();
  STAMP(2);
  LOADF(xa, wa, 0, 0);
  STEP(0, 5, 2, 3);
  STAMP(3);
  STEP(1, 10, 4, 5);
  STEP(2, 10, -1, 6);
  STEP(3, 10, -1, 7);
  STAMP(4);
  STEP(4, 10, -1, -1);
  STEP(5, 5, -1, -1);
  u64 ready = __hip_atomic_load(sync_e + 16, RLX_AGENT);
  STEP(6, 0, -1, -1);
  STAMP(5);
  WAIT_L0(); LOADF(xb, wb, 7, 1); MMI(xa, wa, -1); WAIT_L0(); MMI(xb, wb, -1);
  STAMP(6);
#undef ISS
#undef ISSUE
#undef LOADF
#undef MM3
#undef MMI
#undef STEP
  const int h = 4 * hg + wh;
  f32x4 kvt = {0, 0, 0, 0}, zz = {0, 0, 0, 0};
  const u32x4 ones_u = {0x3f803f80u, 0x3f803f80u, 0x3f803f80u, 0x3f803f80u};
#pragma unroll
  for (int pr = 0; pr < 2; ++pr) {
    u32x4 pk, vv;
#pragma unroll
    for (int m = 0; m < 2; ++m) {
      const f32x4 kk = ak[2 * pr + m], v4 = av[2 * pr + m];
      pk[2 * m] = pk_bf16(phi_f(kk[0]), phi_f(kk[1])); pk[2 * m + 1] = pk_bf16(phi_f(kk[2]), phi_f(kk[3]));
      vv[2 * m] = pk_bf16(v4[0], v4[1]); vv[2 * m + 1] = pk_bf16(v4[2], v4[3]);
    }
    kvt = MFMA16(__builtin_bit_cast(s16x8, vv), __builtin_bit_cast(s16x8, pk), kvt);
    zz = MFMA16(__builtin_bit_cast(s16x8, ones_u), __builtin_bit_cast(s16x8, pk), zz);
  }
  lptr red = smem + (wm * 4 + wh) * 1088 + l15 * 4;
#pragma unroll
  for (int r = 0; r < 4; ++r) ST(float, red + (4 * g + r) * 64) = kvt[r];
  if (g == 0) ST(float, red + 1024) = zz[0];
  const u64 tag = next_tag(epoch);
  for (int spin = 0; ready != tag && spin < (1 << 22); ++spin) { __builtin_amdgcn_s_sleep(2); ready = __hip_atomic_load(sync_e + 16, RLX_AGENT); }
  STAMP(7);
  LBAR();
  STAMP(8);
  u32 arrived = 0;
  if (tid == 0) arrived = __hip_atomic_fetch_add((gu32*)(ws + WS_SYNC + 2048 + hg * 128), 1u, RLX_AGENT);
  float* kvacc = (float*)(ws + WS_KV) + hg * 1088;
  atomicAdd(kvacc + tid, LD(float, smem + tid * 4) + LD(float, smem + 4352 + tid * 4));
  atomicAdd(kvacc + 512 + tid, LD(float, smem + 2048 + tid * 4) + LD(float, smem + 4352 + 2048 + tid * 4));
  if (tid < 64) atomicAdd(kvacc + 1024 + tid, LD(float, smem + 4096 + tid * 4) + LD(float, smem + 4352 + 4096 + tid * 4));
  const rsrc_t rws = mk_rsrc(ws, 6 << 20);
  const int pqv = (g >> 1) * 512 + l15 * 16 + (g & 1) * 256, pqs = (int)WS_PQ + ((tt * 4 + 2 * wm) * 32 + h) * 1024;
#pragma unroll
  for (int pr = 0; pr < 2; ++pr) {
    const f32x4 q0 = aq[2 * pr], q1 = aq[2 * pr + 1];
    const auto s0 = __builtin_amdgcn_permlane16_swap(pk_bf16(phi_f(q0[0]), phi_f(q0[1])), pk_bf16(phi_f(q1[0]), phi_f(q1[1])), false, false);
    const auto s1 = __builtin_amdgcn_permlane16_swap(pk_bf16(phi_f(q0[2]), phi_f(q0[3])), pk_bf16(phi_f(q1[2]), phi_f(q1[3])), false, false);
    __builtin_amdgcn_raw_buffer_store_b128((u32x4){s0[0], s1[0], s0[1], s1[1]}, rws, pqv, pqs + pr * 32768, ST_AUX);
  }
  if (tid == 0 && arrived == 15) __hip_atomic_store(sync_e, tag, RLX_AGENT);
  STAMP(9);
  STAMP_FLUSH(0, ws);
}

static constexpr int TB_ROW = 544, TB_CH = 63 * TB_ROW;
static constexpr int U_OFF = 4 * TB_CH, U_CH = (2048 + 240) * 2;
static constexpr int KR_CP = 4160, KR_CH = 2 * KR_CP;

DI void conv_role(const u16* __restrict__ x, const u16* __restrict__ lar, const u16* __restrict__ aim, const u16* __restrict__ cre,
                  const u16* __restrict__ cim, const u16* __restrict__ ldt, const u16* __restrict__ dsk, const u16* __restrict__ gw,
                  char* __restrict__ ws, int cg, lptr smem) {
  const int tid = threadIdx.x, lane = tid & 63;
  const int w = __builtin_amdgcn_readfirstlane(tid >> 6);
  const int ch = w & 3, half = w >> 2, g = lane >> 4, l15 = lane & 15;
  const int c = 4 * cg + ch;
  STAMP_DECL; STAMP(0);
  const rsrc_t rx = mk_rsrc(x, SEQ * DM * 2);
  const float dsk_f = bf_lo(dsk[c]);
  const bool clear = (cg & 15) == 0;
  u64 epoch = 0;
  u32x4 gx[4], gwv;
  if (w < 4) {
    const int cw = 4 * cg + w;
    const u32 lar2 = *(const u32*)(lar + cw * 128 + 2 * lane), aim2 = *(const u32*)(aim + cw * 128 + 2 * lane);
    const u32 cre2 = *(const u32*)(cre + cw * 128 + 2 * lane), cim2 = *(const u32*)(cim + cw * 128 + 2 * lane);
    const u32 ldt_u = ldt[cw];
    SCHED(); RAW_BAR(); SCHED();
    const float dt = rbf(exp_f(bf_lo(ldt_u)));
    float al[2], th[2];
    al[0] = -rbf(exp_f(bf_lo(lar2))) * dt * LOG2E; al[1] = -rbf(exp_f(bf_hi(lar2))) * dt * LOG2E;
    th[0] = bf_lo(aim2) * dt * 0.15915494309189535f; th[1] = bf_hi(aim2) * dt * 0.15915494309189535f;
    STAMP(1);
    u32 ce = wave_umax(max(max(cre2 & 0x7f80u, (cre2 >> 16) & 0x7f80u), max(cim2 & 0x7f80u, (cim2 >> 16) & 0x7f80u))) >> 7;
    ce = ce < 1 ? 1 : (ce > 253 ? 253 : ce);
    const float cinv = __builtin_bit_cast(float, (254u - ce) << 23);
    const float kscale = 2.f * dt * __builtin_bit_cast(float, ce << 23);
    lptr tb = smem + w * TB_CH + lane * 8;
#define PUT(e, v) ST(u32x2, tb + (e) * TB_ROW) = (u32x2){pk_f16(v[0]), pk_f16(v[1])}
#define CMUL2(d, a, b) do { d[0] = cmul(a[0], b[0]); d[1] = cmul(a[1], b[1]); } while (0)
#define CONJ(v) ((f32x2){(v).x, -(v).y})
    {
      f32x2 y[17][2], wB[2], w16[2], xa[2], xb[2], a[2], t[2];
#pragma unroll
      for (int m = 0; m < 2; ++m) {
        y[1][m] = CONJ(cexp_p(al[m], th[m], 1.f)); w16[m] = cexp_p(al[m], th[m], 16.f); wB[m] = CONJ(cexp_p(al[m], th[m], 256.f));
        y[16][m] = CONJ(w16[m]);
      }
      xa[0] = (f32x2){bf_lo(cre2) * cinv, bf_lo(cim2) * cinv}; xa[1] = (f32x2){bf_hi(cre2) * cinv, bf_hi(cim2) * cinv};
      ST(u32x2, tb + 16 * TB_ROW) = (u32x2){0x3c00u, 0x3c00u};
      PUT(0, xa); PUT(17, y[1]); PUT(32, wB);
      f32x2 w32[2];
      CMUL2(xb, xa, w16); CMUL2(w32, w16, w16); CMUL2(y[2], y[1], y[1]); CMUL2(a, wB, wB);
      PUT(1, xb); PUT(18, y[2]); PUT(41, a);
      f32x2 aS[6][2];
      aS[0][0] = a[0]; aS[0][1] = a[1];
#pragma unroll
      for (int i = 1; i < 8; ++i) {
        CMUL2(xa, xa, w32); CMUL2(xb, xb, w32);
        CMUL2(y[2 * i + 1], y[2 * i - 1], y[2]);
        if (i < 7) CMUL2(y[2 * i + 2], y[2 * i], y[2]);
        if (i < 6) CMUL2(aS[i], aS[i - 1], wB);
        PUT(2 * i, xa); PUT(2 * i + 1, xb); PUT(17 + 2 * i, y[2 * i + 1]);
        if (i < 7) PUT(18 + 2 * i, y[2 * i + 2]);
        if (i == 1) PUT(46, aS[1]);
        if (i >= 2 && i < 6) PUT(51 + 3 * (i - 2), aS[i]);
        CMUL2(t, wB, y[2 * i]); PUT(32 + i, t);
      }
      CMUL2(t, wB, y[16]); PUT(40, t);
#pragma unroll
      for (int s = 0; s < 2; ++s)
#pragma unroll
        for (int m = 1; m < 5; ++m) { CMUL2(t, aS[s], y[4 * m]); PUT(41 + 5 * s + m, t); }
#pragma unroll
      for (int s = 2; s < 6; ++s)
#pragma unroll
        for (int m = 1; m < 3; ++m) { CMUL2(t, aS[s], y[8 * m]); PUT(51 + 3 * (s - 2) + m, t); }
    }
#undef PUT
    STAMP(2);
    f32x4 kd[4] = {{0, 0, 0, 0}, {0, 0, 0, 0}, {0, 0, 0, 0}, {0, 0, 0, 0}};
    {
      const int gq = l15 >> 2, r = min(l15 & 3, 2);
      lptr tch = smem + w * TB_CH + 16 * g;
      lptr pb = tch + l15 * TB_ROW, pa0 = pb + 16 * TB_ROW, pa1 = tch + (32 + ((0x8766654443222100ull >> (4 * l15)) & 15)) * TB_ROW;
      lptr pa2 = tch + (41 + 5 * (gq >> 1) + 2 * (gq & 1) + r) * TB_ROW, pa3 = tch + (51 + 3 * gq + r) * TB_ROW;
      h16x8 f[2][4][5];
#define LDK(bf, q) do { _Pragma("unroll") for (int k2 = 0; k2 < 4; ++k2) { const int o = 64 * (4 * (q) + k2); \
        f[bf][k2][0] = LD(h16x8, pa0 + o); f[bf][k2][1] = LD(h16x8, pa1 + o); f[bf][k2][2] = LD(h16x8, pa2 + o); \
        f[bf][k2][3] = LD(h16x8, pa3 + o); f[bf][k2][4] = LD(h16x8, pb + o); } } while (0)
#define MMK(bf) do { _Pragma("unroll") for (int k2 = 0; k2 < 4; ++k2) _Pragma("unroll") for (int e = 0; e < 4; ++e) \
        kd[e] = MFMA16H(f[bf][k2][e], f[bf][k2][4], kd[e]); } while (0)
      SCHED(); LDK(0, 0); SCHED(); LDK(1, 1); SCHED(); MMK(0); MMK(1);
#undef LDK
#undef MMK
    }
    STAMP(3);
    lptr kr = smem + w * TB_CH;
    {
      const f32x4 k0 = kd[0] * kscale, k1 = kd[1] * kscale;
      if (lane < 8) ST(u32x4, kr + (lane >> 2) * KR_CP + 4096 + 16 * (lane & 3)) = (u32x4){0u, 0u, 0u, 0u};
      const int y = 2044 - 16 * l15 - 4 * g;
      const u32 p32 = pk_bf16(k0[3], k0[2]), p10 = pk_bf16(k0[1], k0[0]);
      ST(u32x2, kr + 2 * y) = (u32x2){p32, p10};
      ST(u16, kr + KR_CP + 2 * y + 2) = (u16)p32;
      ST(u32, kr + KR_CP + 2 * y + 4) = pk_bf16(k0[2], k0[1]);
      ST(u16, kr + KR_CP + 2 * y + 8) = (u16)(p10 >> 16);
      const u32 q10 = pk_bf16(k1[1], k1[0]);
      ST(u32x2, kr + 2 * (y - 256)) = (u32x2){pk_bf16(k1[3], k1[2]), q10};
      ST(u32x2, kr + KR_CP + 2 * (y - 256)) = (u32x2){pk_bf16(k1[3], k1[3]), pk_bf16(k1[2], k1[1])};
      if (lane == 0) ST(u16, kr + KR_CP + 2 * 1792) = (u16)q10;
    }
    {
      const f32x4 k0 = kd[2] * kscale, k1 = kd[3] * kscale;
      const int y2 = 2040 - (512 + 256 * (g >> 1) + 16 * l15 + 8 * (g & 1));
      const u32 b2 = pk_bf16(k0[2], k0[2]), b1 = pk_bf16(k0[1], k0[1]), b0 = pk_bf16(k0[0], k0[0]);
      ST(u32x4, kr + 2 * y2) = (u32x4){b2, b1, pk_bf16(k0[1], k0[0]), b0};
      ST(u32x4, kr + KR_CP + 2 * y2) = (u32x4){b2, pk_bf16(k0[2], k0[1]), b1, b0};
      const int y3 = 2032 - (1024 + 256 * g + 16 * l15);
      const u32 a2 = pk_bf16(k1[2], k1[2]), a1 = pk_bf16(k1[1], k1[1]), a0 = pk_bf16(k1[0], k1[0]);
      ST(u32x4, kr + 2 * y3) = (u32x4){a2, a2, a1, a1};
      ST(u32x4, kr + 2 * y3 + 16) = (u32x4){a1, pk_bf16(k1[1], k1[0]), a0, a0};
      ST(u32x4, kr + KR_CP + 2 * y3) = (u32x4){a2, a2, pk_bf16(k1[2], k1[1]), a1};
      ST(u32x4, kr + KR_CP + 2 * y3 + 16) = (u32x4){a1, a1, a0, a0};
    }
#pragma unroll
    for (int r = 0; r < 4; ++r) gx[r] = __builtin_amdgcn_raw_buffer_load_b128(rx, lane * 16 + 1024 * r, (16 * cg + 4 * w) * 1024, 0);
    gwv = *(const u32x4*)(gw + 8 * lane);
    STAMP(4);
  } else {
    const int t2 = tid - 256;
    if (clear) {
      epoch = __hip_atomic_load((gu64*)(ws + WS_SYNC + (cg >> 4) * 256), RLX_AGENT);
      const rsrc_t rk = mk_rsrc(ws, 65536);
      const u32x4 z4 = {0u, 0u, 0u, 0u};
      __builtin_amdgcn_raw_buffer_store_b128(z4, rk, t2 * 16, (int)WS_KV + (cg >> 4) * 4352, 16);
      if (t2 < 16) __builtin_amdgcn_raw_buffer_store_b128(z4, rk, t2 * 16, (int)WS_KV + (cg >> 4) * 4352 + 4096, 16);
      if (t2 == 16) __builtin_amdgcn_raw_buffer_store_b32(0u, rk, 0, (int)WS_SYNC + 2048 + (cg >> 4) * 128, 16);
    }
    SCHED(); RAW_BAR(); SCHED();
    u32x2 xr[8];
#pragma unroll
    for (int r = 0; r < 8; ++r) xr[r] = __builtin_amdgcn_raw_buffer_load_b64(rx, t2 * 4096 + (r & 3) * 1024, 8 * cg + (r >> 2) * (1024 * 1024), 0);
    STAMP(1);
    if (lane < 30) ST(u32x4, smem + U_OFF + (w - 4) * U_CH + lane * 16) = (u32x4){0u, 0u, 0u, 0u};
#pragma unroll
    for (int hf = 0; hf < 2; ++hf) {
      lptr ub = smem + U_OFF + 480 + t2 * 8 + hf * 2048;
      const u32x2 x0 = xr[4 * hf], x1 = xr[4 * hf + 1], x2 = xr[4 * hf + 2], x3 = xr[4 * hf + 3];
      ST(u32x2, ub + 0 * U_CH) = (u32x2){__builtin_amdgcn_perm(x1.x, x0.x, 0x05040100u), __builtin_amdgcn_perm(x3.x, x2.x, 0x05040100u)};
      ST(u32x2, ub + 1 * U_CH) = (u32x2){__builtin_amdgcn_perm(x1.x, x0.x, 0x07060302u), __builtin_amdgcn_perm(x3.x, x2.x, 0x07060302u)};
      ST(u32x2, ub + 2 * U_CH) = (u32x2){__builtin_amdgcn_perm(x1.y, x0.y, 0x05040100u), __builtin_amdgcn_perm(x3.y, x2.y, 0x05040100u)};
      ST(u32x2, ub + 3 * U_CH) = (u32x2){__builtin_amdgcn_perm(x1.y, x0.y, 0x07060302u), __builtin_amdgcn_perm(x3.y, x2.y, 0x07060302u)};
    }
    WAIT_VM(0);
    STAMP(4);
  }
  lptr ka[8];
  {
    const int yb = 2031 - l15 + 8 * g - 128 * half - 96 - 1792;
    lptr k7 = smem + ch * TB_CH + (yb & 1) * KR_CP + ((yb + 1) >> 1) * 4;
#pragma unroll
    for (int n = 0; n < 8; ++n) { ka[n] = k7 + 512 * (7 - n); asm volatile("" : "+v"(ka[n])); }
  }
  lptr ua = smem + U_OFF + ch * U_CH + (8 * g + 16 * l15 + 224 - 128 * half - 96) * 2;
  STAMP(6);
  LBAR();
  if (clear && tid == 256) __hip_atomic_store((gu64*)(ws + WS_SYNC + (cg >> 4) * 256 + 128), next_tag(epoch), RLX_AGENT);
  STAMP(7);
  f32x4 acc[8];
#pragma unroll
  for (int i = 0; i < 8; ++i) acc[i] = (f32x4){0, 0, 0, 0};
  {
    u32x4 A0[8], A1[8]; s16x8 B0[8], B1[8];
#define LOADC(A, B, rr) do { _Pragma("unroll") for (int n = 0; n < 8; ++n) { \
      lptr kp = ka[n] + 64 * (3 - (rr)); \
      A[n] = (u32x4){LD(u32, kp), LD(u32, kp + 4), LD(u32, kp + 8), LD(u32, kp + 12)}; \
      B[n] = LD(s16x8, ua + 64 * (3 - (rr)) + 512 * n); } } while (0)
#define MMC(A, B) do { _Pragma("unroll") for (int nu = 0; nu < 8; ++nu) _Pragma("unroll") for (int mu = 0; mu < 8 - nu; ++mu) \
      acc[nu + mu] = MFMA16(__builtin_bit_cast(s16x8, A[nu]), B[mu], acc[nu + mu]); } while (0)
#define PINC() do { asm volatile("" : "+v"(acc[0]), "+v"(acc[1]), "+v"(acc[2]), "+v"(acc[3]), "+v"(acc[4]), "+v"(acc[5]), "+v"(acc[6]), "+v"(acc[7])); SCHED(); } while (0)
#define MIXC() do { _Pragma("unroll") for (int q = 0; q < 12; ++q) { __builtin_amdgcn_sched_group_barrier(0x8, 3, 0); __builtin_amdgcn_sched_group_barrier(0x100, 2, 0); } } while (0)
    LOADC(A0, B0, 0);
    SCHED();
    WAIT_L0(); SCHED(); LOADC(A1, B1, 1); MMC(A0, B0); MIXC(); PINC();
    WAIT_L0(); SCHED(); LOADC(A0, B0, 2); MMC(A1, B1); MIXC(); PINC();
    WAIT_L0(); SCHED(); LOADC(A1, B1, 3); MMC(A0, B0); MIXC(); PINC();
    WAIT_L0(); SCHED(); MMC(A1, B1); PINC();
#undef LOADC
#undef MMC
  }
  STAMP(8);
  {
    lptr ex = smem + ch * TB_CH + KR_CH + half * 4096 + lane * 16;
    f32x4 keep[4];
    if (half) {
#pragma unroll
      for (int i = 0; i < 4; ++i) { ST(f32x4, ex + 1024 * i) = acc[i]; keep[i] = acc[4 + i]; }
    } else {
#pragma unroll
      for (int i = 0; i < 4; ++i) { ST(f32x4, ex + 1024 * i) = acc[4 + i]; keep[i] = acc[i]; }
    }
    const int t0 = 1024 * half + 16 * l15 + 4 * g;
    lptr up = smem + U_OFF + ch * U_CH + 480 + 2 * t0;
#pragma unroll
    for (int i = 0; i < 4; ++i) {
      const u32x2 uu = LD(u32x2, up + 512 * i);
      keep[i] += (f32x4){bf_lo(uu.x), bf_hi(uu.x), bf_lo(uu.y), bf_hi(uu.y)} * dsk_f;
    }
    if (w < 4) {
      float sg[4];
#pragma unroll
      for (int r = 0; r < 4; ++r) {
        float s0 = 0.f;
#pragma unroll
        for (int i = 0; i < 4; ++i) s0 = __builtin_amdgcn_fdot2_f32_bf16(u2b(gx[r][i]), u2b(gwv[i]), s0, false);
        sg[r] = rbf(__builtin_amdgcn_rcpf(1.f + __builtin_amdgcn_exp2f(-LOG2E * rbf(wave_sum(s0)))));
      }
      if (lane == 63) *(f32x4*)(ws + WS_G + (16 * cg + 4 * w) * 4) = (f32x4){sg[0], sg[1], sg[2], sg[3]};
    }
    LBAR();
    lptr ei = smem + ch * TB_CH + KR_CH + (half ^ 1) * 4096 + lane * 16;
    const rsrc_t rws = mk_rsrc(ws, 6 << 20);
#pragma unroll
    for (int i = 0; i < 4; ++i) {
      const f32x4 y = keep[i] + LD(f32x4, ei + 1024 * i);
      __builtin_amdgcn_raw_buffer_store_b64((u32x2){pk_bf16(y[0], y[1]), pk_bf16(y[2], y[3])}, rws, 2 * t0 + 512 * i, (int)WS_BT + c * 4096, ST_AUX);
    }
  }
  STAMP(9);
  STAMP_FLUSH(0, ws);
}

__global__ __launch_bounds__(512, 2) void k1(const u16* __restrict__ x, const u16* __restrict__ wqkv, const u16* __restrict__ lar,
                                             const u16* __restrict__ aim, const u16* __restrict__ cre, const u16* __restrict__ cim,
                                             const u16* __restrict__ ldt, const u16* __restrict__ dsk, const u16* __restrict__ gw,
                                             char* __restrict__ ws) {
  extern __shared__ __attribute__((aligned(16))) char smem_[];
  lptr smem = (lptr)smem_;
  const int b = blockIdx.x, xcd = b & 7, slot = b >> 3, idx = slot >> 1;
  if (slot & 1) conv_role(x, lar, aim, cre, cim, ldt, dsk, gw, ws, xcd * 16 + idx, smem);
  else gemm_role(x, wqkv, ws, 4 * (xcd >> 1) + (idx & 3), 4 * (xcd & 1) + (idx >> 2), smem);
}

static constexpr int K2_WO = 0, K2_BT = 65536, K2_KV = 65536 + 8192, K2_EX = K2_KV + 17408, K2_LDS = K2_EX + 16384;

DI void out_role(const u16* __restrict__ wo, const char* __restrict__ ws, u16* __restrict__ out, lptr smem) {
  const int tid = threadIdx.x, lane = tid & 63;
  const int w = __builtin_amdgcn_readfirstlane(tid >> 6);
  const int b = blockIdx.x, xcd = b & 7, idx = b >> 3;
  const int rt = 8 * (xcd >> 1) + (idx & 7), ct = 4 * (xcd & 1) + (idx >> 3);
  const int th = w & 1, kh = w >> 1, hh = lane >> 5, l31 = lane & 31;
  STAMP_DECL; STAMP(0);
  const rsrc_t rws = mk_rsrc(ws, 6 << 20), rwo = mk_rsrc(wo, DM * DM * 2);
  u32x4 kvr[9];
#pragma unroll
  for (int i = 0; i < 8; ++i) kvr[i] = __builtin_amdgcn_raw_buffer_load_b128(rws, tid * 16, (int)WS_KV + 4096 * i, 0);
  kvr[8] = __builtin_amdgcn_raw_buffer_load_b128(rws, (tid & 127) * 16, (int)WS_KV + 32768, 0);
  u32x4 pf[16];
  const int sr = lane >> 3, sp = lane & 7, sc = (sp ^ (((sr >> 1) & 1) << 2)) << 4;
  const int pq0 = (int)WS_PQ + ((2 * rt + th) * 32 + 16 * kh) * 1024;
#define CHUNK(c) do { _Pragma("unroll") for (int i = 4 * (c); i < 4 * (c) + 4; ++i) { \
      pf[i] = __builtin_amdgcn_raw_buffer_load_b128(rws, lane * 16, pq0 + 1024 * i, 0); \
      blds16(rwo, smem + K2_WO + (256 * kh + 16 * i + 8 * th) * 128, sr * 1024 + sc, (256 * kh + 16 * i + 8 * th) * 1024 + 128 * ct); } } while (0)
  CHUNK(0); CHUNK(1);
  STAMP(1);
#define F_(v) u2f(v)
#pragma unroll
  for (int i = 0; i < 8; ++i) ST(u32x2, smem + K2_KV + (tid + 256 * i) * 8) = (u32x2){pk_bf16(F_(kvr[i][0]), F_(kvr[i][1])), pk_bf16(F_(kvr[i][2]), F_(kvr[i][3]))};
  ST(u32x2, smem + K2_KV + (2048 + (tid & 127)) * 8) = (u32x2){pk_bf16(F_(kvr[8][0]), F_(kvr[8][1])), pk_bf16(F_(kvr[8][2]), F_(kvr[8][3]))};
  STAMP(3);
  f32x16 acc[2];
#pragma unroll
  for (int i = 0; i < 16; ++i) { acc[0][i] = 0.f; acc[1][i] = 0.f; }
  f32x16 cinit = acc[0];
  cinit[8] = EPS;
  lptr kva = smem + K2_KV + 16 * kh * 544 + min(l31, 16) * 32 + hh * 16;
  const int blk = (lane >> 4) & 1, q4 = (lane & 15) >> 2, p4 = lane & 3;
  const int sw = ((q4 >> 1) & 1) << 6;
  lptr wa0 = smem + K2_WO + (256 * kh + 4 * hh + q4) * 128 + blk * 32 + p4 * 8 + sw;
  lptr wa1 = smem + K2_WO + (256 * kh + 4 * hh + q4) * 128 + blk * 32 + p4 * 8 + (64 ^ sw);
  float gt;
  {
    f32x16 d[2]; s16x8 kf[16], wf[2][2]; u32x4 of[2];
#define OMM(i) do { d[(i) & 1] = MFMA32(kf[i], __builtin_bit_cast(s16x8, pf[i]), cinit); asm volatile("" : "+v"(d[(i) & 1])); } while (0)
#define NORM(i) do { const f32x16& dd = d[(i) & 1]; const float inv = __builtin_amdgcn_rcpf(dd[8]); \
      of[(i) & 1] = (u32x4){pk_bf16(dd[0] * inv, dd[1] * inv), pk_bf16(dd[2] * inv, dd[3] * inv), pk_bf16(dd[4] * inv, dd[5] * inv), pk_bf16(dd[6] * inv, dd[7] * inv)}; \
      asm volatile("" : "+v"(of[(i) & 1])); } while (0)
#define LDW(i) do { wf[(i) & 1][0] = cat8(tr_read(wa0 + (i) * 2048), tr_read(wa0 + (i) * 2048 + 1024)); \
      wf[(i) & 1][1] = cat8(tr_read(wa1 + (i) * 2048), tr_read(wa1 + (i) * 2048 + 1024)); } while (0)
#define AMM(i) do { acc[0] = MFMA32(wf[(i) & 1][0], __builtin_bit_cast(s16x8, of[(i) & 1]), acc[0]); \
      acc[1] = MFMA32(wf[(i) & 1][1], __builtin_bit_cast(s16x8, of[(i) & 1]), acc[1]); asm volatile("" : "+v"(acc[0]), "+v"(acc[1])); } while (0)
    WAIT_VM(8); LBAR();
    STAMP(4);
#pragma unroll
    for (int i = 0; i < 16; ++i) kf[i] = LD(s16x8, kva + i * 544);
    LDW(0);
    SCHED(); CHUNK(2); SCHED();
    OMM(0); OMM(1); SCHED(); NORM(0); SCHED();
#pragma unroll
    for (int i = 0; i < 16; ++i) {
      if (i == 3) { WAIT_VM(8); RAW_BAR(); SCHED(); CHUNK(3);
        blds16(rws, smem + K2_BT + 16 * w * 128, sr * 4096 + sc, (int)WS_BT + (64 * ct + 16 * w) * 4096 + 128 * rt);
        blds16(rws, smem + K2_BT + (16 * w + 8) * 128, sr * 4096 + sc, (int)WS_BT + (64 * ct + 16 * w + 8) * 4096 + 128 * rt);
        gt = u2f(__builtin_amdgcn_raw_buffer_load_b32(rws, l31 * 4, (int)WS_G + (64 * rt + 32 * th) * 4, 0)); }
      if (i == 7) { WAIT_VM(11); RAW_BAR(); }
      if (i == 11) { WAIT_VM(3); RAW_BAR(); }
      SCHED();
      if (i + 1 < 16) LDW(i + 1);
      SCHED();
      AMM(i);
      if (i + 2 < 16) OMM(i + 2);
      SCHED();
      if (i + 1 < 16) NORM(i + 1);
      SCHED();
    }
#undef OMM
#undef NORM
#undef LDW
#undef AMM
#undef CHUNK
  }
  WAIT_VM(0);
  STAMP(6);
  lptr ex = smem + K2_EX + w * 4096 + lane * 16;
  f32x16 keep;
  if (kh) {
#pragma unroll
    for (int i = 0; i < 4; ++i) ST(f32x4, ex + 1024 * i) = (f32x4){acc[0][4 * i], acc[0][4 * i + 1], acc[0][4 * i + 2], acc[0][4 * i + 3]};
    keep = acc[1];
  } else {
#pragma unroll
    for (int i = 0; i < 4; ++i) ST(f32x4, ex + 1024 * i) = (f32x4){acc[1][4 * i], acc[1][4 * i + 1], acc[1][4 * i + 2], acc[1][4 * i + 3]};
    keep = acc[0];
  }
  LBAR();
  lptr ei = smem + K2_EX + (w ^ 2) * 4096 + lane * 16;
  const float g1 = rbf(1.f - gt);
  lptr ba = smem + K2_BT + (32 * kh + 4 * hh + q4) * 128 + (((th * 64) ^ sw) + blk * 32 + p4 * 8);
  u16* op = out + (size_t)(64 * rt + 32 * th + l31) * DM + 64 * ct + 32 * kh + 8 * hh;
  u32x2 res[4];
#pragma unroll
  for (int i = 0; i < 4; ++i) {
    const f32x4 o = LD(f32x4, ei + 1024 * i);
    const u32x2 bb = __builtin_bit_cast(u32x2, tr_read(ba + i * 1024));
    const float r0 = gt * (keep[4 * i] + o[0]) + g1 * bf_lo(bb.x), r1 = gt * (keep[4 * i + 1] + o[1]) + g1 * bf_hi(bb.x);
    const float r2 = gt * (keep[4 * i + 2] + o[2]) + g1 * bf_lo(bb.y), r3 = gt * (keep[4 * i + 3] + o[3]) + g1 * bf_hi(bb.y);
    res[i] = (u32x2){pk_bf16(r0, r1), pk_bf16(r2, r3)};
  }
#pragma unroll
  for (int i = 0; i < 2; ++i) {
    const auto sx = __builtin_amdgcn_permlane32_swap(res[2 * i].x, res[2 * i + 1].x, false, false);
    const auto sy = __builtin_amdgcn_permlane32_swap(res[2 * i].y, res[2 * i + 1].y, false, false);
    *(u32x4*)(op + 16 * i) = (u32x4){sx[0], sy[0], sx[1], sy[1]};
  }
  STAMP(9);
  STAMP_FLUSH(1, ws);
}

__global__ __launch_bounds__(256, 2) void k2(const u16* __restrict__ wo, const char* __restrict__ ws, u16* __restrict__ out) {
  extern __shared__ __attribute__((aligned(16))) char smem_[];
  out_role(wo, ws, out, (lptr)smem_);
}

extern "C" void kernel_launch(void* const* d_in, const int* in_sizes, int n_in, void* d_out, int out_size, void* d_ws,
                              size_t ws_size, hipStream_t stream) {
  (void)in_sizes; (void)n_in; (void)out_size; (void)ws_size;
  (void)hipFuncSetAttribute((const void*)k1, hipFuncAttributeMaxDynamicSharedMemorySize, 163840);
  (void)hipFuncSetAttribute((const void*)k2, hipFuncAttributeMaxDynamicSharedMemorySize, K2_LDS);
  k1<<<256, 512, 163840, stream>>>((const u16*)d_in[0], (const u16*)d_in[1], (const u16*)d_in[4], (const u16*)d_in[5],
                                   (const u16*)d_in[6], (const u16*)d_in[7], (const u16*)d_in[8], (const u16*)d_in[9],
                                   (const u16*)d_in[10], (char*)d_ws);
  k2<<<256, 256, K2_LDS, stream>>>((const u16*)d_in[2], (const char*)d_ws, (u16*)d_out);
}
```

```cpp
#include <hip/hip_runtime.h>
#include <hip/hip_bf16.h>
#include <stdint.h>

typedef unsigned int u32;
typedef unsigned short u16;
typedef __attribute__((ext_vector_type(8))) short s16x8;
typedef __attribute__((ext_vector_type(4))) short s16x4;
typedef __attribute__((ext_vector_type(8))) _Float16 h16x8;
typedef __attribute__((ext_vector_type(2))) _Float16 h16x2;
typedef __attribute__((ext_vector_type(2))) __bf16 b16x2;
typedef __attribute__((ext_vector_type(2))) float f32x2;
typedef __attribute__((ext_vector_type(4))) float f32x4;
typedef __attribute__((ext_vector_type(16))) float f32x16;
typedef __attribute__((ext_vector_type(2))) u32 u32x2;
typedef __attribute__((ext_vector_type(4))) u32 u32x4;
#define LDS_AS __attribute__((address_space(3)))
#define DI __device__ __forceinline__

static constexpr int SEQ = 2048, DM = 512;
static constexpr size_t WS_KV = 0;
static constexpr size_t WS_G = 65536;
static constexpr size_t WS_PQ = 1u << 20;
static constexpr size_t WS_BT = 4u << 20;
static constexpr size_t WS_SYNC = 49152;
typedef unsigned long long u64;
typedef __attribute__((address_space(1))) u64 gu64;
typedef __attribute__((address_space(1))) u32 gu32;
#define RLX_AGENT __ATOMIC_RELAXED, __HIP_MEMORY_SCOPE_AGENT
__device__ __forceinline__ u64 next_tag(u64 e) { return e * 6364136223846793005ull + 1442695040888963407ull; }

#define STAMP_ARG
#define STAMP_PASS
#define STAMP_DECL do {} while (0)
#define STAMP(i) do {} while (0)
#define STAMP_FLUSH(wsp) do {} while (0)
#define ST_AUX 16
#define WAIT_VM(n) asm volatile("s_waitcnt vmcnt(" #n ")" ::: "memory")
#define WAIT_L0() __builtin_amdgcn_s_waitcnt(0xC07F)
#define RAW_BAR() __builtin_amdgcn_s_barrier()
#define SCHED() __builtin_amdgcn_sched_barrier(0)
#define LBAR() do { WAIT_L0(); asm volatile("" ::: "memory"); RAW_BAR(); asm volatile("" ::: "memory"); } while (0)
typedef LDS_AS char* lptr;
#define LD(T, p) (*(const LDS_AS T*)(p))
#define ST(T, p) (*(LDS_AS T*)(p))

DI u32 pk_bf16(float a, float b) { return __builtin_bit_cast(u32, __builtin_convertvector((f32x2){a, b}, b16x2)); }
DI u32 pk_f16(f32x2 v) { return __builtin_bit_cast(u32, __builtin_convertvector(v, h16x2)); }
DI float bf_lo(u32 v) { return __builtin_bit_cast(float, v << 16); }
DI float bf_hi(u32 v) { return __builtin_bit_cast(float, v & 0xffff0000u); }
DI float u2f(u32 v) { return __builtin_bit_cast(float, v); }
DI b16x2 u2b(u32 v) { return __builtin_bit_cast(b16x2, v); }
DI float rbf(float v) { return bf_lo(pk_bf16(v, v)); }
static constexpr float LOG2E = 1.44269504088896341f, EPS = 1e-6f;
DI float phi_f(float t) {
  float lo, hi;
  asm("v_min_f32 %0, %1, %2" : "=v"(lo) : "s"(EPS), "v"(t));
  asm("v_max_f32 %0, %1, %2" : "=v"(hi) : "s"(EPS), "v"(t));
  return hi + __builtin_amdgcn_exp2f(__builtin_fmaf(lo, LOG2E, -EPS * LOG2E));
}
DI float exp_f(float v) {
  const float p = v * LOG2E;
  const float r = __builtin_fmaf(v, 1.925963033500011e-8f, __builtin_fmaf(v, LOG2E, -p));
  return __builtin_amdgcn_exp2f(p) * __builtin_fmaf(r, 0.6931471805599453f, 1.f);
}
#define DPP_F(v, ctrl, rmask, bc) __builtin_bit_cast(float, __builtin_amdgcn_update_dpp(0, __builtin_bit_cast(int, v), ctrl, rmask, 0xf, bc))
DI float wave_sum(float r) {
  r += DPP_F(r, 0x111, 0xf, true); r += DPP_F(r, 0x112, 0xf, true); r += DPP_F(r, 0x114, 0xf, true); r += DPP_F(r, 0x118, 0xf, true);
  r += DPP_F(r, 0x142, 0xa, false); r += DPP_F(r, 0x143, 0xc, false);
  return r;
}
DI u32 wave_umax(u32 r) {
#define DPP_U(v, ctrl, rmask, bc) (u32)__builtin_amdgcn_update_dpp(0, (int)(v), ctrl, rmask, 0xf, bc)
  r = max(r, DPP_U(r, 0x111, 0xf, true)); r = max(r, DPP_U(r, 0x112, 0xf, true)); r = max(r, DPP_U(r, 0x114, 0xf, true));
  r = max(r, DPP_U(r, 0x118, 0xf, true)); r = max(r, DPP_U(r, 0x142, 0xa, false)); r = max(r, DPP_U(r, 0x143, 0xc, false));
  return (u32)__builtin_amdgcn_readlane((int)r, 63);
}
DI s16x4 tr_read(lptr p) { return __builtin_amdgcn_ds_read_tr16_b64_v4i16((LDS_AS s16x4*)p); }
DI s16x8 cat8(s16x4 a, s16x4 b) { return __builtin_shufflevector(a, b, 0, 1, 2, 3, 4, 5, 6, 7); }
typedef __amdgpu_buffer_rsrc_t rsrc_t;
DI rsrc_t mk_rsrc(const void* p, int bytes) { return __builtin_amdgcn_make_buffer_rsrc((void*)p, 0, bytes, 0x00020000); }
DI void blds16(rsrc_t r, lptr l, int voff, int soff) { __builtin_amdgcn_raw_ptr_buffer_load_lds(r, (LDS_AS void*)l, 16, voff, soff, 0, 0); }
DI f32x2 cmul(f32x2 a, f32x2 w) {
  f32x2 t, r;
  asm("v_pk_mul_f32 %0, %1, %2 op_sel:[1,1] op_sel_hi:[1,0]" : "=v"(t) : "v"(a), "v"(w));
  asm("v_pk_fma_f32 %0, %1, %2, %3 op_sel_hi:[0,1,1] neg_lo:[0,0,1]" : "=v"(r) : "v"(a), "v"(w), "v"(t));
  return r;
}
DI f32x2 cexp_p(float alpha, float theta, float p) {
  float m = __builtin_amdgcn_exp2f(alpha * p);
  float f = __builtin_amdgcn_fractf(theta * p);
  return (f32x2){m * __builtin_amdgcn_cosf(f), m * __builtin_amdgcn_sinf(f)};
}
#define MFMA16(a, b, c) __builtin_amdgcn_mfma_f32_16x16x32_bf16((a), (b), (c), 0, 0, 0)
#define MFMA16H(a, b, c) __builtin_amdgcn_mfma_f32_16x16x32_f16((a), (b), (c), 0, 0, 0)
#define MFMA32(a, b, c) __builtin_amdgcn_mfma_f32_32x32x16_bf16((a), (b), (c), 0, 0, 0)

static constexpr int STAGE = 40960;
static constexpr int P2_WO = 0, P2_KV = 65536, P2_BT = P2_KV + 18432, P2_EX = P2_BT + 8192;

DI void fetch_wo(const u16* __restrict__ wo, int ct, int w, int lane, lptr smem) {
  const rsrc_t rwo = mk_rsrc(wo, DM * DM * 2);
  const int sr = lane >> 3, sp = lane & 7, sc = (sp ^ (((sr >> 1) & 1) << 2)) << 4;
#pragma unroll
  for (int i = 0; i < 8; ++i) blds16(rwo, smem + P2_WO + (64 * w + 8 * i) * 128, sr * 1024 + sc, (64 * w + 8 * i) * 1024 + 128 * ct);
}

DI void gemm_role(const u16* __restrict__ x, const u16* __restrict__ wqkv, const u16* __restrict__ wo, char* __restrict__ ws, int tt, int hg,
                  int ct, u64 epoch, lptr smem STAMP_ARG) {
  const int tid = threadIdx.x, lane = tid & 63;
  const int w = __builtin_amdgcn_readfirstlane(tid >> 6);
  const int wm = w >> 2, wh = w & 3, g = lane >> 4, l15 = lane & 15;
  const int sr = lane >> 3, sp = lane & 7;
  const rsrc_t rx = mk_rsrc(x, SEQ * DM * 2), rw = mk_rsrc(wqkv, DM * 1536 * 2);
  const int xv = sr * 1024 + ((sp ^ sr) << 4), xs0 = (tt * 128 + 16 * w) * 1024;
  const int fw = ((sr >> 1) & 1) | ((w & 1) << 1);
  const int wv = sr * 3072 + ((sp ^ (fw << 1)) << 4), ws0 = 8 * w * 3072 + hg * 128;
#define ISS(s, i) do { lptr sb = smem + ((s) & 3) * STAGE; \
    if ((i) < 2) blds16(rx, sb + w * 2048 + (i) * 1024, xv, xs0 + (s) * 128 + (i) * 8192); \
    else blds16(rw, sb + 16384 + ((i) - 2) * 8192 + w * 1024, wv, ws0 + (s) * 64 * 3072 + ((i) - 2) * 1024); } while (0)
#define ISSUE(s) do { ISS(s, 0); ISS(s, 1); ISS(s, 2); ISS(s, 3); ISS(s, 4); } while (0)
  gu64* sync_z = (gu64*)(ws + WS_SYNC + 128 + hg * 128);
  ISSUE(0); ISSUE(1);
  const int xo = (64 * wm + l15) * 128 + ((g ^ (lane & 7)) << 4);
  const int q4 = l15 >> 2, p4 = lane & 3;
  const int wo_ = 16384 + (8 * g + q4) * 128 + ((wh ^ (((q4 >> 1) & 1) | ((g & 1) << 1))) << 5) + 8 * p4;
  f32x4 aq[4], ak[4], av[4];
#pragma unroll
  for (int i = 0; i < 4; ++i) { aq[i] = (f32x4){EPS, EPS, EPS, EPS}; ak[i] = aq[i]; av[i] = (f32x4){0, 0, 0, 0}; }
  s16x8 xa[4], wa[3], xb[4], wb[3];
#define LOADF(xf, wf, s, ks) do { lptr sb = smem + ((s) & 3) * STAGE; \
    _Pragma("unroll") for (int st = 0; st < 3; ++st) \
      wf[st] = cat8(tr_read(sb + wo_ + st * 8192 + (ks) * 4096), tr_read(sb + wo_ + st * 8192 + (ks) * 4096 + 512)); \
    _Pragma("unroll") for (int mt = 0; mt < 4; ++mt) xf[mt] = LD(s16x8, sb + ((xo ^ ((ks) * 64)) + mt * 2048)); } while (0)
#define MM3(xf, wf, mt) do { aq[mt] = MFMA16(wf[0], xf[mt], aq[mt]); ak[mt] = MFMA16(xf[mt], wf[1], ak[mt]); av[mt] = MFMA16(xf[mt], wf[2], av[mt]); \
    asm volatile("" : "+v"(aq[mt]), "+v"(ak[mt]), "+v"(av[mt])); } while (0)
#define MMI(xf, wf, is) do { SCHED(); MM3(xf, wf, 0); SCHED(); if ((is) >= 0) { ISS(is, 0); ISS(is, 1); } SCHED(); MM3(xf, wf, 1); SCHED(); \
    if ((is) >= 0) { ISS(is, 2); ISS(is, 3); } SCHED(); MM3(xf, wf, 2); SCHED(); if ((is) >= 0) ISS(is, 4); SCHED(); MM3(xf, wf, 3); SCHED(); } while (0)
#define STEP(s, vm, i1, i2) do { WAIT_L0(); LOADF(xb, wb, s, 1); MMI(xa, wa, i1); \
    WAIT_L0(); WAIT_VM(vm); RAW_BAR(); LOADF(xa, wa, (s) + 1, 0); MMI(xb, wb, i2); } while (0)
  STAMP(1);
  WAIT_VM(5); RAW_BAR();
  STAMP(2);
  LOADF(xa, wa, 0, 0);
  STEP(0, 5, 2, 3);
  STAMP(3);
  STEP(1, 10, 4, 5);
  STEP(2, 10, -1, 6);
  STEP(3, 10, -1, 7);
  STAMP(4);
  STEP(4, 10, -1, -1);
  STEP(5, 5, -1, -1);
  u64 ready = __hip_atomic_load(sync_z, RLX_AGENT);
  STEP(6, 0, -1, -1);
  fetch_wo(wo, ct, w, lane, smem);
  STAMP(5);
  WAIT_L0(); LOADF(xb, wb, 7, 1); MMI(xa, wa, -1); WAIT_L0(); MMI(xb, wb, -1);
  STAMP(6);
#undef ISS
#undef ISSUE
#undef LOADF
#undef MM3
#undef MMI
#undef STEP
  const int h = 4 * hg + wh;
  f32x4 kvt = {0, 0, 0, 0}, zz = {0, 0, 0, 0};
  const u32x4 ones_u = {0x3f803f80u, 0x3f803f80u, 0x3f803f80u, 0x3f803f80u};
#pragma unroll
  for (int pr = 0; pr < 2; ++pr) {
    u32x4 pk, vv;
#pragma unroll
    for (int m = 0; m < 2; ++m) {
      const f32x4 kk = ak[2 * pr + m], v4 = av[2 * pr + m];
      pk[2 * m] = pk_bf16(phi_f(kk[0]), phi_f(kk[1])); pk[2 * m + 1] = pk_bf16(phi_f(kk[2]), phi_f(kk[3]));
      vv[2 * m] = pk_bf16(v4[0], v4[1]); vv[2 * m + 1] = pk_bf16(v4[2], v4[3]);
    }
    kvt = MFMA16(__builtin_bit_cast(s16x8, vv), __builtin_bit_cast(s16x8, pk), kvt);
    zz = MFMA16(__builtin_bit_cast(s16x8, ones_u), __builtin_bit_cast(s16x8, pk), zz);
  }
  lptr red = smem + 66560;
#pragma unroll
  for (int r = 0; r < 4; ++r) ST(float, red + (wm * 4 + wh) * 1088 + l15 * 4 + (4 * g + r) * 64) = kvt[r];
  if (g == 0) ST(float, red + (wm * 4 + wh) * 1088 + l15 * 4 + 1024) = zz[0];
  const u64 tag = next_tag(epoch);
  for (int spin = 0; ready != tag && spin < (1 << 22); ++spin) { __builtin_amdgcn_s_sleep(2); ready = __hip_atomic_load(sync_z, RLX_AGENT); }
  STAMP(7);
  LBAR();
  STAMP(8);
  float* kvacc = (float*)(ws + WS_KV) + hg * 1088;
  atomicAdd(kvacc + tid, LD(float, red + tid * 4) + LD(float, red + 4352 + tid * 4));
  atomicAdd(kvacc + 512 + tid, LD(float, red + 2048 + tid * 4) + LD(float, red + 4352 + 2048 + tid * 4));
  if (tid < 64) atomicAdd(kvacc + 1024 + tid, LD(float, red + 4096 + tid * 4) + LD(float, red + 4352 + 4096 + tid * 4));
  const rsrc_t rws = mk_rsrc(ws, 6 << 20);
  const int pqv = (g >> 1) * 512 + l15 * 16 + (g & 1) * 256, pqs = (int)WS_PQ + ((tt * 4 + 2 * wm) * 32 + h) * 1024;
#pragma unroll
  for (int pr = 0; pr < 2; ++pr) {
    const f32x4 q0 = aq[2 * pr], q1 = aq[2 * pr + 1];
    const auto s0 = __builtin_amdgcn_permlane16_swap(pk_bf16(phi_f(q0[0]), phi_f(q0[1])), pk_bf16(phi_f(q1[0]), phi_f(q1[1])), false, false);
    const auto s1 = __builtin_amdgcn_permlane16_swap(pk_bf16(phi_f(q0[2]), phi_f(q0[3])), pk_bf16(phi_f(q1[2]), phi_f(q1[3])), false, false);
    __builtin_amdgcn_raw_buffer_store_b128((u32x4){s0[0], s1[0], s0[1], s1[1]}, rws, pqv, pqs + pr * 32768, ST_AUX);
  }
  STAMP(9);
  WAIT_VM(0);
}

static constexpr int TB_ROW = 544, TB_CH = 63 * TB_ROW;
static constexpr int U_OFF = 4 * TB_CH, U_CH = (2048 + 240) * 2;
static constexpr int KR_CP = 4160, KR_CH = 2 * KR_CP;

DI void conv_role(const u16* __restrict__ x, const u16* __restrict__ lar, const u16* __restrict__ aim, const u16* __restrict__ cre,
                  const u16* __restrict__ cim, const u16* __restrict__ ldt, const u16* __restrict__ dsk, const u16* __restrict__ gw,
                  const u16* __restrict__ wo, char* __restrict__ ws, int cg, int ct, u64 epoch, lptr smem STAMP_ARG) {
  const int tid = threadIdx.x, lane = tid & 63;
  const int w = __builtin_amdgcn_readfirstlane(tid >> 6);
  const int ch = w & 3, half = w >> 2, g = lane >> 4, l15 = lane & 15;
  const int c = 4 * cg + ch;
  const rsrc_t rx = mk_rsrc(x, SEQ * DM * 2);
  const float dsk_f = bf_lo(dsk[c]);
  const bool clear = (cg & 15) == 0;
  u32x4 gx[4], gwv;
  if (w < 4) {
    const int cw = 4 * cg + w;
    const u32 lar2 = *(const u32*)(lar + cw * 128 + 2 * lane), aim2 = *(const u32*)(aim + cw * 128 + 2 * lane);
    const u32 cre2 = *(const u32*)(cre + cw * 128 + 2 * lane), cim2 = *(const u32*)(cim + cw * 128 + 2 * lane);
    const u32 ldt_u = ldt[cw];
    SCHED(); RAW_BAR(); SCHED();
    const float dt = rbf(exp_f(bf_lo(ldt_u)));
    float al[2], th[2];
    al[0] = -rbf(exp_f(bf_lo(lar2))) * dt * LOG2E; al[1] = -rbf(exp_f(bf_hi(lar2))) * dt * LOG2E;
    th[0] = bf_lo(aim2) * dt * 0.15915494309189535f; th[1] = bf_hi(aim2) * dt * 0.15915494309189535f;
    STAMP(1);
    u32 ce = wave_umax(max(max(cre2 & 0x7f80u, (cre2 >> 16) & 0x7f80u), max(cim2 & 0x7f80u, (cim2 >> 16) & 0x7f80u))) >> 7;
    ce = ce < 1 ? 1 : (ce > 253 ? 253 : ce);
    const float cinv = __builtin_bit_cast(float, (254u - ce) << 23);
    const float kscale = 2.f * dt * __builtin_bit_cast(float, ce << 23);
    lptr tb = smem + w * TB_CH + lane * 8;
#define PUT(e, v) ST(u32x2, tb + (e) * TB_ROW) = (u32x2){pk_f16(v[0]), pk_f16(v[1])}
#define CMUL2(d, a, b) do { d[0] = cmul(a[0], b[0]); d[1] = cmul(a[1], b[1]); } while (0)
#define CONJ(v) ((f32x2){(v).x, -(v).y})
    {
      f32x2 y[17][2], wB[2], w16[2], xa[2], xb[2], a[2], t[2];
#pragma unroll
      for (int m = 0; m < 2; ++m) {
        y[1][m] = CONJ(cexp_p(al[m], th[m], 1.f)); w16[m] = cexp_p(al[m], th[m], 16.f); wB[m] = CONJ(cexp_p(al[m], th[m], 256.f));
        y[16][m] = CONJ(w16[m]);
      }
      xa[0] = (f32x2){bf_lo(cre2) * cinv, bf_lo(cim2) * cinv}; xa[1] = (f32x2){bf_hi(cre2) * cinv, bf_hi(cim2) * cinv};
      ST(u32x2, tb + 16 * TB_ROW) = (u32x2){0x3c00u, 0x3c00u};
      PUT(0, xa); PUT(17, y[1]); PUT(32, wB);
      f32x2 w32[2];
      CMUL2(xb, xa, w16); CMUL2(w32, w16, w16); CMUL2(y[2], y[1], y[1]); CMUL2(a, wB, wB);
      PUT(1, xb); PUT(18, y[2]); PUT(41, a);
      f32x2 aS[6][2];
      aS[0][0] = a[0]; aS[0][1] = a[1];
#pragma unroll
      for (int i = 1; i < 8; ++i) {
        CMUL2(xa, xa, w32); CMUL2(xb, xb, w32);
        CMUL2(y[2 * i + 1], y[2 * i - 1], y[2]);
        if (i < 7) CMUL2(y[2 * i + 2], y[2 * i], y[2]);
        if (i < 6) CMUL2(aS[i], aS[i - 1], wB);
        PUT(2 * i, xa); PUT(2 * i + 1, xb); PUT(17 + 2 * i, y[2 * i + 1]);
        if (i < 7) PUT(18 + 2 * i, y[2 * i + 2]);
        if (i == 1) PUT(46, aS[1]);
        if (i >= 2 && i < 6) PUT(51 + 3 * (i - 2), aS[i]);
        CMUL2(t, wB, y[2 * i]); PUT(32 + i, t);
      }
      CMUL2(t, wB, y[16]); PUT(40, t);
#pragma unroll
      for (int s = 0; s < 2; ++s)
#pragma unroll
        for (int m = 1; m < 5; ++m) { CMUL2(t, aS[s], y[4 * m]); PUT(41 + 5 * s + m, t); }
#pragma unroll
      for (int s = 2; s < 6; ++s)
#pragma unroll
        for (int m = 1; m < 3; ++m) { CMUL2(t, aS[s], y[8 * m]); PUT(51 + 3 * (s - 2) + m, t); }
    }
#undef PUT
    STAMP(2);
    f32x4 kd[4] = {{0, 0, 0, 0}, {0, 0, 0, 0}, {0, 0, 0, 0}, {0, 0, 0, 0}};
    {
      const int gq = l15 >> 2, r = min(l15 & 3, 2);
      lptr tch = smem + w * TB_CH + 16 * g;
      lptr pb = tch + l15 * TB_ROW, pa0 = pb + 16 * TB_ROW, pa1 = tch + (32 + ((0x8766654443222100ull >> (4 * l15)) & 15)) * TB_ROW;
      lptr pa2 = tch + (41 + 5 * (gq >> 1) + 2 * (gq & 1) + r) * TB_ROW, pa3 = tch + (51 + 3 * gq + r) * TB_ROW;
      h16x8 f[2][4][5];
#define LDK(bf, q) do { _Pragma("unroll") for (int k2 = 0; k2 < 4; ++k2) { const int o = 64 * (4 * (q) + k2); \
        f[bf][k2][0] = LD(h16x8, pa0 + o); f[bf][k2][1] = LD(h16x8, pa1 + o); f[bf][k2][2] = LD(h16x8, pa2 + o); \
        f[bf][k2][3] = LD(h16x8, pa3 + o); f[bf][k2][4] = LD(h16x8, pb + o); } } while (0)
#define MMK(bf) do { _Pragma("unroll") for (int k2 = 0; k2 < 4; ++k2) _Pragma("unroll") for (int e = 0; e < 4; ++e) \
        kd[e] = MFMA16H(f[bf][k2][e], f[bf][k2][4], kd[e]); } while (0)
      SCHED(); LDK(0, 0); SCHED(); LDK(1, 1); SCHED(); MMK(0); MMK(1);
#undef LDK
#undef MMK
    }
    STAMP(3);
    lptr kr = smem + w * TB_CH;
    {
      const f32x4 k0 = kd[0] * kscale, k1 = kd[1] * kscale;
      if (lane < 8) ST(u32x4, kr + (lane >> 2) * KR_CP + 4096 + 16 * (lane & 3)) = (u32x4){0u, 0u, 0u, 0u};
      const int y = 2044 - 16 * l15 - 4 * g;
      const u32 p32 = pk_bf16(k0[3], k0[2]), p10 = pk_bf16(k0[1], k0[0]);
      ST(u32x2, kr + 2 * y) = (u32x2){p32, p10};
      ST(u16, kr + KR_CP + 2 * y + 2) = (u16)p32;
      ST(u32, kr + KR_CP + 2 * y + 4) = pk_bf16(k0[2], k0[1]);
      ST(u16, kr + KR_CP + 2 * y + 8) = (u16)(p10 >> 16);
      const u32 q10 = pk_bf16(k1[1], k1[0]);
      ST(u32x2, kr + 2 * (y - 256)) = (u32x2){pk_bf16(k1[3], k1[2]), q10};
      ST(u32x2, kr + KR_CP + 2 * (y - 256)) = (u32x2){pk_bf16(k1[3], k1[3]), pk_bf16(k1[2], k1[1])};
      if (lane == 0) ST(u16, kr + KR_CP + 2 * 1792) = (u16)q10;
    }
    {
      const f32x4 k0 = kd[2] * kscale, k1 = kd[3] * kscale;
      const int y2 = 2040 - (512 + 256 * (g >> 1) + 16 * l15 + 8 * (g & 1));
      const u32 b2 = pk_bf16(k0[2], k0[2]), b1 = pk_bf16(k0[1], k0[1]), b0 = pk_bf16(k0[0], k0[0]);
      ST(u32x4, kr + 2 * y2) = (u32x4){b2, b1, pk_bf16(k0[1], k0[0]), b0};
      ST(u32x4, kr + KR_CP + 2 * y2) = (u32x4){b2, pk_bf16(k0[2], k0[1]), b1, b0};
      const int y3 = 2032 - (1024 + 256 * g + 16 * l15);
      const u32 a2 = pk_bf16(k1[2], k1[2]), a1 = pk_bf16(k1[1], k1[1]), a0 = pk_bf16(k1[0], k1[0]);
      ST(u32x4, kr + 2 * y3) = (u32x4){a2, a2, a1, a1};
      ST(u32x4, kr + 2 * y3 + 16) = (u32x4){a1, pk_bf16(k1[1], k1[0]), a0, a0};
      ST(u32x4, kr + KR_CP + 2 * y3) = (u32x4){a2, a2, pk_bf16(k1[2], k1[1]), a1};
      ST(u32x4, kr + KR_CP + 2 * y3 + 16) = (u32x4){a1, a1, a0, a0};
    }
#pragma unroll
    for (int r = 0; r < 4; ++r) gx[r] = __builtin_amdgcn_raw_buffer_load_b128(rx, lane * 16 + 1024 * r, (16 * cg + 4 * w) * 1024, 0);
    gwv = *(const u32x4*)(gw + 8 * lane);
    STAMP(4);
  } else {
    const int t2 = tid - 256;
    if (clear) {
      const rsrc_t rk = mk_rsrc(ws, 65536);
      const u32x4 z4 = {0u, 0u, 0u, 0u};
      __builtin_amdgcn_raw_buffer_store_b128(z4, rk, t2 * 16, (int)WS_KV + (cg >> 4) * 4352, 16);
      if (t2 < 16) __builtin_amdgcn_raw_buffer_store_b128(z4, rk, t2 * 16, (int)WS_KV + (cg >> 4) * 4352 + 4096, 16);
    }
    SCHED(); RAW_BAR(); SCHED();
    u32x2 xr[8];
#pragma unroll
    for (int r = 0; r < 8; ++r) xr[r] = __builtin_amdgcn_raw_buffer_load_b64(rx, t2 * 4096 + (r & 3) * 1024, 8 * cg + (r >> 2) * (1024 * 1024), 0);
    STAMP(1);
    if (lane < 30) ST(u32x4, smem + U_OFF + (w - 4) * U_CH + lane * 16) = (u32x4){0u, 0u, 0u, 0u};
#pragma unroll
    for (int hf = 0; hf < 2; ++hf) {
      lptr ub = smem + U_OFF + 480 + t2 * 8 + hf * 2048;
      const u32x2 x0 = xr[4 * hf], x1 = xr[4 * hf + 1], x2 = xr[4 * hf + 2], x3 = xr[4 * hf + 3];
      ST(u32x2, ub + 0 * U_CH) = (u32x2){__builtin_amdgcn_perm(x1.x, x0.x, 0x05040100u), __builtin_amdgcn_perm(x3.x, x2.x, 0x05040100u)};
      ST(u32x2, ub + 1 * U_CH) = (u32x2){__builtin_amdgcn_perm(x1.x, x0.x, 0x07060302u), __builtin_amdgcn_perm(x3.x, x2.x, 0x07060302u)};
      ST(u32x2, ub + 2 * U_CH) = (u32x2){__builtin_amdgcn_perm(x1.y, x0.y, 0x05040100u), __builtin_amdgcn_perm(x3.y, x2.y, 0x05040100u)};
      ST(u32x2, ub + 3 * U_CH) = (u32x2){__builtin_amdgcn_perm(x1.y, x0.y, 0x07060302u), __builtin_amdgcn_perm(x3.y, x2.y, 0x07060302u)};
    }
    WAIT_VM(0);
    STAMP(4);
  }
  lptr ka[8];
  {
    const int yb = 2031 - l15 + 8 * g - 128 * half - 96 - 1792;
    lptr k7 = smem + ch * TB_CH + (yb & 1) * KR_CP + ((yb + 1) >> 1) * 4;
#pragma unroll
    for (int n = 0; n < 8; ++n) { ka[n] = k7 + 512 * (7 - n); asm volatile("" : "+v"(ka[n])); }
  }
  lptr ua = smem + U_OFF + ch * U_CH + (8 * g + 16 * l15 + 224 - 128 * half - 96) * 2;
  STAMP(6);
  LBAR();
  if (clear && tid == 256) __hip_atomic_store((gu64*)(ws + WS_SYNC + 128 + (cg >> 4) * 128), next_tag(epoch), RLX_AGENT);
  STAMP(7);
  f32x4 acc[8];
#pragma unroll
  for (int i = 0; i < 8; ++i) acc[i] = (f32x4){0, 0, 0, 0};
  {
    u32x4 A0[8], A1[8]; s16x8 B0[8], B1[8];
#define LOADC(A, B, rr) do { _Pragma("unroll") for (int n = 0; n < 8; ++n) { \
      lptr kp = ka[n] + 64 * (3 - (rr)); \
      A[n] = (u32x4){LD(u32, kp), LD(u32, kp + 4), LD(u32, kp + 8), LD(u32, kp + 12)}; \
      B[n] = LD(s16x8, ua + 64 * (3 - (rr)) + 512 * n); } } while (0)
#define MMC(A, B) do { _Pragma("unroll") for (int nu = 0; nu < 8; ++nu) _Pragma("unroll") for (int mu = 0; mu < 8 - nu; ++mu) \
      acc[nu + mu] = MFMA16(__builtin_bit_cast(s16x8, A[nu]), B[mu], acc[nu + mu]); } while (0)
#define PINC() do { asm volatile("" : "+v"(acc[0]), "+v"(acc[1]), "+v"(acc[2]), "+v"(acc[3]), "+v"(acc[4]), "+v"(acc[5]), "+v"(acc[6]), "+v"(acc[7])); SCHED(); } while (0)
#define MIXC() do { _Pragma("unroll") for (int q = 0; q < 12; ++q) { __builtin_amdgcn_sched_group_barrier(0x8, 3, 0); __builtin_amdgcn_sched_group_barrier(0x100, 2, 0); } } while (0)
    LOADC(A0, B0, 0);
    SCHED();
    WAIT_L0(); SCHED(); LOADC(A1, B1, 1); MMC(A0, B0); MIXC(); PINC();
    WAIT_L0(); SCHED(); LOADC(A0, B0, 2); MMC(A1, B1); MIXC(); PINC();
    WAIT_L0(); SCHED(); LOADC(A1, B1, 3); MMC(A0, B0); MIXC(); PINC();
    WAIT_L0(); SCHED(); MMC(A1, B1); PINC();
#undef LOADC
#undef MMC
  }
  STAMP(8);
  {
    lptr ex = smem + (2 + half) * TB_CH + KR_CH + ch * 4096 + lane * 16;
    f32x4 keep[4];
    if (half) {
#pragma unroll
      for (int i = 0; i < 4; ++i) { ST(f32x4, ex + 1024 * i) = acc[i]; keep[i] = acc[4 + i]; }
    } else {
#pragma unroll
      for (int i = 0; i < 4; ++i) { ST(f32x4, ex + 1024 * i) = acc[4 + i]; keep[i] = acc[i]; }
    }
    const int t0 = 1024 * half + 16 * l15 + 4 * g;
    const rsrc_t rws = mk_rsrc(ws, 6 << 20);
    lptr up = smem + U_OFF + ch * U_CH + 480 + 2 * t0;
#pragma unroll
    for (int i = 0; i < 4; ++i) {
      const u32x2 uu = LD(u32x2, up + 512 * i);
      keep[i] += (f32x4){bf_lo(uu.x), bf_hi(uu.x), bf_lo(uu.y), bf_hi(uu.y)} * dsk_f;
    }
    if (w < 4) {
      float sg[4];
#pragma unroll
      for (int r = 0; r < 4; ++r) {
        float s0 = 0.f;
#pragma unroll
        for (int i = 0; i < 4; ++i) s0 = __builtin_amdgcn_fdot2_f32_bf16(u2b(gx[r][i]), u2b(gwv[i]), s0, false);
        sg[r] = rbf(__builtin_amdgcn_rcpf(1.f + __builtin_amdgcn_exp2f(-LOG2E * rbf(wave_sum(s0)))));
      }
      if (lane == 63) __builtin_amdgcn_raw_buffer_store_b128(__builtin_bit_cast(u32x4, (f32x4){sg[0], sg[1], sg[2], sg[3]}), rws, 0, (int)WS_G + (16 * cg + 4 * w) * 4, ST_AUX);
    }
    LBAR();
    lptr ei = smem + (3 - half) * TB_CH + KR_CH + ch * 4096 + lane * 16;
    u32x2 yb[4];
#pragma unroll
    for (int i = 0; i < 4; ++i) {
      const f32x4 y = keep[i] + LD(f32x4, ei + 1024 * i);
      yb[i] = (u32x2){pk_bf16(y[0], y[1]), pk_bf16(y[2], y[3])};
    }
#pragma unroll
    for (int i = 0; i < 2; ++i) {
      const auto sx = __builtin_amdgcn_permlane16_swap(yb[2 * i].x, yb[2 * i + 1].x, false, false);
      const auto sy = __builtin_amdgcn_permlane16_swap(yb[2 * i].y, yb[2 * i + 1].y, false, false);
      __builtin_amdgcn_raw_buffer_store_b128((u32x4){sx[0], sy[0], sx[1], sy[1]}, rws, 2048 * half + 32 * l15 + 16 * (g >> 1) + 512 * (g & 1) + 1024 * i,
                                             (int)WS_BT + c * 4096, ST_AUX);
    }
  }
  SCHED();
  fetch_wo(wo, ct, w, lane, smem);
  STAMP(9);
  WAIT_VM(8);
}

DI void out_role(const char* __restrict__ ws, u16* __restrict__ out, int rt, int ct, lptr smem STAMP_ARG) {
  const int tid = threadIdx.x, lane = tid & 63;
  const int w = __builtin_amdgcn_readfirstlane(tid >> 6);
  const int th = w & 1, kq = w >> 1, hh = lane >> 5, l31 = lane & 31;
  const rsrc_t rws = mk_rsrc(ws, 6 << 20);
  u32x4 kvr[5];
#pragma unroll
  for (int i = 0; i < 4; ++i) kvr[i] = __builtin_amdgcn_raw_buffer_load_b128(rws, tid * 16, (int)WS_KV + 8192 * i, 16);
  kvr[4] = __builtin_amdgcn_raw_buffer_load_b128(rws, (tid & 127) * 16, (int)WS_KV + 32768, 16);
  u32x4 pf[8];
#pragma unroll
  for (int i = 0; i < 8; ++i) pf[i] = __builtin_amdgcn_raw_buffer_load_b128(rws, lane * 16, (int)WS_PQ + ((2 * rt + th) * 32 + 8 * kq + i) * 1024, 16);
  const int sr = lane >> 3, sp = lane & 7;
  const u32x4 btr = __builtin_amdgcn_raw_buffer_load_b128(rws, sr * 4096 + sp * 16, (int)WS_BT + (64 * ct + 8 * w) * 4096 + 128 * rt, 16);
  const float gt = u2f(__builtin_amdgcn_raw_buffer_load_b32(rws, l31 * 4, (int)WS_G + (64 * rt + 32 * th) * 4, 16));
  STAMP(12);
#pragma unroll
  for (int i = 0; i < 4; ++i) ST(u32x2, smem + P2_KV + (tid + 512 * i) * 8) = (u32x2){pk_bf16(u2f(kvr[i][0]), u2f(kvr[i][1])), pk_bf16(u2f(kvr[i][2]), u2f(kvr[i][3]))};
  ST(u32x2, smem + P2_KV + (2048 + (tid & 127)) * 8) = (u32x2){pk_bf16(u2f(kvr[4][0]), u2f(kvr[4][1])), pk_bf16(u2f(kvr[4][2]), u2f(kvr[4][3]))};
  f32x16 acc[2];
#pragma unroll
  for (int i = 0; i < 16; ++i) { acc[0][i] = 0.f; acc[1][i] = 0.f; }
  f32x16 cinit = acc[0];
  cinit[8] = EPS;
  lptr kva = smem + P2_KV + 8 * kq * 544 + min(l31, 16) * 32 + hh * 16;
  const int blk = (lane >> 4) & 1, q4 = (lane & 15) >> 2, p4 = lane & 3;
  const int sw = ((q4 >> 1) & 1) << 6;
  lptr wa0 = smem + P2_WO + (128 * kq + 4 * hh + q4) * 128 + blk * 32 + p4 * 8 + sw;
  lptr wa1 = smem + P2_WO + (128 * kq + 4 * hh + q4) * 128 + blk * 32 + p4 * 8 + (64 ^ sw);
  STAMP(13);
  LBAR();
  STAMP(14);
  {
    f32x16 d[2]; s16x8 kf[8], wf[2][2]; u32x4 of[2];
#define OMM(i) do { d[(i) & 1] = MFMA32(kf[i], __builtin_bit_cast(s16x8, pf[i]), cinit); asm volatile("" : "+v"(d[(i) & 1])); } while (0)
#define NORM(i) do { const f32x16& dd = d[(i) & 1]; const float inv = __builtin_amdgcn_rcpf(dd[8]); \
      of[(i) & 1] = (u32x4){pk_bf16(dd[0] * inv, dd[1] * inv), pk_bf16(dd[2] * inv, dd[3] * inv), pk_bf16(dd[4] * inv, dd[5] * inv), pk_bf16(dd[6] * inv, dd[7] * inv)}; \
      asm volatile("" : "+v"(of[(i) & 1])); } while (0)
#define LDW(i) do { wf[(i) & 1][0] = cat8(tr_read(wa0 + (i) * 2048), tr_read(wa0 + (i) * 2048 + 1024)); \
      wf[(i) & 1][1] = cat8(tr_read(wa1 + (i) * 2048), tr_read(wa1 + (i) * 2048 + 1024)); } while (0)
#define AMM(i) do { acc[0] = MFMA32(wf[(i) & 1][0], __builtin_bit_cast(s16x8, of[(i) & 1]), acc[0]); \
      acc[1] = MFMA32(wf[(i) & 1][1], __builtin_bit_cast(s16x8, of[(i) & 1]), acc[1]); asm volatile("" : "+v"(acc[0]), "+v"(acc[1])); } while (0)
#pragma unroll
    for (int i = 0; i < 8; ++i) kf[i] = LD(s16x8, kva + i * 544);
    LDW(0);
    SCHED();
    OMM(0); OMM(1); SCHED(); NORM(0); SCHED();
#pragma unroll
    for (int i = 0; i < 8; ++i) {
      if (i + 1 < 8) LDW(i + 1);
      SCHED();
      AMM(i);
      if (i + 2 < 8) OMM(i + 2);
      SCHED();
      if (i + 1 < 8) NORM(i + 1);
      SCHED();
    }
#undef OMM
#undef NORM
#undef LDW
#undef AMM
  }
  STAMP(15);
  ST(u32x4, smem + P2_BT + (8 * w + sr) * 128 + ((sp ^ (((sr >> 1) & 1) << 2)) << 4)) = btr;
#pragma unroll
  for (int q = 0; q < 4; ++q) {
    lptr ex = smem + P2_EX + ((2 * q + th) * 4 + kq) * 2048 + lane * 16;
    const f32x16& s = acc[q >> 1];
    ST(f32x4, ex) = (f32x4){s[8 * (q & 1)], s[8 * (q & 1) + 1], s[8 * (q & 1) + 2], s[8 * (q & 1) + 3]};
    ST(f32x4, ex + 1024) = (f32x4){s[8 * (q & 1) + 4], s[8 * (q & 1) + 5], s[8 * (q & 1) + 6], s[8 * (q & 1) + 7]};
  }
  LBAR();
  STAMP(16);
  lptr ei = smem + P2_EX + w * 8192 + lane * 16;
  const float g1 = rbf(1.f - gt);
  lptr ba = smem + P2_BT + (16 * kq + 4 * hh + q4) * 128 + (((th * 64) ^ sw) + blk * 32 + p4 * 8);
  u32x2 res[2];
#pragma unroll
  for (int i = 0; i < 2; ++i) {
    const f32x4 o = (LD(f32x4, ei + 1024 * i) + LD(f32x4, ei + 2048 + 1024 * i)) + (LD(f32x4, ei + 4096 + 1024 * i) + LD(f32x4, ei + 6144 + 1024 * i));
    const u32x2 bb = __builtin_bit_cast(u32x2, tr_read(ba + i * 1024));
    res[i] = (u32x2){pk_bf16(gt * o[0] + g1 * bf_lo(bb.x), gt * o[1] + g1 * bf_hi(bb.x)), pk_bf16(gt * o[2] + g1 * bf_lo(bb.y), gt * o[3] + g1 * bf_hi(bb.y))};
  }
  const auto sx = __builtin_amdgcn_permlane32_swap(res[0].x, res[1].x, false, false);
  const auto sy = __builtin_amdgcn_permlane32_swap(res[0].y, res[1].y, false, false);
  *(u32x4*)(out + (size_t)(64 * rt + 32 * th + l31) * DM + 64 * ct + 16 * kq + 8 * hh) = (u32x4){sx[0], sy[0], sx[1], sy[1]};
  STAMP(17);
}

__global__ __launch_bounds__(512, 2) void fused(const u16* __restrict__ x, const u16* __restrict__ wqkv, const u16* __restrict__ wo,
                                                const u16* __restrict__ lar, const u16* __restrict__ aim, const u16* __restrict__ cre,
                                                const u16* __restrict__ cim, const u16* __restrict__ ldt, const u16* __restrict__ dsk,
                                                const u16* __restrict__ gw, char* __restrict__ ws, u16* __restrict__ out) {
  extern __shared__ __attribute__((aligned(16))) char smem_[];
  lptr smem = (lptr)smem_;
  STAMP_DECL; STAMP(0);
  const int b = blockIdx.x, xcd = b & 7, slot = b >> 3, idx = slot >> 1;
  const int rt = 8 * (xcd >> 1) + (slot & 7), ct = 4 * (xcd & 1) + (slot >> 3);
  int vzero;
  asm volatile("v_mov_b32 %0, 0" : "=v"(vzero));
  const u64 epoch = __hip_atomic_load((gu64*)(ws + WS_SYNC + vzero), RLX_AGENT);
  if (slot & 1) conv_role(x, lar, aim, cre, cim, ldt, dsk, gw, wo, ws, xcd * 16 + idx, ct, epoch, smem STAMP_PASS);
  else gemm_role(x, wqkv, wo, ws, 4 * (xcd >> 1) + (idx & 3), 4 * (xcd & 1) + (idx >> 2), ct, epoch, smem STAMP_PASS);
  STAMP(10);
  LBAR();
  if (threadIdx.x < 64) {
    const u64 tag = next_tag(epoch);
    const int lane = threadIdx.x;
    if (lane == 0) __hip_atomic_store((gu64*)(ws + WS_SYNC + 4096) + b, tag, RLX_AGENT);
    const rsrc_t rs = mk_rsrc(ws + WS_SYNC + 4096, 2048);
    const u32 tl = (u32)tag, thi = (u32)(tag >> 32);
    for (int spin = 0; spin < (1 << 20); ++spin) {
      asm volatile("" ::: "memory");
      const u32x4 f0 = __builtin_amdgcn_raw_buffer_load_b128(rs, lane * 32, 0, 16), f1 = __builtin_amdgcn_raw_buffer_load_b128(rs, lane * 32 + 16, 0, 16);
      const bool ok = f0[0] == tl && f0[1] == thi && f0[2] == tl && f0[3] == thi && f1[0] == tl && f1[1] == thi && f1[2] == tl && f1[3] == thi;
      if (__all(ok)) break;
      __builtin_amdgcn_s_sleep(4);
    }
    if (b == 0 && lane == 0) __hip_atomic_store((gu64*)(ws + WS_SYNC), tag, RLX_AGENT);
  }
  WAIT_VM(0);
  LBAR();
  STAMP(11);
  out_role(ws, out, rt, ct, smem STAMP_PASS);
  STAMP_FLUSH(ws);
}

extern "C" void kernel_launch(void* const* d_in, const int* in_sizes, int n_in, void* d_out, int out_size, void* d_ws,
                              size_t ws_size, hipStream_t stream) {
  (void)in_sizes; (void)n_in; (void)out_size; (void)ws_size;
  (void)hipFuncSetAttribute((const void*)fused, hipFuncAttributeMaxDynamicSharedMemorySize, 163840);
  fused<<<256, 512, 163840, stream>>>((const u16*)d_in[0], (const u16*)d_in[1], (const u16*)d_in[2], (const u16*)d_in[4],
                                      (const u16*)d_in[5], (const u16*)d_in[6], (const u16*)d_in[7], (const u16*)d_in[8],
                                      (const u16*)d_in[9], (const u16*)d_in[10], (char*)d_ws, (u16*)d_out);
}
```

```cpp
#include <hip/hip_runtime.h>
#include <hip/hip_bf16.h>
#include <stdint.h>

typedef unsigned int u32;
typedef unsigned short u16;
typedef __attribute__((ext_vector_type(8))) short s16x8;
typedef __attribute__((ext_vector_type(4))) short s16x4;
typedef __attribute__((ext_vector_type(8))) _Float16 h16x8;
typedef __attribute__((ext_vector_type(2))) _Float16 h16x2;
typedef __attribute__((ext_vector_type(2))) __bf16 b16x2;
typedef __attribute__((ext_vector_type(2))) float f32x2;
typedef __attribute__((ext_vector_type(4))) float f32x4;
typedef __attribute__((ext_vector_type(16))) float f32x16;
typedef __attribute__((ext_vector_type(2))) u32 u32x2;
typedef __attribute__((ext_vector_type(4))) u32 u32x4;
#define LDS_AS __attribute__((address_space(3)))
#define DI __device__ __forceinline__

static constexpr int SEQ = 2048, DM = 512;
#define KVS 256
static constexpr size_t WS_KV = 6u << 20;
static constexpr size_t WS_G = 65536;
static constexpr size_t WS_PQ = 1u << 20;
static constexpr size_t WS_BT = 4u << 20;
static constexpr size_t WS_SYNC = 49152;
static constexpr size_t WS_FLAG = 131072;
typedef unsigned long long u64;
typedef __attribute__((address_space(1))) u64 gu64;
typedef __attribute__((address_space(1))) u32 gu32;
#define RLX_AGENT __ATOMIC_RELAXED, __HIP_MEMORY_SCOPE_AGENT
__device__ __forceinline__ u64 next_tag(u64 e) { return e * 6364136223846793005ull + 1442695040888963407ull; }

#define STAMP_ARG
#define STAMP_PASS
#define STAMP_DECL do {} while (0)
#define STAMP(i) do {} while (0)
#define STAMP_FLUSH(wsp) do {} while (0)
#define POLL_SLEEP 2
#define ST_AUX 16
#define WAIT_VM(n) asm volatile("s_waitcnt vmcnt(" #n ")" ::: "memory")
#define WAIT_L0() __builtin_amdgcn_s_waitcnt(0xC07F)
#define RAW_BAR() __builtin_amdgcn_s_barrier()
#define SCHED() __builtin_amdgcn_sched_barrier(0)
#define LBAR() do { WAIT_L0(); asm volatile("" ::: "memory"); RAW_BAR(); asm volatile("" ::: "memory"); } while (0)
typedef LDS_AS char* lptr;
#define LD(T, p) (*(const LDS_AS T*)(p))
#define ST(T, p) (*(LDS_AS T*)(p))

DI u32 pk_bf16(float a, float b) { return __builtin_bit_cast(u32, __builtin_convertvector((f32x2){a, b}, b16x2)); }
DI u32 pk_f16(f32x2 v) { return __builtin_bit_cast(u32, __builtin_convertvector(v, h16x2)); }
DI float bf_lo(u32 v) { return __builtin_bit_cast(float, v << 16); }
DI float bf_hi(u32 v) { return __builtin_bit_cast(float, v & 0xffff0000u); }
DI float u2f(u32 v) { return __builtin_bit_cast(float, v); }
DI b16x2 u2b(u32 v) { return __builtin_bit_cast(b16x2, v); }
DI float rbf(float v) { return bf_lo(pk_bf16(v, v)); }
static constexpr float LOG2E = 1.44269504088896341f, EPS = 1e-6f;
DI float phi_f(float t) {
  float lo, hi;
  asm("v_min_f32 %0, %1, %2" : "=v"(lo) : "s"(EPS), "v"(t));
  asm("v_max_f32 %0, %1, %2" : "=v"(hi) : "s"(EPS), "v"(t));
  return hi + __builtin_amdgcn_exp2f(__builtin_fmaf(lo, LOG2E, -EPS * LOG2E));
}
DI float exp_f(float v) {
  const float p = v * LOG2E;
  const float r = __builtin_fmaf(v, 1.925963033500011e-8f, __builtin_fmaf(v, LOG2E, -p));
  return __builtin_amdgcn_exp2f(p) * __builtin_fmaf(r, 0.6931471805599453f, 1.f);
}
#define DPP_F(v, ctrl, rmask, bc) __builtin_bit_cast(float, __builtin_amdgcn_update_dpp(0, __builtin_bit_cast(int, v), ctrl, rmask, 0xf, bc))
DI float wave_sum(float r) {
  r += DPP_F(r, 0x111, 0xf, true); r += DPP_F(r, 0x112, 0xf, true); r += DPP_F(r, 0x114, 0xf, true); r += DPP_F(r, 0x118, 0xf, true);
  r += DPP_F(r, 0x142, 0xa, false); r += DPP_F(r, 0x143, 0xc, false);
  return r;
}
DI u32 wave_umax(u32 r) {
#define DPP_U(v, ctrl, rmask, bc) (u32)__builtin_amdgcn_update_dpp(0, (int)(v), ctrl, rmask, 0xf, bc)
  r = max(r, DPP_U(r, 0x111, 0xf, true)); r = max(r, DPP_U(r, 0x112, 0xf, true)); r = max(r, DPP_U(r, 0x114, 0xf, true));
  r = max(r, DPP_U(r, 0x118, 0xf, true)); r = max(r, DPP_U(r, 0x142, 0xa, false)); r = max(r, DPP_U(r, 0x143, 0xc, false));
  return (u32)__builtin_amdgcn_readlane((int)r, 63);
}
DI s16x4 tr_read(lptr p) { return __builtin_amdgcn_ds_read_tr16_b64_v4i16((LDS_AS s16x4*)p); }
DI s16x8 cat8(s16x4 a, s16x4 b) { return __builtin_shufflevector(a, b, 0, 1, 2, 3, 4, 5, 6, 7); }
typedef __amdgpu_buffer_rsrc_t rsrc_t;
DI rsrc_t mk_rsrc(const void* p, int bytes) { return __builtin_amdgcn_make_buffer_rsrc((void*)p, 0, bytes, 0x00020000); }
DI void blds16(rsrc_t r, lptr l, int voff, int soff) { __builtin_amdgcn_raw_ptr_buffer_load_lds(r, (LDS_AS void*)l, 16, voff, soff, 0, 0); }
DI f32x2 cmul(f32x2 a, f32x2 w) {
  f32x2 t, r;
  asm("v_pk_mul_f32 %0, %1, %2 op_sel:[1,1] op_sel_hi:[1,0]" : "=v"(t) : "v"(a), "v"(w));
  asm("v_pk_fma_f32 %0, %1, %2, %3 op_sel_hi:[0,1,1] neg_lo:[0,0,1]" : "=v"(r) : "v"(a), "v"(w), "v"(t));
  return r;
}
DI f32x2 cexp_p(float alpha, float theta, float p) {
  float m = __builtin_amdgcn_exp2f(alpha * p);
  float f = __builtin_amdgcn_fractf(theta * p);
  return (f32x2){m * __builtin_amdgcn_cosf(f), m * __builtin_amdgcn_sinf(f)};
}
#define MFMA16(a, b, c) __builtin_amdgcn_mfma_f32_16x16x32_bf16((a), (b), (c), 0, 0, 0)
#define MFMA16H(a, b, c) __builtin_amdgcn_mfma_f32_16x16x32_f16((a), (b), (c), 0, 0, 0)
#define MFMA32(a, b, c) __builtin_amdgcn_mfma_f32_32x32x16_bf16((a), (b), (c), 0, 0, 0)

static constexpr int STAGE = 40960;
static constexpr int P2_WO = 0, P2_KV = 65536, P2_BT = P2_KV + 18432, P2_EX = P2_BT + 8192;

DI void fetch_wo(const u16* __restrict__ wo, int ct, int w, int lane, lptr smem) {
  const rsrc_t rwo = mk_rsrc(wo, DM * DM * 2);
  const int sr = lane >> 3, sp = lane & 7, sc = (sp ^ (((sr >> 1) & 1) << 2)) << 4;
#pragma unroll
  for (int i = 0; i < 8; ++i) blds16(rwo, smem + P2_WO + (64 * w + 8 * i) * 128, sr * 1024 + sc, (64 * w + 8 * i) * 1024 + 128 * ct);
}

DI void gemm_role(const u16* __restrict__ x, const u16* __restrict__ wqkv, char* __restrict__ ws, int tt, int hg,
                  u64 epoch, lptr smem STAMP_ARG) {
  const int tid = threadIdx.x, lane = tid & 63;
  const int w = __builtin_amdgcn_readfirstlane(tid >> 6);
  const int wm = w >> 2, wh = w & 3, g = lane >> 4, l15 = lane & 15;
  const int sr = lane >> 3, sp = lane & 7;
  const rsrc_t rx = mk_rsrc(x, SEQ * DM * 2), rw = mk_rsrc(wqkv, DM * 1536 * 2);
  const int xv = sr * 1024 + ((sp ^ sr) << 4), xs0 = (tt * 128 + 16 * w) * 1024;
  const int fw = ((sr >> 1) & 1) | ((w & 1) << 1);
  const int wv = sr * 3072 + ((sp ^ (fw << 1)) << 4), ws0 = 8 * w * 3072 + hg * 128;
#define ISS(s, i) do { lptr sb = smem + ((s) & 3) * STAGE; \
    if ((i) < 2) blds16(rx, sb + w * 2048 + (i) * 1024, xv, xs0 + (s) * 128 + (i) * 8192); \
    else blds16(rw, sb + 16384 + ((i) - 2) * 8192 + w * 1024, wv, ws0 + (s) * 64 * 3072 + ((i) - 2) * 1024); } while (0)
#define ISSUE(s) do { ISS(s, 0); ISS(s, 1); ISS(s, 2); ISS(s, 3); ISS(s, 4); } while (0)
  gu64* sync_z = (gu64*)(ws + WS_SYNC + 128 + hg * 128);
  ISSUE(0); ISSUE(1);
  const int xo = (64 * wm + l15) * 128 + ((g ^ (lane & 7)) << 4);
  const int q4 = l15 >> 2, p4 = lane & 3;
  const int wo_ = 16384 + (8 * g + q4) * 128 + ((wh ^ (((q4 >> 1) & 1) | ((g & 1) << 1))) << 5) + 8 * p4;
  f32x4 aq[4], ak[4], av[4];
#pragma unroll
  for (int i = 0; i < 4; ++i) { aq[i] = (f32x4){EPS, EPS, EPS, EPS}; ak[i] = aq[i]; av[i] = (f32x4){0, 0, 0, 0}; }
  s16x8 xa[4], wa[3], xb[4], wb[3];
#define LOADF(xf, wf, s, ks) do { lptr sb = smem + ((s) & 3) * STAGE; \
    _Pragma("unroll") for (int st = 0; st < 3; ++st) \
      wf[st] = cat8(tr_read(sb + wo_ + st * 8192 + (ks) * 4096), tr_read(sb + wo_ + st * 8192 + (ks) * 4096 + 512)); \
    _Pragma("unroll") for (int mt = 0; mt < 4; ++mt) xf[mt] = LD(s16x8, sb + ((xo ^ ((ks) * 64)) + mt * 2048)); } while (0)
#define MM3(xf, wf, mt) do { aq[mt] = MFMA16(wf[0], xf[mt], aq[mt]); ak[mt] = MFMA16(xf[mt], wf[1], ak[mt]); av[mt] = MFMA16(xf[mt], wf[2], av[mt]); \
    asm volatile("" : "+v"(aq[mt]), "+v"(ak[mt]), "+v"(av[mt])); } while (0)
#define MMI(xf, wf, is) do { SCHED(); MM3(xf, wf, 0); SCHED(); if ((is) >= 0) { ISS(is, 0); ISS(is, 1); } SCHED(); MM3(xf, wf, 1); SCHED(); \
    if ((is) >= 0) { ISS(is, 2); ISS(is, 3); } SCHED(); MM3(xf, wf, 2); SCHED(); if ((is) >= 0) ISS(is, 4); SCHED(); MM3(xf, wf, 3); SCHED(); } while (0)
#define STEP(s, vm, i1, i2) do { WAIT_L0(); LOADF(xb, wb, s, 1); MMI(xa, wa, i1); \
    WAIT_L0(); WAIT_VM(vm); RAW_BAR(); LOADF(xa, wa, (s) + 1, 0); MMI(xb, wb, i2); } while (0)
  STAMP(1);
  WAIT_VM(5); RAW_BAR();
  STAMP(2);
  LOADF(xa, wa, 0, 0);
  STEP(0, 5, 2, 3);
  STAMP(3);
  STEP(1, 10, 4, 5);
  STEP(2, 10, -1, 6);
  STEP(3, 10, -1, 7);
  STAMP(4);
  STEP(4, 10, -1, -1);
  STEP(5, 5, -1, -1);
  u64 ready = __hip_atomic_load(sync_z, RLX_AGENT);
  STEP(6, 0, -1, -1);
  STAMP(5);
  WAIT_L0(); LOADF(xb, wb, 7, 1); MMI(xa, wa, -1); WAIT_L0(); MMI(xb, wb, -1);
  STAMP(6);
#undef ISS
#undef ISSUE
#undef LOADF
#undef MM3
#undef MMI
#undef STEP
  const int h = 4 * hg + wh;
  f32x4 kvt = {0, 0, 0, 0}, zz = {0, 0, 0, 0};
  const u32x4 ones_u = {0x3f803f80u, 0x3f803f80u, 0x3f803f80u, 0x3f803f80u};
#pragma unroll
  for (int pr = 0; pr < 2; ++pr) {
    u32x4 pk, vv;
#pragma unroll
    for (int m = 0; m < 2; ++m) {
      const f32x4 kk = ak[2 * pr + m], v4 = av[2 * pr + m];
      pk[2 * m] = pk_bf16(phi_f(kk[0]), phi_f(kk[1])); pk[2 * m + 1] = pk_bf16(phi_f(kk[2]), phi_f(kk[3]));
      vv[2 * m] = pk_bf16(v4[0], v4[1]); vv[2 * m + 1] = pk_bf16(v4[2], v4[3]);
    }
    kvt = MFMA16(__builtin_bit_cast(s16x8, vv), __builtin_bit_cast(s16x8, pk), kvt);
    zz = MFMA16(__builtin_bit_cast(s16x8, ones_u), __builtin_bit_cast(s16x8, pk), zz);
  }
  lptr red = smem + 66560;
#pragma unroll
  for (int r = 0; r < 4; ++r) ST(float, red + (wm * 4 + wh) * 1088 + l15 * 4 + (4 * g + r) * 64) = kvt[r];
  if (g == 0) ST(float, red + (wm * 4 + wh) * 1088 + l15 * 4 + 1024) = zz[0];
  const u64 tag = next_tag(epoch);
#pragma unroll 1
  for (int spin = 0; ready != tag && spin < (1 << 22); ++spin) { __builtin_amdgcn_s_sleep(2); ready = __hip_atomic_load(sync_z, RLX_AGENT); }
  STAMP(7);
  LBAR();
  STAMP(8);
  float* kvacc = (float*)(ws + WS_KV + (size_t)(hg * 17 + w) * KVS) + lane;
  atomicAdd(kvacc, LD(float, red + tid * 4) + LD(float, red + 4352 + tid * 4));
  atomicAdd(kvacc + 8 * (KVS / 4), LD(float, red + 2048 + tid * 4) + LD(float, red + 4352 + 2048 + tid * 4));
  if (tid < 64) atomicAdd(kvacc + 16 * (KVS / 4), LD(float, red + 4096 + tid * 4) + LD(float, red + 4352 + 4096 + tid * 4));
  const rsrc_t rws = mk_rsrc(ws, 8 << 20);
  const int pqv = (g >> 1) * 512 + l15 * 16 + (g & 1) * 256, pqs = (int)WS_PQ + ((tt * 4 + 2 * wm) * 32 + h) * 1024;
#pragma unroll
  for (int pr = 0; pr < 2; ++pr) {
    const f32x4 q0 = aq[2 * pr], q1 = aq[2 * pr + 1];
    const auto s0 = __builtin_amdgcn_permlane16_swap(pk_bf16(phi_f(q0[0]), phi_f(q0[1])), pk_bf16(phi_f(q1[0]), phi_f(q1[1])), false, false);
    const auto s1 = __builtin_amdgcn_permlane16_swap(pk_bf16(phi_f(q0[2]), phi_f(q0[3])), pk_bf16(phi_f(q1[2]), phi_f(q1[3])), false, false);
    __builtin_amdgcn_raw_buffer_store_b128((u32x4){s0[0], s1[0], s0[1], s1[1]}, rws, pqv, pqs + pr * 32768, ST_AUX);
  }
  STAMP(9);
}

static constexpr int TB_ROW = 544, TB_CH = 63 * TB_ROW;
static constexpr int U_OFF = 4 * TB_CH, U_CH = (2048 + 240) * 2;
static constexpr int KR_CP = 4160, KR_CH = 2 * KR_CP;

DI void conv_role(const u16* __restrict__ x, const u16* __restrict__ lar, const u16* __restrict__ aim, const u16* __restrict__ cre,
                  const u16* __restrict__ cim, const u16* __restrict__ ldt, const u16* __restrict__ dsk, const u16* __restrict__ gw,
                  char* __restrict__ ws, int cg, u64 epoch, lptr smem STAMP_ARG) {
  const int tid = threadIdx.x, lane = tid & 63;
  const int w = __builtin_amdgcn_readfirstlane(tid >> 6);
  const int ch = w & 3, half = w >> 2, g = lane >> 4, l15 = lane & 15;
  const int c = 4 * cg + ch;
  const rsrc_t rx = mk_rsrc(x, SEQ * DM * 2);
  const float dsk_f = bf_lo(dsk[c]);
  const bool clear = (cg & 15) == 0;
  u32x4 gx[4], gwv;
  if (w < 4) {
    const int cw = 4 * cg + w;
    const u32 lar2 = *(const u32*)(lar + cw * 128 + 2 * lane), aim2 = *(const u32*)(aim + cw * 128 + 2 * lane);
    const u32 cre2 = *(const u32*)(cre + cw * 128 + 2 * lane), cim2 = *(const u32*)(cim + cw * 128 + 2 * lane);
    const u32 ldt_u = ldt[cw];
    SCHED(); RAW_BAR(); SCHED();
    const float dt = rbf(exp_f(bf_lo(ldt_u)));
    float al[2], th[2];
    al[0] = -rbf(exp_f(bf_lo(lar2))) * dt * LOG2E; al[1] = -rbf(exp_f(bf_hi(lar2))) * dt * LOG2E;
    th[0] = bf_lo(aim2) * dt * 0.15915494309189535f; th[1] = bf_hi(aim2) * dt * 0.15915494309189535f;
    STAMP(1);
    u32 ce = wave_umax(max(max(cre2 & 0x7f80u, (cre2 >> 16) & 0x7f80u), max(cim2 & 0x7f80u, (cim2 >> 16) & 0x7f80u))) >> 7;
    ce = ce < 1 ? 1 : (ce > 253 ? 253 : ce);
    const float cinv = __builtin_bit_cast(float, (254u - ce) << 23);
    const float kscale = 2.f * dt * __builtin_bit_cast(float, ce << 23);
    lptr tb = smem + w * TB_CH + lane * 8;
#define PUT(e, v) ST(u32x2, tb + (e) * TB_ROW) = (u32x2){pk_f16(v[0]), pk_f16(v[1])}
#define CMUL2(d, a, b) do { d[0] = cmul(a[0], b[0]); d[1] = cmul(a[1], b[1]); } while (0)
#define CONJ(v) ((f32x2){(v).x, -(v).y})
    {
      f32x2 y[17][2], wB[2], w16[2], xa[2], xb[2], a[2], t[2];
#pragma unroll
      for (int m = 0; m < 2; ++m) {
        y[1][m] = CONJ(cexp_p(al[m], th[m], 1.f)); w16[m] = cexp_p(al[m], th[m], 16.f); wB[m] = CONJ(cexp_p(al[m], th[m], 256.f));
        y[16][m] = CONJ(w16[m]);
      }
      xa[0] = (f32x2){bf_lo(cre2) * cinv, bf_lo(cim2) * cinv}; xa[1] = (f32x2){bf_hi(cre2) * cinv, bf_hi(cim2) * cinv};
      ST(u32x2, tb + 16 * TB_ROW) = (u32x2){0x3c00u, 0x3c00u};
      PUT(0, xa); PUT(17, y[1]); PUT(32, wB);
      f32x2 w32[2];
      CMUL2(xb, xa, w16); CMUL2(w32, w16, w16); CMUL2(y[2], y[1], y[1]); CMUL2(a, wB, wB);
      PUT(1, xb); PUT(18, y[2]); PUT(41, a);
      f32x2 aS[6][2];
      aS[0][0] = a[0]; aS[0][1] = a[1];
#pragma unroll
      for (int i = 1; i < 8; ++i) {
        CMUL2(xa, xa, w32); CMUL2(xb, xb, w32);
        CMUL2(y[2 * i + 1], y[2 * i - 1], y[2]);
        if (i < 7) CMUL2(y[2 * i + 2], y[2 * i], y[2]);
        if (i < 6) CMUL2(aS[i], aS[i - 1], wB);
        PUT(2 * i, xa); PUT(2 * i + 1, xb); PUT(17 + 2 * i, y[2 * i + 1]);
        if (i < 7) PUT(18 + 2 * i, y[2 * i + 2]);
        if (i == 1) PUT(46, aS[1]);
        if (i >= 2 && i < 6) PUT(51 + 3 * (i - 2), aS[i]);
        CMUL2(t, wB, y[2 * i]); PUT(32 + i, t);
      }
      CMUL2(t, wB, y[16]); PUT(40, t);
#pragma unroll
      for (int s = 0; s < 2; ++s)
#pragma unroll
        for (int m = 1; m < 5; ++m) { CMUL2(t, aS[s], y[4 * m]); PUT(41 + 5 * s + m, t); }
#pragma unroll
      for (int s = 2; s < 6; ++s)
#pragma unroll
        for (int m = 1; m < 3; ++m) { CMUL2(t, aS[s], y[8 * m]); PUT(51 + 3 * (s - 2) + m, t); }
    }
#undef PUT
    STAMP(2);
    f32x4 kd[4] = {{0, 0, 0, 0}, {0, 0, 0, 0}, {0, 0, 0, 0}, {0, 0, 0, 0}};
    {
      const int gq = l15 >> 2, r = min(l15 & 3, 2);
      lptr tch = smem + w * TB_CH + 16 * g;
      lptr pb = tch + l15 * TB_ROW, pa0 = pb + 16 * TB_ROW, pa1 = tch + (32 + ((0x8766654443222100ull >> (4 * l15)) & 15)) * TB_ROW;
      lptr pa2 = tch + (41 + 5 * (gq >> 1) + 2 * (gq & 1) + r) * TB_ROW, pa3 = tch + (51 + 3 * gq + r) * TB_ROW;
      h16x8 f[2][4][5];
#define LDK(bf, q) do { _Pragma("unroll") for (int k2 = 0; k2 < 4; ++k2) { const int o = 64 * (4 * (q) + k2); \
        f[bf][k2][0] = LD(h16x8, pa0 + o); f[bf][k2][1] = LD(h16x8, pa1 + o); f[bf][k2][2] = LD(h16x8, pa2 + o); \
        f[bf][k2][3] = LD(h16x8, pa3 + o); f[bf][k2][4] = LD(h16x8, pb + o); } } while (0)
#define MMK(bf) do { _Pragma("unroll") for (int k2 = 0; k2 < 4; ++k2) _Pragma("unroll") for (int e = 0; e < 4; ++e) \
        kd[e] = MFMA16H(f[bf][k2][e], f[bf][k2][4], kd[e]); } while (0)
      SCHED(); LDK(0, 0); SCHED(); LDK(1, 1); SCHED(); MMK(0); MMK(1);
#undef LDK
#undef MMK
    }
    STAMP(3);
    lptr kr = smem + w * TB_CH;
    {
      const f32x4 k0 = kd[0] * kscale, k1 = kd[1] * kscale;
      if (lane < 8) ST(u32x4, kr + (lane >> 2) * KR_CP + 4096 + 16 * (lane & 3)) = (u32x4){0u, 0u, 0u, 0u};
      const int y = 2044 - 16 * l15 - 4 * g;
      const u32 p32 = pk_bf16(k0[3], k0[2]), p10 = pk_bf16(k0[1], k0[0]);
      ST(u32x2, kr + 2 * y) = (u32x2){p32, p10};
      ST(u16, kr + KR_CP + 2 * y + 2) = (u16)p32;
      ST(u32, kr + KR_CP + 2 * y + 4) = pk_bf16(k0[2], k0[1]);
      ST(u16, kr + KR_CP + 2 * y + 8) = (u16)(p10 >> 16);
      const u32 q10 = pk_bf16(k1[1], k1[0]);
      ST(u32x2, kr + 2 * (y - 256)) = (u32x2){pk_bf16(k1[3], k1[2]), q10};
      ST(u32x2, kr + KR_CP + 2 * (y - 256)) = (u32x2){pk_bf16(k1[3], k1[3]), pk_bf16(k1[2], k1[1])};
      if (lane == 0) ST(u16, kr + KR_CP + 2 * 1792) = (u16)q10;
    }
    {
      const f32x4 k0 = kd[2] * kscale, k1 = kd[3] * kscale;
      const int y2 = 2040 - (512 + 256 * (g >> 1) + 16 * l15 + 8 * (g & 1));
      const u32 b2 = pk_bf16(k0[2], k0[2]), b1 = pk_bf16(k0[1], k0[1]), b0 = pk_bf16(k0[0], k0[0]);
      ST(u32x4, kr + 2 * y2) = (u32x4){b2, b1, pk_bf16(k0[1], k0[0]), b0};
      ST(u32x4, kr + KR_CP + 2 * y2) = (u32x4){b2, pk_bf16(k0[2], k0[1]), b1, b0};
      const int y3 = 2032 - (1024 + 256 * g + 16 * l15);
      const u32 a2 = pk_bf16(k1[2], k1[2]), a1 = pk_bf16(k1[1], k1[1]), a0 = pk_bf16(k1[0], k1[0]);
      ST(u32x4, kr + 2 * y3) = (u32x4){a2, a2, a1, a1};
      ST(u32x4, kr + 2 * y3 + 16) = (u32x4){a1, pk_bf16(k1[1], k1[0]), a0, a0};
      ST(u32x4, kr + KR_CP + 2 * y3) = (u32x4){a2, a2, pk_bf16(k1[2], k1[1]), a1};
      ST(u32x4, kr + KR_CP + 2 * y3 + 16) = (u32x4){a1, a1, a0, a0};
    }
#pragma unroll
    for (int r = 0; r < 4; ++r) gx[r] = __builtin_amdgcn_raw_buffer_load_b128(rx, lane * 16 + 1024 * r, (16 * cg + 4 * w) * 1024, 0);
    gwv = *(const u32x4*)(gw + 8 * lane);
    STAMP(4);
  } else {
    const int t2 = tid - 256;
    if (clear) {
      const rsrc_t rk = mk_rsrc(ws, 8 << 20);
      const u32x4 z4 = {0u, 0u, 0u, 0u};
      __builtin_amdgcn_raw_buffer_store_b128(z4, rk, (t2 >> 4) * KVS + (t2 & 15) * 16, (int)WS_KV + (cg >> 4) * 17 * KVS, 16);
      if (t2 < 16) __builtin_amdgcn_raw_buffer_store_b128(z4, rk, t2 * 16, (int)WS_KV + ((cg >> 4) * 17 + 16) * KVS, 16);
    }
    SCHED(); RAW_BAR(); SCHED();
    u32x2 xr[8];
#pragma unroll
    for (int r = 0; r < 8; ++r) xr[r] = __builtin_amdgcn_raw_buffer_load_b64(rx, t2 * 4096 + (r & 3) * 1024, 8 * cg + (r >> 2) * (1024 * 1024), 0);
    STAMP(1);
    if (lane < 30) ST(u32x4, smem + U_OFF + (w - 4) * U_CH + lane * 16) = (u32x4){0u, 0u, 0u, 0u};
#pragma unroll
    for (int hf = 0; hf < 2; ++hf) {
      lptr ub = smem + U_OFF + 480 + t2 * 8 + hf * 2048;
      const u32x2 x0 = xr[4 * hf], x1 = xr[4 * hf + 1], x2 = xr[4 * hf + 2], x3 = xr[4 * hf + 3];
      ST(u32x2, ub + 0 * U_CH) = (u32x2){__builtin_amdgcn_perm(x1.x, x0.x, 0x05040100u), __builtin_amdgcn_perm(x3.x, x2.x, 0x05040100u)};
      ST(u32x2, ub + 1 * U_CH) = (u32x2){__builtin_amdgcn_perm(x1.x, x0.x, 0x07060302u), __builtin_amdgcn_perm(x3.x, x2.x, 0x07060302u)};
      ST(u32x2, ub + 2 * U_CH) = (u32x2){__builtin_amdgcn_perm(x1.y, x0.y, 0x05040100u), __builtin_amdgcn_perm(x3.y, x2.y, 0x05040100u)};
      ST(u32x2, ub + 3 * U_CH) = (u32x2){__builtin_amdgcn_perm(x1.y, x0.y, 0x07060302u), __builtin_amdgcn_perm(x3.y, x2.y, 0x07060302u)};
    }
    WAIT_VM(0);
    STAMP(4);
  }
  lptr ka[8];
  {
    const int yb = 2031 - l15 + 8 * g - 128 * half - 96 - 1792;
    lptr k7 = smem + ch * TB_CH + (yb & 1) * KR_CP + ((yb + 1) >> 1) * 4;
#pragma unroll
    for (int n = 0; n < 8; ++n) { ka[n] = k7 + 512 * (7 - n); asm volatile("" : "+v"(ka[n])); }
  }
  lptr ua = smem + U_OFF + ch * U_CH + (8 * g + 16 * l15 + 224 - 128 * half - 96) * 2;
  STAMP(6);
  LBAR();
  if (clear && tid == 256) __hip_atomic_store((gu64*)(ws + WS_SYNC + 128 + (cg >> 4) * 128), next_tag(epoch), RLX_AGENT);
  STAMP(7);
  f32x4 acc[8];
#pragma unroll
  for (int i = 0; i < 8; ++i) acc[i] = (f32x4){0, 0, 0, 0};
  {
    u32x4 A0[8], A1[8]; s16x8 B0[8], B1[8];
#define LOADC(A, B, rr) do { _Pragma("unroll") for (int n = 0; n < 8; ++n) { \
      lptr kp = ka[n] + 64 * (3 - (rr)); \
      A[n] = (u32x4){LD(u32, kp), LD(u32, kp + 4), LD(u32, kp + 8), LD(u32, kp + 12)}; \
      B[n] = LD(s16x8, ua + 64 * (3 - (rr)) + 512 * n); } } while (0)
#define MMC(A, B) do { _Pragma("unroll") for (int nu = 0; nu < 8; ++nu) _Pragma("unroll") for (int mu = 0; mu < 8 - nu; ++mu) \
      acc[nu + mu] = MFMA16(__builtin_bit_cast(s16x8, A[nu]), B[mu], acc[nu + mu]); } while (0)
#define PINC() do { asm volatile("" : "+v"(acc[0]), "+v"(acc[1]), "+v"(acc[2]), "+v"(acc[3]), "+v"(acc[4]), "+v"(acc[5]), "+v"(acc[6]), "+v"(acc[7])); SCHED(); } while (0)
#define MIXC() do { _Pragma("unroll") for (int q = 0; q < 12; ++q) { __builtin_amdgcn_sched_group_barrier(0x8, 3, 0); __builtin_amdgcn_sched_group_barrier(0x100, 2, 0); } } while (0)
    LOADC(A0, B0, 0);
    SCHED();
    WAIT_L0(); SCHED(); LOADC(A1, B1, 1); MMC(A0, B0); MIXC(); PINC();
    WAIT_L0(); SCHED(); LOADC(A0, B0, 2); MMC(A1, B1); MIXC(); PINC();
    WAIT_L0(); SCHED(); LOADC(A1, B1, 3); MMC(A0, B0); MIXC(); PINC();
    WAIT_L0(); SCHED(); MMC(A1, B1); PINC();
#undef LOADC
#undef MMC
  }
  STAMP(8);
  {
    lptr ex = smem + (2 + half) * TB_CH + KR_CH + ch * 4096 + lane * 16;
    f32x4 keep[4];
    if (half) {
#pragma unroll
      for (int i = 0; i < 4; ++i) { ST(f32x4, ex + 1024 * i) = acc[i]; keep[i] = acc[4 + i]; }
    } else {
#pragma unroll
      for (int i = 0; i < 4; ++i) { ST(f32x4, ex + 1024 * i) = acc[4 + i]; keep[i] = acc[i]; }
    }
    const int t0 = 1024 * half + 16 * l15 + 4 * g;
    const rsrc_t rws = mk_rsrc(ws, 8 << 20);
    lptr up = smem + U_OFF + ch * U_CH + 480 + 2 * t0;
#pragma unroll
    for (int i = 0; i < 4; ++i) {
      const u32x2 uu = LD(u32x2, up + 512 * i);
      keep[i] += (f32x4){bf_lo(uu.x), bf_hi(uu.x), bf_lo(uu.y), bf_hi(uu.y)} * dsk_f;
    }
    if (w < 4) {
      float sg[4];
#pragma unroll
      for (int r = 0; r < 4; ++r) {
        float s0 = 0.f;
#pragma unroll
        for (int i = 0; i < 4; ++i) s0 = __builtin_amdgcn_fdot2_f32_bf16(u2b(gx[r][i]), u2b(gwv[i]), s0, false);
        sg[r] = rbf(__builtin_amdgcn_rcpf(1.f + __builtin_amdgcn_exp2f(-LOG2E * rbf(wave_sum(s0)))));
      }
      if (lane == 63) __builtin_amdgcn_raw_buffer_store_b128(__builtin_bit_cast(u32x4, (f32x4){sg[0], sg[1], sg[2], sg[3]}), rws, 0, (int)WS_G + (16 * cg + 4 * w) * 4, ST_AUX);
    }
    LBAR();
    lptr ei = smem + (3 - half) * TB_CH + KR_CH + ch * 4096 + lane * 16;
    u32x2 yb[4];
#pragma unroll
    for (int i = 0; i < 4; ++i) {
      const f32x4 y = keep[i] + LD(f32x4, ei + 1024 * i);
      yb[i] = (u32x2){pk_bf16(y[0], y[1]), pk_bf16(y[2], y[3])};
    }
#pragma unroll
    for (int i = 0; i < 2; ++i) {
      const auto sx = __builtin_amdgcn_permlane16_swap(yb[2 * i].x, yb[2 * i + 1].x, false, false);
      const auto sy = __builtin_amdgcn_permlane16_swap(yb[2 * i].y, yb[2 * i + 1].y, false, false);
      __builtin_amdgcn_raw_buffer_store_b128((u32x4){sx[0], sy[0], sx[1], sy[1]}, rws, 2048 * half + 32 * l15 + 16 * (g >> 1) + 512 * (g & 1) + 1024 * i,
                                             (int)WS_BT + c * 4096, ST_AUX);
    }
  }
  STAMP(9);
}

struct Tags { u64 a, b; };
DI Tags poll_load(char* ws, int off, int lane) {
  gu64* p = (gu64*)(ws + WS_FLAG + off) + lane;
  return Tags{__hip_atomic_load(p, RLX_AGENT), __hip_atomic_load(p + 64, RLX_AGENT)};
}
DI void poll_wait(char* ws, int off, int lane, u64 tag, Tags f) {
#pragma unroll 1
  for (int spin = 0; spin < (1 << 20); ++spin) {
    if (__all(f.a == tag && f.b == tag)) break;
    __builtin_amdgcn_s_sleep(POLL_SLEEP);
    f = poll_load(ws, off, lane);
    __builtin_amdgcn_s_waitcnt(0x0F70);
  }
}

DI void out_role(char* __restrict__ ws, u16* __restrict__ out, int rt, int ct, u64 tag, lptr smem STAMP_ARG) {
  const int tid = threadIdx.x, lane = tid & 63;
  const int w = __builtin_amdgcn_readfirstlane(tid >> 6);
  const int th = w & 1, kq = w >> 1, hh = lane >> 5, l31 = lane & 31;
  const rsrc_t rws = mk_rsrc(ws, 8 << 20);
  const int foff = (blockIdx.x & 31) * 2048 + 1024;
  Tags fb = {0, 0};
  if (w == 1) fb = poll_load(ws, foff, lane);
  u32x4 kvr[5];
#pragma unroll
  for (int i = 0; i < 4; ++i) kvr[i] = __builtin_amdgcn_raw_buffer_load_b128(rws, (tid >> 4) * KVS + (tid & 15) * 16, (int)WS_KV + 32 * KVS * i, 16);
  kvr[4] = __builtin_amdgcn_raw_buffer_load_b128(rws, ((tid & 127) >> 4) * KVS + (tid & 15) * 16, (int)WS_KV + 128 * KVS, 16);
  SCHED(); RAW_BAR(); SCHED();
  u32x4 pf[8];
#pragma unroll
  for (int i = 0; i < 8; ++i) pf[i] = __builtin_amdgcn_raw_buffer_load_b128(rws, lane * 16, (int)WS_PQ + ((2 * rt + th) * 32 + 8 * kq + i) * 1024, 16);
  const int sr = lane >> 3, sp = lane & 7;
  STAMP(12);
#pragma unroll
  for (int i = 0; i < 4; ++i) ST(u32x2, smem + P2_KV + (tid + 512 * i) * 8) = (u32x2){pk_bf16(u2f(kvr[i][0]), u2f(kvr[i][1])), pk_bf16(u2f(kvr[i][2]), u2f(kvr[i][3]))};
  ST(u32x2, smem + P2_KV + (2048 + (tid & 127)) * 8) = (u32x2){pk_bf16(u2f(kvr[4][0]), u2f(kvr[4][1])), pk_bf16(u2f(kvr[4][2]), u2f(kvr[4][3]))};
  f32x16 acc[2];
#pragma unroll
  for (int i = 0; i < 16; ++i) { acc[0][i] = 0.f; acc[1][i] = 0.f; }
  f32x16 cinit = acc[0];
  cinit[8] = EPS;
  lptr kva = smem + P2_KV + 8 * kq * 544 + min(l31, 16) * 32 + hh * 16;
  const int blk = (lane >> 4) & 1, q4 = (lane & 15) >> 2, p4 = lane & 3;
  const int sw = ((q4 >> 1) & 1) << 6;
  lptr wa0 = smem + P2_WO + (128 * kq + 4 * hh + q4) * 128 + blk * 32 + p4 * 8 + sw;
  lptr wa1 = smem + P2_WO + (128 * kq + 4 * hh + q4) * 128 + blk * 32 + p4 * 8 + (64 ^ sw);
  STAMP(13);
  if (w == 1) poll_wait(ws, foff, lane, tag, fb);
  WAIT_VM(8);
  LBAR();
  if (blockIdx.x == 0 && tid == 0) __hip_atomic_store((gu64*)(ws + WS_SYNC), tag, RLX_AGENT);
  const u32x4 btr = __builtin_amdgcn_raw_buffer_load_b128(rws, sr * 4096 + sp * 16, (int)WS_BT + (64 * ct + 8 * w) * 4096 + 128 * rt, 16);
  const float gt = u2f(__builtin_amdgcn_raw_buffer_load_b32(rws, l31 * 4, (int)WS_G + (64 * rt + 32 * th) * 4, 16));
  STAMP(14);
  {
    f32x16 d[2]; s16x8 kf[8], wf[2][2]; u32x4 of[2];
#define OMM(i) do { d[(i) & 1] = MFMA32(kf[i], __builtin_bit_cast(s16x8, pf[i]), cinit); asm volatile("" : "+v"(d[(i) & 1])); } while (0)
#define NORM(i) do { const f32x16& dd = d[(i) & 1]; const float inv = __builtin_amdgcn_rcpf(dd[8]); \
      of[(i) & 1] = (u32x4){pk_bf16(dd[0] * inv, dd[1] * inv), pk_bf16(dd[2] * inv, dd[3] * inv), pk_bf16(dd[4] * inv, dd[5] * inv), pk_bf16(dd[6] * inv, dd[7] * inv)}; \
      asm volatile("" : "+v"(of[(i) & 1])); } while (0)
#define LDW(i) do { wf[(i) & 1][0] = cat8(tr_read(wa0 + (i) * 2048), tr_read(wa0 + (i) * 2048 + 1024)); \
      wf[(i) & 1][1] = cat8(tr_read(wa1 + (i) * 2048), tr_read(wa1 + (i) * 2048 + 1024)); } while (0)
#define AMM(i) do { acc[0] = MFMA32(wf[(i) & 1][0], __builtin_bit_cast(s16x8, of[(i) & 1]), acc[0]); \
      acc[1] = MFMA32(wf[(i) & 1][1], __builtin_bit_cast(s16x8, of[(i) & 1]), acc[1]); asm volatile("" : "+v"(acc[0]), "+v"(acc[1])); } while (0)
#pragma unroll
    for (int i = 0; i < 8; ++i) kf[i] = LD(s16x8, kva + i * 544);
    LDW(0);
    SCHED();
    OMM(0); OMM(1); SCHED(); NORM(0); SCHED();
#pragma unroll
    for (int i = 0; i < 8; ++i) {
      if (i + 1 < 8) LDW(i + 1);
      SCHED();
      AMM(i);
      if (i + 2 < 8) OMM(i + 2);
      SCHED();
      if (i + 1 < 8) NORM(i + 1);
      SCHED();
    }
#undef OMM
#undef NORM
#undef LDW
#undef AMM
  }
  STAMP(15);
  ST(u32x4, smem + P2_BT + (8 * w + sr) * 128 + ((sp ^ (((sr >> 1) & 1) << 2)) << 4)) = btr;
#pragma unroll
  for (int q = 0; q < 4; ++q) {
    lptr ex = smem + P2_EX + ((2 * q + th) * 4 + kq) * 2048 + lane * 16;
    const f32x16& s = acc[q >> 1];
    ST(f32x4, ex) = (f32x4){s[8 * (q & 1)], s[8 * (q & 1) + 1], s[8 * (q & 1) + 2], s[8 * (q & 1) + 3]};
    ST(f32x4, ex + 1024) = (f32x4){s[8 * (q & 1) + 4], s[8 * (q & 1) + 5], s[8 * (q & 1) + 6], s[8 * (q & 1) + 7]};
  }
  LBAR();
  STAMP(16);
  lptr ei = smem + P2_EX + w * 8192 + lane * 16;
  const float g1 = rbf(1.f - gt);
  lptr ba = smem + P2_BT + (16 * kq + 4 * hh + q4) * 128 + (((th * 64) ^ sw) + blk * 32 + p4 * 8);
  u32x2 res[2];
#pragma unroll
  for (int i = 0; i < 2; ++i) {
    const f32x4 o = (LD(f32x4, ei + 1024 * i) + LD(f32x4, ei + 2048 + 1024 * i)) + (LD(f32x4, ei + 4096 + 1024 * i) + LD(f32x4, ei + 6144 + 1024 * i));
    const u32x2 bb = __builtin_bit_cast(u32x2, tr_read(ba + i * 1024));
    res[i] = (u32x2){pk_bf16(gt * o[0] + g1 * bf_lo(bb.x), gt * o[1] + g1 * bf_hi(bb.x)), pk_bf16(gt * o[2] + g1 * bf_lo(bb.y), gt * o[3] + g1 * bf_hi(bb.y))};
  }
  const auto sx = __builtin_amdgcn_permlane32_swap(res[0].x, res[1].x, false, false);
  const auto sy = __builtin_amdgcn_permlane32_swap(res[0].y, res[1].y, false, false);
  *(u32x4*)(out + (size_t)(64 * rt + 32 * th + l31) * DM + 64 * ct + 16 * kq + 8 * hh) = (u32x4){sx[0], sy[0], sx[1], sy[1]};
  STAMP(17);
}

__global__ __launch_bounds__(512, 2) void fused(const u16* __restrict__ x, const u16* __restrict__ wqkv, const u16* __restrict__ wo,
                                                const u16* __restrict__ lar, const u16* __restrict__ aim, const u16* __restrict__ cre,
                                                const u16* __restrict__ cim, const u16* __restrict__ ldt, const u16* __restrict__ dsk,
                                                const u16* __restrict__ gw, char* __restrict__ ws, u16* __restrict__ out) {
  extern __shared__ __attribute__((aligned(16))) char smem_[];
  lptr smem = (lptr)smem_;
  STAMP_DECL; STAMP(0);
  const int b = blockIdx.x, xcd = b & 7, slot = b >> 3, idx = slot >> 1;
  const int rt = 8 * (xcd >> 1) + (slot & 7), ct = 4 * (xcd & 1) + (slot >> 3);
  int vzero;
  asm volatile("v_mov_b32 %0, 0" : "=v"(vzero));
  const u64 epoch = __hip_atomic_load((gu64*)(ws + WS_SYNC + vzero), RLX_AGENT);
  if (slot & 1) conv_role(x, lar, aim, cre, cim, ldt, dsk, gw, ws, xcd * 16 + idx, epoch, smem STAMP_PASS);
  else gemm_role(x, wqkv, ws, 4 * (xcd >> 1) + (idx & 3), 4 * (xcd & 1) + (idx >> 2), epoch, smem STAMP_PASS);
  const int lane = threadIdx.x & 63, w = __builtin_amdgcn_readfirstlane(threadIdx.x >> 6);
  const u64 tag = next_tag(epoch);
  SCHED();
  fetch_wo(wo, ct, w, lane, smem);
  WAIT_VM(8);
  STAMP(10);
  LBAR();
  if (threadIdx.x < 32) __hip_atomic_store((gu64*)(ws + WS_FLAG + threadIdx.x * 2048 + (slot & 1) * 1024) + (xcd * 16 + idx), tag, RLX_AGENT);
  if (w == 0) poll_wait(ws, (b & 31) * 2048, lane, tag, poll_load(ws, (b & 31) * 2048, lane));
  RAW_BAR();
  STAMP(11);
  out_role(ws, out, rt, ct, tag, smem STAMP_PASS);
  STAMP_FLUSH(ws);
}

extern "C" void kernel_launch(void* const* d_in, const int* in_sizes, int n_in, void* d_out, int out_size, void* d_ws,
                              size_t ws_size, hipStream_t stream) {
  (void)in_sizes; (void)n_in; (void)out_size; (void)ws_size;
  (void)hipFuncSetAttribute((const void*)fused, hipFuncAttributeMaxDynamicSharedMemorySize, 163840);
  fused<<<256, 512, 163840, stream>>>((const u16*)d_in[0], (const u16*)d_in[1], (const u16*)d_in[2], (const u16*)d_in[4],
                                      (const u16*)d_in[5], (const u16*)d_in[6], (const u16*)d_in[7], (const u16*)d_in[8],
                                      (const u16*)d_in[9], (const u16*)d_in[10], (char*)d_ws, (u16*)d_out);
}
```

```cpp
#include <hip/hip_runtime.h>
#include <hip/hip_bf16.h>
#include <stdint.h>

typedef unsigned int u32;
typedef unsigned short u16;
typedef __attribute__((ext_vector_type(8))) short s16x8;
typedef __attribute__((ext_vector_type(4))) short s16x4;
typedef __attribute__((ext_vector_type(8))) _Float16 h16x8;
typedef __attribute__((ext_vector_type(2))) _Float16 h16x2;
typedef __attribute__((ext_vector_type(2))) __bf16 b16x2;
typedef __attribute__((ext_vector_type(2))) float f32x2;
typedef __attribute__((ext_vector_type(4))) float f32x4;
typedef __attribute__((ext_vector_type(16))) float f32x16;
typedef __attribute__((ext_vector_type(2))) u32 u32x2;
typedef __attribute__((ext_vector_type(4))) u32 u32x4;
#define LDS_AS __attribute__((address_space(3)))
#define DI __device__ __forceinline__

static constexpr int SEQ = 2048, DM = 512;
#define KVC 1
static constexpr size_t WS_KV = 6u << 20;
static constexpr int KV_CP = 36864;
static constexpr size_t WS_G = 65536;
static constexpr size_t WS_PQ = 1u << 20;
static constexpr size_t WS_BT = 4u << 20;
static constexpr size_t WS_SYNC = 49152;
static constexpr size_t WS_FLAG = 131072;
typedef unsigned long long u64;
typedef __attribute__((address_space(1))) u64 gu64;
typedef __attribute__((address_space(1))) u32 gu32;
#define RLX_AGENT __ATOMIC_RELAXED, __HIP_MEMORY_SCOPE_AGENT
__device__ __forceinline__ u64 next_tag(u64 e) { return e * 6364136223846793005ull + 1442695040888963407ull; }

#define STAMP_ARG
#define STAMP_PASS
#define STAMP_DECL do {} while (0)
#define STAMP(i) do {} while (0)
#define CYC(i) do {} while (0)
#define CYCD(i, j) do {} while (0)
#define STAMP_FLUSH(wsp) do {} while (0)
#define POLL_SLEEP 2
#define ST_AUX 16
#define WAIT_VM(n) asm volatile("s_waitcnt vmcnt(" #n ")" ::: "memory")
#define WAIT_L0() __builtin_amdgcn_s_waitcnt(0xC07F)
#define RAW_BAR() __builtin_amdgcn_s_barrier()
#define SCHED() __builtin_amdgcn_sched_barrier(0)
#define LBAR() do { WAIT_L0(); asm volatile("" ::: "memory"); RAW_BAR(); asm volatile("" ::: "memory"); } while (0)
typedef LDS_AS char* lptr;
#define LD(T, p) (*(const LDS_AS T*)(p))
#define ST(T, p) (*(LDS_AS T*)(p))

DI u32 pk_bf16(float a, float b) { return __builtin_bit_cast(u32, __builtin_convertvector((f32x2){a, b}, b16x2)); }
DI u32 pk_f16(f32x2 v) { return __builtin_bit_cast(u32, __builtin_convertvector(v, h16x2)); }
DI float bf_lo(u32 v) { return __builtin_bit_cast(float, v << 16); }
DI float bf_hi(u32 v) { return __builtin_bit_cast(float, v & 0xffff0000u); }
DI float u2f(u32 v) { return __builtin_bit_cast(float, v); }
DI b16x2 u2b(u32 v) { return __builtin_bit_cast(b16x2, v); }
DI float rbf(float v) { return bf_lo(pk_bf16(v, v)); }
static constexpr float LOG2E = 1.44269504088896341f, EPS = 1e-6f;
DI float phi_f(float t) {
  float lo, hi;
  asm("v_min_f32 %0, %1, %2" : "=v"(lo) : "s"(EPS), "v"(t));
  asm("v_max_f32 %0, %1, %2" : "=v"(hi) : "s"(EPS), "v"(t));
  return hi + __builtin_amdgcn_exp2f(__builtin_fmaf(lo, LOG2E, -EPS * LOG2E));
}
DI float exp_f(float v) {
  const float p = v * LOG2E;
  const float r = __builtin_fmaf(v, 1.925963033500011e-8f, __builtin_fmaf(v, LOG2E, -p));
  return __builtin_amdgcn_exp2f(p) * __builtin_fmaf(r, 0.6931471805599453f, 1.f);
}
#define DPP_F(v, ctrl, rmask, bc) __builtin_bit_cast(float, __builtin_amdgcn_update_dpp(0, __builtin_bit_cast(int, v), ctrl, rmask, 0xf, bc))
DI float wave_sum(float r) {
  r += DPP_F(r, 0x111, 0xf, true); r += DPP_F(r, 0x112, 0xf, true); r += DPP_F(r, 0x114, 0xf, true); r += DPP_F(r, 0x118, 0xf, true);
  r += DPP_F(r, 0x142, 0xa, false); r += DPP_F(r, 0x143, 0xc, false);
  return r;
}
DI u32 wave_umax(u32 r) {
#define DPP_U(v, ctrl, rmask, bc) (u32)__builtin_amdgcn_update_dpp(0, (int)(v), ctrl, rmask, 0xf, bc)
  r = max(r, DPP_U(r, 0x111, 0xf, true)); r = max(r, DPP_U(r, 0x112, 0xf, true)); r = max(r, DPP_U(r, 0x114, 0xf, true));
  r = max(r, DPP_U(r, 0x118, 0xf, true)); r = max(r, DPP_U(r, 0x142, 0xa, false)); r = max(r, DPP_U(r, 0x143, 0xc, false));
  return (u32)__builtin_amdgcn_readlane((int)r, 63);
}
DI s16x4 tr_read(lptr p) { return __builtin_amdgcn_ds_read_tr16_b64_v4i16((LDS_AS s16x4*)p); }
DI s16x8 cat8(s16x4 a, s16x4 b) { return __builtin_shufflevector(a, b, 0, 1, 2, 3, 4, 5, 6, 7); }
typedef __amdgpu_buffer_rsrc_t rsrc_t;
DI rsrc_t mk_rsrc(const void* p, int bytes) { return __builtin_amdgcn_make_buffer_rsrc((void*)p, 0, bytes, 0x00020000); }
DI void blds16(rsrc_t r, lptr l, int voff, int soff) { __builtin_amdgcn_raw_ptr_buffer_load_lds(r, (LDS_AS void*)l, 16, voff, soff, 0, 0); }
#define CM_MUL(t, a, w) "v_pk_mul_f32 " t ", " a ", " w " op_sel:[1,1] op_sel_hi:[1,0]\n\t"
#define CM_FMA(r, a, w, t) "v_pk_fma_f32 " r ", " a ", " w ", " t " op_sel_hi:[0,1,1] neg_lo:[0,0,1]\n\t"
DI void cmul2(f32x2 (&r)[2], const f32x2 (&a)[2], const f32x2 (&w)[2]) {
  f32x2 r0, r1;
  asm(CM_MUL("%0", "%2", "%4") CM_MUL("%1", "%3", "%5") CM_FMA("%0", "%2", "%4", "%0") CM_FMA("%1", "%3", "%5", "%1")
      : "=&v"(r0), "=&v"(r1) : "v"(a[0]), "v"(a[1]), "v"(w[0]), "v"(w[1]));
  r[0] = r0; r[1] = r1;
}
DI void cmul4(f32x2 (&r)[2], const f32x2 (&a)[2], const f32x2 (&w)[2], f32x2 (&q)[2], const f32x2 (&b)[2], const f32x2 (&v)[2]) {
  f32x2 r0, r1, q0, q1;
  asm(CM_MUL("%0", "%4", "%6") CM_MUL("%1", "%5", "%7") CM_MUL("%2", "%8", "%10") CM_MUL("%3", "%9", "%11")
      CM_FMA("%0", "%4", "%6", "%0") CM_FMA("%1", "%5", "%7", "%1") CM_FMA("%2", "%8", "%10", "%2") CM_FMA("%3", "%9", "%11", "%3")
      : "=&v"(r0), "=&v"(r1), "=&v"(q0), "=&v"(q1)
      : "v"(a[0]), "v"(a[1]), "v"(w[0]), "v"(w[1]), "v"(b[0]), "v"(b[1]), "v"(v[0]), "v"(v[1]));
  r[0] = r0; r[1] = r1; q[0] = q0; q[1] = q1;
}
DI f32x2 cexp_p(float alpha, float theta, float p) {
  float m = __builtin_amdgcn_exp2f(alpha * p);
  float f = __builtin_amdgcn_fractf(theta * p);
  return (f32x2){m * __builtin_amdgcn_cosf(f), m * __builtin_amdgcn_sinf(f)};
}
#define MFMA16(a, b, c) __builtin_amdgcn_mfma_f32_16x16x32_bf16((a), (b), (c), 0, 0, 0)
#define MFMA16H(a, b, c) __builtin_amdgcn_mfma_f32_16x16x32_f16((a), (b), (c), 0, 0, 0)
#define MFMA32(a, b, c) __builtin_amdgcn_mfma_f32_32x32x16_bf16((a), (b), (c), 0, 0, 0)

static constexpr int STAGE = 40960;
static constexpr int P2_WO = 0, P2_KV = 65536, P2_OK = P2_KV + 17408, P2_BT = P2_KV + 18432, P2_EX = P2_BT + 8192;

DI u32 touch_pages(const char* ws, const u16* out, int rt, int lane) {
  const char* p = lane < 4 ? ws + (size_t)lane * (2u << 20) : (const char*)out + (size_t)rt * 65536;
  return lane < 5 ? *(const u32*)p : 0u;
}
DI void fetch_wo(const u16* __restrict__ wo, int ct, int w, int lane, lptr smem) {
  const rsrc_t rwo = mk_rsrc(wo, DM * DM * 2);
  const int sr = lane >> 3, sp = lane & 7, sc = (sp ^ (((sr >> 1) & 1) << 2)) << 4;
#pragma unroll
  for (int i = 0; i < 8; ++i) blds16(rwo, smem + P2_WO + (64 * w + 8 * i) * 128, sr * 1024 + sc, (64 * w + 8 * i) * 1024 + 128 * ct);
}

DI void gemm_role(const u16* __restrict__ x, const u16* __restrict__ wqkv, const u16* __restrict__ wo, int ct, char* __restrict__ ws, const u16* out, int rt, int tt, int hg, int pq4,
                  u64 epoch, lptr smem STAMP_ARG) {
  const int tid = threadIdx.x, lane = tid & 63;
  const int w = __builtin_amdgcn_readfirstlane(tid >> 6);
  const int wm = w >> 2, wh = w & 3, g = lane >> 4, l15 = lane & 15;
  const int sr = lane >> 3, sp = lane & 7;
  const rsrc_t rx = mk_rsrc(x, SEQ * DM * 2), rw = mk_rsrc(wqkv, DM * 1536 * 2);
  const int xv = sr * 1024 + ((sp ^ sr) << 4), xs0 = (tt * 128 + 16 * w) * 1024;
  const int fw = ((sr >> 1) & 1) | ((w & 1) << 1);
  const int wv = sr * 3072 + ((sp ^ (fw << 1)) << 4), ws0 = 8 * w * 3072 + hg * 128;
  const rsrc_t rwo = mk_rsrc(wo, DM * DM * 2);
  const int ov = sr * 1024 + ((sp ^ (((sr >> 1) & 1) << 2)) << 4);
#define ISS(s, i) do { lptr sb = smem + ((s) & 3) * STAGE; \
    if ((s) >= 8) { if ((i) < 4) blds16(rwo, smem + P2_WO + (64 * w + 32 * ((s) - 8) + 8 * (i)) * 128, ov, (64 * w + 32 * ((s) - 8) + 8 * (i)) * 1024 + 128 * ct); } \
    else if ((i) < 2) blds16(rx, sb + w * 2048 + (i) * 1024, xv, xs0 + (s) * 128 + (i) * 8192); \
    else blds16(rw, sb + 16384 + ((i) - 2) * 8192 + w * 1024, wv, ws0 + (s) * 64 * 3072 + ((i) - 2) * 1024); } while (0)
#define ISSUE(s) do { ISS(s, 0); ISS(s, 1); ISS(s, 2); ISS(s, 3); ISS(s, 4); } while (0)
  gu64* sync_z = (gu64*)(ws + WS_SYNC + 128 + hg * 128);
  ISSUE(0);
  SCHED(); RAW_BAR(); SCHED();
  ISSUE(1);
  u32 pv, pv2 = 0;
  {
    const int q = pq4;
    if (w < 3) pv = __builtin_amdgcn_raw_buffer_load_b32(rx, (32 * (q >> 2) + (lane >> 1)) * 1024 + (lane & 1) * 128, tt * 131072 + 256 + w * 256, 0);
    else { const int j = min((w - 3) * 64 + lane, 287);
      pv = __builtin_amdgcn_raw_buffer_load_b32(rw, (128 + 96 * (q & 3) + j / 3) * 3072 + (j % 3) * 1024, hg * 128, 0); }
  }
  const int xo = (64 * wm + l15) * 128 + ((g ^ (lane & 7)) << 4);
  const int q4 = l15 >> 2, p4 = lane & 3;
  const int wo_ = 16384 + (8 * g + q4) * 128 + ((wh ^ (((q4 >> 1) & 1) | ((g & 1) << 1))) << 5) + 8 * p4;
  f32x4 aq[4], ak[4], av[4];
#pragma unroll
  for (int i = 0; i < 4; ++i) { aq[i] = (f32x4){EPS, EPS, EPS, EPS}; ak[i] = aq[i]; av[i] = (f32x4){0, 0, 0, 0}; }
  s16x8 xa[4], wa[3], xb[4], wb[3];
#define LDWF(wf, s, ks, st) wf[st] = cat8(tr_read(smem + ((s) & 3) * STAGE + wo_ + (st) * 8192 + (ks) * 4096), tr_read(smem + ((s) & 3) * STAGE + wo_ + (st) * 8192 + (ks) * 4096 + 512))
#define LDXF(xf, s, ks, mt) xf[mt] = LD(s16x8, smem + ((s) & 3) * STAGE + ((xo ^ ((ks) * 64)) + (mt) * 2048))
#define LOADF(xf, wf, s, ks) do { LDWF(wf, s, ks, 0); LDWF(wf, s, ks, 1); LDWF(wf, s, ks, 2); \
    LDXF(xf, s, ks, 0); LDXF(xf, s, ks, 1); LDXF(xf, s, ks, 2); LDXF(xf, s, ks, 3); } while (0)
#define MM3(xf, wf, mt) do { aq[mt] = MFMA16(wf[0], xf[mt], aq[mt]); ak[mt] = MFMA16(xf[mt], wf[1], ak[mt]); av[mt] = MFMA16(xf[mt], wf[2], av[mt]); \
    asm volatile("" : "+v"(aq[mt]), "+v"(ak[mt]), "+v"(av[mt])); } while (0)
#define HALF(xf, wf, xn, wn, ls, lk, is) do { SCHED(); MM3(xf, wf, 0); SCHED(); \
    if ((ls) >= 0) { LDWF(wn, ls, lk, 0); LDXF(xn, ls, lk, 0); LDWF(wn, ls, lk, 1); } if ((is) >= 0) ISS(is, 0); SCHED(); MM3(xf, wf, 1); SCHED(); \
    if ((ls) >= 0) { LDWF(wn, ls, lk, 2); LDXF(xn, ls, lk, 1); LDXF(xn, ls, lk, 2); } if ((is) >= 0) ISS(is, 1); SCHED(); MM3(xf, wf, 2); SCHED(); \
    if ((ls) >= 0) LDXF(xn, ls, lk, 3); if ((is) >= 0) { ISS(is, 2); ISS(is, 3); } SCHED(); MM3(xf, wf, 3); SCHED(); \
    if ((is) >= 0) ISS(is, 4); SCHED(); } while (0)
#define STEP(s, vm, i1, i2) do { WAIT_L0(); HALF(xa, wa, xb, wb, s, 1, i1); \
    WAIT_L0(); WAIT_VM(vm); RAW_BAR(); HALF(xb, wb, xa, wa, (s) + 1, 0, i2); } while (0)
  STAMP(1);
  WAIT_VM(6); RAW_BAR();
  STAMP(2);
  LOADF(xa, wa, 0, 0);
  STEP(0, 5, 2, 3);
  STAMP(3);
  STEP(1, 10, 4, 5);
  STEP(2, 10, -1, 6);
  STEP(3, 10, -1, 7);
  STAMP(4); CYC(20);
  if (w == 0) pv2 = touch_pages(ws, out, rt, lane);
  STEP(4, 10, -1, -1);
  u64 ready = __hip_atomic_load(sync_z, RLX_AGENT);
  STEP(5, 5, -1, 8);
  STEP(6, 8, 9, -1);
  STAMP(5); CYC(21); CYCD(20, 21); CYC(21);
  {
    const u64 tag = next_tag(epoch);
#pragma unroll 1
    for (int spin = 0; ready != tag && spin < (1 << 22); ++spin) {
      __builtin_amdgcn_s_sleep(2); ready = __hip_atomic_load(sync_z, RLX_AGENT); __builtin_amdgcn_s_waitcnt(0x0F70);
    }
  }
  WAIT_L0(); HALF(xa, wa, xb, wb, 7, 1, -1); WAIT_L0(); SCHED();
#pragma unroll
  for (int mt = 0; mt < 4; ++mt) aq[mt] = MFMA16(wb[0], xb[mt], aq[mt]);
  asm volatile("" : "+v"(aq[0]), "+v"(aq[1]), "+v"(aq[2]), "+v"(aq[3]));
  SCHED();
#pragma unroll
  for (int mt = 0; mt < 4; ++mt) { ak[mt] = MFMA16(xb[mt], wb[1], ak[mt]); av[mt] = MFMA16(xb[mt], wb[2], av[mt]); }
  asm volatile("" : "+v"(ak[0]), "+v"(ak[1]), "+v"(ak[2]), "+v"(ak[3]), "+v"(av[0]), "+v"(av[1]), "+v"(av[2]), "+v"(av[3]));
  SCHED();
  asm volatile("" :: "v"(pv), "v"(pv2));
  STAMP(6);
#undef ISS
#undef ISSUE
#undef LOADF
#undef MM3
#undef HALF
#undef LDWF
#undef LDXF
#undef STEP
  const int h = 4 * hg + wh;
  const rsrc_t rws = mk_rsrc(ws, 8 << 20);
  const int pqv = (g >> 1) * 512 + l15 * 16 + (g & 1) * 256, pqs = (int)WS_PQ + ((tt * 4 + 2 * wm) * 32 + h) * 1024;
#pragma unroll
  for (int pr = 0; pr < 2; ++pr) {
    const f32x4 q0 = aq[2 * pr], q1 = aq[2 * pr + 1];
    const auto s0 = __builtin_amdgcn_permlane16_swap(pk_bf16(phi_f(q0[0]), phi_f(q0[1])), pk_bf16(phi_f(q1[0]), phi_f(q1[1])), false, false);
    const auto s1 = __builtin_amdgcn_permlane16_swap(pk_bf16(phi_f(q0[2]), phi_f(q0[3])), pk_bf16(phi_f(q1[2]), phi_f(q1[3])), false, false);
    __builtin_amdgcn_raw_buffer_store_b128((u32x4){s0[0], s1[0], s0[1], s1[1]}, rws, pqv, pqs + pr * 32768, ST_AUX);
  }
  SCHED();
  STAMP(7);
  f32x4 kvt = {0, 0, 0, 0}, zz = {0, 0, 0, 0};
  const u32x4 ones_u = {0x3f803f80u, 0x3f803f80u, 0x3f803f80u, 0x3f803f80u};
#pragma unroll
  for (int pr = 0; pr < 2; ++pr) {
    u32x4 pk, vv;
#pragma unroll
    for (int m = 0; m < 2; ++m) {
      const f32x4 kk = ak[2 * pr + m], v4 = av[2 * pr + m];
      pk[2 * m] = pk_bf16(phi_f(kk[0]), phi_f(kk[1])); pk[2 * m + 1] = pk_bf16(phi_f(kk[2]), phi_f(kk[3]));
      vv[2 * m] = pk_bf16(v4[0], v4[1]); vv[2 * m + 1] = pk_bf16(v4[2], v4[3]);
    }
    kvt = MFMA16(__builtin_bit_cast(s16x8, vv), __builtin_bit_cast(s16x8, pk), kvt);
    zz = MFMA16(__builtin_bit_cast(s16x8, ones_u), __builtin_bit_cast(s16x8, pk), zz);
  }
  lptr red = smem + 66560;
#pragma unroll
  for (int r = 0; r < 4; ++r) ST(float, red + (wm * 4 + wh) * 1088 + l15 * 4 + (4 * g + r) * 64) = kvt[r];
  if (g == 0) ST(float, red + (wm * 4 + wh) * 1088 + l15 * 4 + 1024) = zz[0];
  LBAR();
  STAMP(8);
  float* kvacc = (float*)(ws + WS_KV + (tt % KVC) * KV_CP) + hg * 1088 + tid;
  atomicAdd(kvacc, LD(float, red + tid * 4) + LD(float, red + 4352 + tid * 4));
  atomicAdd(kvacc + 512, LD(float, red + 2048 + tid * 4) + LD(float, red + 4352 + 2048 + tid * 4));
  if (tid < 64) atomicAdd(kvacc + 1024, LD(float, red + 4096 + tid * 4) + LD(float, red + 4352 + 4096 + tid * 4));
  STAMP(9); CYC(22); CYCD(21, 22);
}

static constexpr int TB_ROW = 544, TB_BUF = 16 * TB_ROW, TB_CH = 2 * TB_BUF;
static constexpr int KR_OFF = 4 * TB_CH, KR_CP = 4160, KR_CH = 2 * KR_CP;
static constexpr int U_OFF = KR_OFF + 4 * KR_CH, U_CH = (2048 + 240) * 2;
static constexpr int EX_OFF = U_OFF + 4 * U_CH;
static_assert(EX_OFF + 32768 <= 163840 && KR_OFF >= 65536, "LDS map");

DI void conv_role(const u16* __restrict__ x, const u16* __restrict__ lar, const u16* __restrict__ aim, const u16* __restrict__ cre,
                  const u16* __restrict__ cim, const u16* __restrict__ ldt, const u16* __restrict__ dsk, const u16* __restrict__ gw,
                  const u16* __restrict__ wo, char* __restrict__ ws, const u16* out, int rt, int cg, int ct, u64 epoch, lptr smem STAMP_ARG) {
  const int tid = threadIdx.x, lane = tid & 63;
  const int w = __builtin_amdgcn_readfirstlane(tid >> 6);
  const int ch = w & 3, half = w >> 2, g = lane >> 4, l15 = lane & 15;
  const int c = 4 * cg + ch;
  const rsrc_t rx = mk_rsrc(x, SEQ * DM * 2);
  const float dsk_f = bf_lo(dsk[c]);
  const bool clear = (cg & 15) == 0;
  u32x4 gx[4], gwv;
  u32 pv = 0;
  if (w < 4) {
    const int cw = 4 * cg + w;
    const u32 lar2 = *(const u32*)(lar + cw * 128 + 2 * lane), aim2 = *(const u32*)(aim + cw * 128 + 2 * lane);
    const u32 cre2 = *(const u32*)(cre + cw * 128 + 2 * lane), cim2 = *(const u32*)(cim + cw * 128 + 2 * lane);
    const u32 ldt_u = ldt[cw];
    if (lane < 8) ST(u32x4, smem + KR_OFF + w * KR_CH + (lane >> 2) * KR_CP + 4096 + 16 * (lane & 3)) = (u32x4){0u, 0u, 0u, 0u};
    SCHED(); RAW_BAR(); SCHED();
    const float dt = rbf(exp_f(bf_lo(ldt_u)));
    float al[2], th[2];
    al[0] = -rbf(exp_f(bf_lo(lar2))) * dt * LOG2E; al[1] = -rbf(exp_f(bf_hi(lar2))) * dt * LOG2E;
    th[0] = bf_lo(aim2) * dt * 0.15915494309189535f; th[1] = bf_hi(aim2) * dt * 0.15915494309189535f;
    STAMP(1);
    u32 ce = wave_umax(max(max(cre2 & 0x7f80u, (cre2 >> 16) & 0x7f80u), max(cim2 & 0x7f80u, (cim2 >> 16) & 0x7f80u))) >> 7;
    ce = ce < 1 ? 1 : (ce > 253 ? 253 : ce);
    const float cinv = __builtin_bit_cast(float, (254u - ce) << 23);
    const float kscale = 2.f * dt * __builtin_bit_cast(float, ce << 23);
    lptr tb = smem + w * TB_CH + lane * 8, tr = smem + w * TB_CH + 16 * g, kr = smem + KR_OFF + w * KR_CH;
#define PUT(buf, e, v) ST(u32x2, tb + (buf) * TB_BUF + (e) * TB_ROW) = (u32x2){pk_f16(v[0]), pk_f16(v[1])}
#define CONJ(v) ((f32x2){(v).x, -(v).y})
#define GETF(f, buf, row) do { lptr p_ = tr + (buf) * TB_BUF + (row) * TB_ROW; \
      _Pragma("unroll") for (int ks = 0; ks < 8; ++ks) f[ks] = LD(h16x8, p_ + 64 * ks); } while (0)
#define TILE(d, f) do { d = (f32x4){0, 0, 0, 0}; _Pragma("unroll") for (int ks = 0; ks < 8; ++ks) d = MFMA16H(f[ks], fx[ks], d); } while (0)
    f32x2 y[17][2], wB[2], w16[2], w32[2], xa[2], xb[2], aS[6][2], t[2], t2[2];
#pragma unroll
    for (int m = 0; m < 2; ++m) {
      y[1][m] = CONJ(cexp_p(al[m], th[m], 1.f)); w16[m] = cexp_p(al[m], th[m], 16.f); wB[m] = CONJ(cexp_p(al[m], th[m], 256.f));
      y[16][m] = CONJ(w16[m]);
    }
    xa[0] = (f32x2){bf_lo(cre2) * cinv, bf_lo(cim2) * cinv}; xa[1] = (f32x2){bf_hi(cre2) * cinv, bf_hi(cim2) * cinv};
    ST(u32x2, tb + TB_BUF) = (u32x2){0x3c00u, 0x3c00u};
    PUT(0, 0, xa); PUT(1, 1, y[1]);
    cmul4(xb, xa, w16, w32, w16, w16); cmul4(y[2], y[1], y[1], aS[0], wB, wB);
    PUT(0, 1, xb); PUT(1, 2, y[2]);
#pragma unroll
    for (int i = 1; i < 8; ++i) {
      cmul4(xa, xa, w32, xb, xb, w32);
      if (i < 7) cmul4(y[2 * i + 1], y[2 * i - 1], y[2], y[2 * i + 2], y[2 * i], y[2]); else cmul2(y[15], y[13], y[2]);
      PUT(0, 2 * i, xa); PUT(0, 2 * i + 1, xb); PUT(1, 2 * i + 1, y[2 * i + 1]);
      if (i < 7) PUT(1, 2 * i + 2, y[2 * i + 2]);
    }
    STAMP(2);
    h16x8 fx[8], fa[8], fb[8];
    f32x4 kd;
    const int gq = l15 >> 2, r3 = min(l15 & 3, 2), yk = 2044 - 16 * l15 - 4 * g;
    SCHED();
    GETF(fx, 0, l15); GETF(fa, 1, l15);
    SCHED();
    PUT(0, 0, wB);
#pragma unroll
    for (int m = 1; m < 9; m += 2) {
      cmul4(t, wB, y[2 * m], t2, wB, y[2 * m + 2]);
      if (m == 1) cmul4(aS[1], aS[0], wB, aS[2], aS[0], aS[0]);
      if (m == 3) cmul4(aS[3], aS[2], wB, aS[4], aS[2], aS[0]);
      if (m == 5) cmul2(aS[5], aS[4], wB);
      PUT(0, m, t); PUT(0, m + 1, t2);
    }
    SCHED();
    GETF(fb, 0, (int)((0x8766654443222100ull >> (4 * l15)) & 15));
    TILE(kd, fa);
    SCHED();
#pragma unroll
    for (int s = 0; s < 2; ++s) {
      PUT(1, 5 * s, aS[s]);
#pragma unroll
      for (int m = 1; m < 5; m += 2) { cmul4(t, aS[s], y[4 * m], t2, aS[s], y[4 * m + 4]); PUT(1, 5 * s + m, t); PUT(1, 5 * s + m + 1, t2); }
    }
    {
      const f32x4 k0 = kd * kscale;
      const u32 p32 = pk_bf16(k0[3], k0[2]), p10 = pk_bf16(k0[1], k0[0]);
      ST(u32x2, kr + 2 * yk) = (u32x2){p32, p10};
      ST(u16, kr + KR_CP + 2 * yk + 2) = (u16)p32;
      ST(u32, kr + KR_CP + 2 * yk + 4) = pk_bf16(k0[2], k0[1]);
      ST(u16, kr + KR_CP + 2 * yk + 8) = (u16)(p10 >> 16);
    }
    SCHED();
    GETF(fa, 1, 5 * (gq >> 1) + 2 * (gq & 1) + r3);
    TILE(kd, fb);
    SCHED();
#pragma unroll
    for (int s = 2; s < 6; ++s) {
      cmul4(t, aS[s], y[8], t2, aS[s], y[16]);
      PUT(0, 3 * (s - 2), aS[s]); PUT(0, 3 * (s - 2) + 1, t); PUT(0, 3 * (s - 2) + 2, t2);
    }
    {
      const f32x4 k1 = kd * kscale;
      const u32 q10 = pk_bf16(k1[1], k1[0]);
      ST(u32x2, kr + 2 * (yk - 256)) = (u32x2){pk_bf16(k1[3], k1[2]), q10};
      ST(u32x2, kr + KR_CP + 2 * (yk - 256)) = (u32x2){pk_bf16(k1[3], k1[3]), pk_bf16(k1[2], k1[1])};
      if (lane == 0) ST(u16, kr + KR_CP + 2 * 1792) = (u16)q10;
    }
    SCHED();
    GETF(fb, 0, 3 * gq + r3);
    TILE(kd, fa);
    STAMP(3);
    {
      const f32x4 k0 = kd * kscale;
      const int y2 = 2040 - (512 + 256 * (g >> 1) + 16 * l15 + 8 * (g & 1));
      const u32 b2 = pk_bf16(k0[2], k0[2]), b1 = pk_bf16(k0[1], k0[1]), b0 = pk_bf16(k0[0], k0[0]);
      ST(u32x4, kr + 2 * y2) = (u32x4){b2, b1, pk_bf16(k0[1], k0[0]), b0};
      ST(u32x4, kr + KR_CP + 2 * y2) = (u32x4){b2, pk_bf16(k0[2], k0[1]), b1, b0};
    }
    TILE(kd, fb);
    {
      const f32x4 k1 = kd * kscale;
      const int y3 = 2032 - (1024 + 256 * g + 16 * l15);
      const u32 a2 = pk_bf16(k1[2], k1[2]), a1 = pk_bf16(k1[1], k1[1]), a0 = pk_bf16(k1[0], k1[0]);
      ST(u32x4, kr + 2 * y3) = (u32x4){a2, a2, a1, a1};
      ST(u32x4, kr + 2 * y3 + 16) = (u32x4){a1, pk_bf16(k1[1], k1[0]), a0, a0};
      ST(u32x4, kr + KR_CP + 2 * y3) = (u32x4){a2, a2, pk_bf16(k1[2], k1[1]), a1};
      ST(u32x4, kr + KR_CP + 2 * y3 + 16) = (u32x4){a1, a1, a0, a0};
    }
#undef PUT
#undef GETF
#undef TILE
#pragma unroll
    for (int r = 0; r < 4; ++r) gx[r] = __builtin_amdgcn_raw_buffer_load_b128(rx, lane * 16 + 1024 * r, (16 * cg + 4 * w) * 1024, 0);
    gwv = *(const u32x4*)(gw + 8 * lane);
    if (w == 0) pv = touch_pages(ws, out, rt, lane);
    STAMP(4);
  } else {
    const int t2 = (tid + 16 * cg) & 255;
    if (clear) {
      const rsrc_t rk = mk_rsrc(ws, 8 << 20);
      const u32x4 z4 = {0u, 0u, 0u, 0u};
#pragma unroll
      for (int cp = 0; cp < KVC; ++cp) {
        __builtin_amdgcn_raw_buffer_store_b128(z4, rk, t2 * 16, (int)WS_KV + cp * KV_CP + (cg >> 4) * 4352, 16);
        if (t2 < 16) __builtin_amdgcn_raw_buffer_store_b128(z4, rk, t2 * 16, (int)WS_KV + cp * KV_CP + (cg >> 4) * 4352 + 4096, 16);
      }
    }
    SCHED(); RAW_BAR(); SCHED();
    u32x2 xr[8];
#pragma unroll
    for (int r = 0; r < 8; ++r) xr[r] = __builtin_amdgcn_raw_buffer_load_b64(rx, t2 * 4096 + (r & 3) * 1024, 8 * cg + (r >> 2) * (1024 * 1024), 0);
    STAMP(1);
    if (lane < 30) ST(u32x4, smem + U_OFF + (w - 4) * U_CH + lane * 16) = (u32x4){0u, 0u, 0u, 0u};
#pragma unroll
    for (int hf = 0; hf < 2; ++hf) {
      lptr ub = smem + U_OFF + 480 + t2 * 8 + hf * 2048;
      const u32x2 x0 = xr[4 * hf], x1 = xr[4 * hf + 1], x2 = xr[4 * hf + 2], x3 = xr[4 * hf + 3];
      ST(u32x2, ub + 0 * U_CH) = (u32x2){__builtin_amdgcn_perm(x1.x, x0.x, 0x05040100u), __builtin_amdgcn_perm(x3.x, x2.x, 0x05040100u)};
      ST(u32x2, ub + 1 * U_CH) = (u32x2){__builtin_amdgcn_perm(x1.x, x0.x, 0x07060302u), __builtin_amdgcn_perm(x3.x, x2.x, 0x07060302u)};
      ST(u32x2, ub + 2 * U_CH) = (u32x2){__builtin_amdgcn_perm(x1.y, x0.y, 0x05040100u), __builtin_amdgcn_perm(x3.y, x2.y, 0x05040100u)};
      ST(u32x2, ub + 3 * U_CH) = (u32x2){__builtin_amdgcn_perm(x1.y, x0.y, 0x07060302u), __builtin_amdgcn_perm(x3.y, x2.y, 0x07060302u)};
    }
    WAIT_VM(0);
    STAMP(4);
  }
  lptr ka[8];
  {
    const int yb = 2031 - l15 + 8 * g - 128 * half - 96 - 1792;
    lptr k7 = smem + KR_OFF + ch * KR_CH + (yb & 1) * KR_CP + ((yb + 1) >> 1) * 4;
#pragma unroll
    for (int n = 0; n < 8; ++n) { ka[n] = k7 + 512 * (7 - n); asm volatile("" : "+v"(ka[n])); }
  }
  lptr ua = smem + U_OFF + ch * U_CH + (8 * g + 16 * l15 + 224 - 128 * half - 96) * 2;
  STAMP(6);
  LBAR();
  if (clear && tid == 256) __hip_atomic_store((gu64*)(ws + WS_SYNC + 128 + (cg >> 4) * 128), next_tag(epoch), RLX_AGENT);
  if (half) { fetch_wo(wo, ct, 2 * ch, lane, smem); fetch_wo(wo, ct, 2 * ch + 1, lane, smem); }
  STAMP(7); CYC(20);
  f32x4 acc[8];
#pragma unroll
  for (int i = 0; i < 8; ++i) acc[i] = (f32x4){0, 0, 0, 0};
  {
    u32x4 A0[8], A1[8]; s16x8 B0[8], B1[8];
#define LOADC(A, B, rr) do { _Pragma("unroll") for (int n = 0; n < 8; ++n) { \
      lptr kp = ka[n] + 64 * (3 - (rr)); \
      A[n] = (u32x4){LD(u32, kp), LD(u32, kp + 4), LD(u32, kp + 8), LD(u32, kp + 12)}; \
      B[n] = LD(s16x8, ua + 64 * (3 - (rr)) + 512 * n); } } while (0)
#define MMC(A, B) do { _Pragma("unroll") for (int nu = 0; nu < 8; ++nu) _Pragma("unroll") for (int mu = 0; mu < 8 - nu; ++mu) \
      acc[nu + mu] = MFMA16(__builtin_bit_cast(s16x8, A[nu]), B[mu], acc[nu + mu]); } while (0)
#define PINC() do { asm volatile("" : "+v"(acc[0]), "+v"(acc[1]), "+v"(acc[2]), "+v"(acc[3]), "+v"(acc[4]), "+v"(acc[5]), "+v"(acc[6]), "+v"(acc[7])); SCHED(); } while (0)
#define MIXC() do { _Pragma("unroll") for (int q = 0; q < 12; ++q) { __builtin_amdgcn_sched_group_barrier(0x8, 3, 0); __builtin_amdgcn_sched_group_barrier(0x100, 2, 0); } } while (0)
    LOADC(A0, B0, 0);
    SCHED();
    WAIT_L0(); SCHED(); LOADC(A1, B1, 1); MMC(A0, B0); MIXC(); PINC();
    WAIT_L0(); SCHED(); LOADC(A0, B0, 2); MMC(A1, B1); MIXC(); PINC();
    WAIT_L0(); SCHED(); LOADC(A1, B1, 3); MMC(A0, B0); MIXC(); PINC();
    WAIT_L0(); SCHED(); MMC(A1, B1); PINC();
#undef LOADC
#undef MMC
  }
  STAMP(8); CYC(21); CYCD(20, 21);
  {
    lptr ex = smem + EX_OFF + w * 4096 + lane * 16;
    f32x4 keep[4];
    if (half) {
#pragma unroll
      for (int i = 0; i < 4; ++i) { ST(f32x4, ex + 1024 * i) = acc[i]; keep[i] = acc[4 + i]; }
    } else {
#pragma unroll
      for (int i = 0; i < 4; ++i) { ST(f32x4, ex + 1024 * i) = acc[4 + i]; keep[i] = acc[i]; }
    }
    const int t0 = 1024 * half + 16 * l15 + 4 * g;
    const rsrc_t rws = mk_rsrc(ws, 8 << 20);
    lptr up = smem + U_OFF + ch * U_CH + 480 + 2 * t0;
#pragma unroll
    for (int i = 0; i < 4; ++i) {
      const u32x2 uu = LD(u32x2, up + 512 * i);
      keep[i] += (f32x4){bf_lo(uu.x), bf_hi(uu.x), bf_lo(uu.y), bf_hi(uu.y)} * dsk_f;
    }
    asm volatile("" :: "v"(pv));
    if (w < 4) {
      float sg[4];
#pragma unroll
      for (int r = 0; r < 4; ++r) {
        float s0 = 0.f;
#pragma unroll
        for (int i = 0; i < 4; ++i) s0 = __builtin_amdgcn_fdot2_f32_bf16(u2b(gx[r][i]), u2b(gwv[i]), s0, false);
        sg[r] = rbf(__builtin_amdgcn_rcpf(1.f + __builtin_amdgcn_exp2f(-LOG2E * rbf(wave_sum(s0)))));
      }
      if (lane == 63) __builtin_amdgcn_raw_buffer_store_b128(__builtin_bit_cast(u32x4, (f32x4){sg[0], sg[1], sg[2], sg[3]}), rws, 0, (int)WS_G + (16 * cg + 4 * w) * 4, ST_AUX);
    }
    LBAR();
    lptr ei = smem + EX_OFF + (w ^ 4) * 4096 + lane * 16;
    u32x2 yb[4];
#pragma unroll
    for (int i = 0; i < 4; ++i) {
      const f32x4 y = keep[i] + LD(f32x4, ei + 1024 * i);
      yb[i] = (u32x2){pk_bf16(y[0], y[1]), pk_bf16(y[2], y[3])};
    }
#pragma unroll
    for (int i = 0; i < 2; ++i) {
      const auto sx = __builtin_amdgcn_permlane16_swap(yb[2 * i].x, yb[2 * i + 1].x, false, false);
      const auto sy = __builtin_amdgcn_permlane16_swap(yb[2 * i].y, yb[2 * i + 1].y, false, false);
      __builtin_amdgcn_raw_buffer_store_b128((u32x4){sx[0], sy[0], sx[1], sy[1]}, rws, 2048 * half + 32 * l15 + 16 * (g >> 1) + 512 * (g & 1) + 1024 * i,
                                             (int)WS_BT + c * 4096, ST_AUX);
    }
  }
  STAMP(9);
}

struct Tags { u64 a, b; };
DI Tags poll_load(char* ws, int off, int lane) {
  gu64* p = (gu64*)(ws + WS_FLAG + off) + lane;
  return Tags{__hip_atomic_load(p, RLX_AGENT), __hip_atomic_load(p + 64, RLX_AGENT)};
}
DI void poll_wait(char* ws, int off, int lane, u64 tag, Tags& f, u64 ma, u64 mb) {
#pragma unroll 1
  for (int spin = 0; spin < (1 << 20); ++spin) {
    if ((__ballot(f.a != tag) & ma) == 0 && (__ballot(f.b != tag) & mb) == 0) break;
    __builtin_amdgcn_s_sleep(POLL_SLEEP);
    f = poll_load(ws, off, lane);
    __builtin_amdgcn_s_waitcnt(0x0F70);
  }
}

DI void out_role(char* __restrict__ ws, u16* __restrict__ out, int rt, int ct, u64 tag, lptr smem STAMP_ARG) {
  const int tid = threadIdx.x, lane = tid & 63;
  const int w = __builtin_amdgcn_readfirstlane(tid >> 6);
  const int th = w & 1, kq = w >> 1, hh = lane >> 5, l31 = lane & 31;
  const rsrc_t rws = mk_rsrc(ws, 8 << 20);
  const int foff = (blockIdx.x & 31) * 2048;
  Tags fl = {0, 0};
  if (w == 0) { fl = poll_load(ws, foff, lane); poll_wait(ws, foff, lane, tag, fl, ~0ull, ~0ull); }
  RAW_BAR();
  STAMP(11);
  if (w == 1) fl = poll_load(ws, foff + 1024, lane);
  f32x4 kvr[KVC][5];
#pragma unroll
  for (int cp = 0; cp < KVC; ++cp) {
#pragma unroll
    for (int i = 0; i < 4; ++i) kvr[cp][i] = __builtin_bit_cast(f32x4, __builtin_amdgcn_raw_buffer_load_b128(rws, tid * 16, (int)WS_KV + cp * KV_CP + 8192 * i, 16));
    kvr[cp][4] = __builtin_bit_cast(f32x4, __builtin_amdgcn_raw_buffer_load_b128(rws, (tid & 127) * 16, (int)WS_KV + cp * KV_CP + 32768, 16));
  }
  SCHED(); RAW_BAR(); SCHED();
  u32x4 pf[8];
#define LOAD_PQ(i0, i1) do { _Pragma("unroll") for (int i = i0; i < i1; ++i) \
    pf[i] = __builtin_amdgcn_raw_buffer_load_b128(rws, lane * 16, (int)WS_PQ + ((2 * rt + th) * 32 + 8 * kq + i) * 1024, 16); } while (0)
  LOAD_PQ(0, 3);
  STAMP(12);
#pragma unroll
  for (int i = 0; i < 5; ++i) {
    f32x4 v = kvr[0][i];
#pragma unroll
    for (int cp = 1; cp < KVC; ++cp) v += kvr[cp][i];
    ST(u32x2, smem + P2_KV + (i < 4 ? tid + 512 * i : 2048 + (tid & 127)) * 8) = (u32x2){pk_bf16(v[0], v[1]), pk_bf16(v[2], v[3])};
  }
  f32x16 acc[2];
#pragma unroll
  for (int i = 0; i < 16; ++i) { acc[0][i] = 0.f; acc[1][i] = 0.f; }
  f32x16 cinit = acc[0];
  cinit[8] = EPS;
  lptr kva = smem + P2_KV + 8 * kq * 544 + min(l31, 16) * 32 + hh * 16;
  const int blk = (lane >> 4) & 1, q4 = (lane & 15) >> 2, p4 = lane & 3;
  const int sw = ((q4 >> 1) & 1) << 6;
  lptr wa0 = smem + P2_WO + (128 * kq + 4 * hh + q4) * 128 + blk * 32 + p4 * 8 + sw;
  lptr wa1 = smem + P2_WO + (128 * kq + 4 * hh + q4) * 128 + blk * 32 + p4 * 8 + (64 ^ sw);
  STAMP(13);
  if (w == 1) { const u32 ok = __all(fl.a == tag && fl.b == tag); if (lane == 0) ST(u32, smem + P2_OK) = ok; }
  LBAR();
  LOAD_PQ(3, 8);
#undef LOAD_PQ
  const bool early = __builtin_amdgcn_readfirstlane(LD(u32, smem + P2_OK)) != 0;
  u32x4 btr; float gt;
#define LOAD_B() do { \
    btr = __builtin_amdgcn_raw_buffer_load_b128(rws, (lane >> 2) * 4096 + (lane & 3) * 16, (int)WS_BT + (64 * ct + 16 * kq) * 4096 + 128 * rt + 64 * th, 16); \
    gt = u2f(__builtin_amdgcn_raw_buffer_load_b32(rws, l31 * 4, (int)WS_G + (64 * rt + 32 * th) * 4, 16)); } while (0)
  if (early) LOAD_B();
  STAMP(14);
  {
    f32x16 d[2]; s16x8 kf[8], wf[2][2]; u32x4 of[2];
#define OMM(i) do { d[(i) & 1] = MFMA32(kf[i], __builtin_bit_cast(s16x8, pf[i]), cinit); asm volatile("" : "+v"(d[(i) & 1])); } while (0)
#define NORM(i) do { const f32x16& dd = d[(i) & 1]; const float inv = __builtin_amdgcn_rcpf(dd[8]); \
      of[(i) & 1] = (u32x4){pk_bf16(dd[0] * inv, dd[1] * inv), pk_bf16(dd[2] * inv, dd[3] * inv), pk_bf16(dd[4] * inv, dd[5] * inv), pk_bf16(dd[6] * inv, dd[7] * inv)}; \
      asm volatile("" : "+v"(of[(i) & 1]) : "v"(dd)); } while (0)
#define LDW(i) do { wf[(i) & 1][0] = cat8(tr_read(wa0 + (i) * 2048), tr_read(wa0 + (i) * 2048 + 1024)); \
      wf[(i) & 1][1] = cat8(tr_read(wa1 + (i) * 2048), tr_read(wa1 + (i) * 2048 + 1024)); } while (0)
#define AMM(i) do { acc[0] = MFMA32(wf[(i) & 1][0], __builtin_bit_cast(s16x8, of[(i) & 1]), acc[0]); \
      acc[1] = MFMA32(wf[(i) & 1][1], __builtin_bit_cast(s16x8, of[(i) & 1]), acc[1]); asm volatile("" : "+v"(acc[0]), "+v"(acc[1])); } while (0)
#pragma unroll
    for (int i = 0; i < 8; ++i) kf[i] = LD(s16x8, kva + i * 544);
    LDW(0);
    SCHED();
    OMM(0); OMM(1); SCHED(); NORM(0); SCHED();
#pragma unroll
    for (int i = 0; i < 8; ++i) {
      if (i + 1 < 8) LDW(i + 1);
      SCHED();
      AMM(i);
      if (i + 2 < 8) OMM(i + 2);
      SCHED();
      if (i + 1 < 8) NORM(i + 1);
      SCHED();
    }
#undef OMM
#undef NORM
#undef LDW
#undef AMM
  }
  STAMP(15);
#pragma unroll
  for (int q = 0; q < 4; ++q) {
    lptr ex = smem + P2_EX + ((2 * q + th) * 4 + kq) * 2048 + lane * 16;
    const f32x16& s = acc[q >> 1];
    ST(f32x4, ex) = (f32x4){s[8 * (q & 1)], s[8 * (q & 1) + 1], s[8 * (q & 1) + 2], s[8 * (q & 1) + 3]};
    ST(f32x4, ex + 1024) = (f32x4){s[8 * (q & 1) + 4], s[8 * (q & 1) + 5], s[8 * (q & 1) + 6], s[8 * (q & 1) + 7]};
  }
  if (!early && w == 1) poll_wait(ws, foff + 1024, lane, tag, fl, ~0ull, ~0ull);
  LBAR();
  if (!early) LOAD_B();
#undef LOAD_B
  if (blockIdx.x == 0 && tid == 0) __hip_atomic_store((gu64*)(ws + WS_SYNC), tag, RLX_AGENT);
  STAMP(16);
  lptr ei = smem + P2_EX + w * 8192 + lane * 16;
  ST(u32x4, smem + P2_BT + w * 1024 + lane * 16) = btr;
  const float g1 = rbf(1.f - gt);
  lptr ba = smem + P2_BT + w * 1024 + (4 * hh + q4) * 64 + blk * 32 + p4 * 8;
  u32x2 res[2];
#pragma unroll
  for (int i = 0; i < 2; ++i) {
    const f32x4 o = (LD(f32x4, ei + 1024 * i) + LD(f32x4, ei + 2048 + 1024 * i)) + (LD(f32x4, ei + 4096 + 1024 * i) + LD(f32x4, ei + 6144 + 1024 * i));
    const u32x2 bb = __builtin_bit_cast(u32x2, tr_read(ba + i * 512));
    res[i] = (u32x2){pk_bf16(gt * o[0] + g1 * bf_lo(bb.x), gt * o[1] + g1 * bf_hi(bb.x)), pk_bf16(gt * o[2] + g1 * bf_lo(bb.y), gt * o[3] + g1 * bf_hi(bb.y))};
  }
  const auto sx = __builtin_amdgcn_permlane32_swap(res[0].x, res[1].x, false, false);
  const auto sy = __builtin_amdgcn_permlane32_swap(res[0].y, res[1].y, false, false);
  *(u32x4*)(out + (size_t)(64 * rt + 32 * th + l31) * DM + 64 * ct + 16 * kq + 8 * hh) = (u32x4){sx[0], sy[0], sx[1], sy[1]};
  STAMP(17);
}

__global__ __launch_bounds__(512, 2) void fused(const u16* __restrict__ x, const u16* __restrict__ wqkv, const u16* __restrict__ wo,
                                                const u16* __restrict__ lar, const u16* __restrict__ aim, const u16* __restrict__ cre,
                                                const u16* __restrict__ cim, const u16* __restrict__ ldt, const u16* __restrict__ dsk,
                                                const u16* __restrict__ gw, char* __restrict__ ws, u16* __restrict__ out) {
  extern __shared__ __attribute__((aligned(16))) char smem_[];
  lptr smem = (lptr)smem_;
  STAMP_DECL; STAMP(0);
  const int b = blockIdx.x, xcd = b & 7, slot = b >> 3, idx = slot >> 1;
  const int rt = 8 * (xcd >> 1) + (slot & 7), ct = 4 * (xcd & 1) + (slot >> 3);
  const int tt = 4 * (xcd >> 1) + (idx & 3), hg = 4 * (xcd & 1) + (idx >> 2);
  int vzero;
  asm volatile("v_mov_b32 %0, 0" : "=v"(vzero));
  const u64 epoch = __hip_atomic_load((gu64*)(ws + WS_SYNC + vzero), RLX_AGENT);
  if (slot & 1) conv_role(x, lar, aim, cre, cim, ldt, dsk, gw, wo, ws, out, rt, xcd * 16 + idx, ct, epoch, smem STAMP_PASS);
  else gemm_role(x, wqkv, wo, ct, ws, out, rt, tt, hg, idx, epoch, smem STAMP_PASS);
  const int lane = threadIdx.x & 63, w = __builtin_amdgcn_readfirstlane(threadIdx.x >> 6);
  const u64 tag = next_tag(epoch);
  SCHED();
  WAIT_VM(0);
  STAMP(10);
  LBAR();
  if (threadIdx.x < 32) __hip_atomic_store((gu64*)(ws + WS_FLAG + threadIdx.x * 2048) + ((slot & 1) ? 128 + xcd * 16 + idx : tt * 8 + hg), tag, RLX_AGENT);
  out_role(ws, out, rt, ct, tag, smem STAMP_PASS);
  STAMP_FLUSH(ws);
}

extern "C" void kernel_launch(void* const* d_in, const int* in_sizes, int n_in, void* d_out, int out_size, void* d_ws,
                              size_t ws_size, hipStream_t stream) {
  (void)in_sizes; (void)n_in; (void)out_size; (void)ws_size;
  (void)hipFuncSetAttribute((const void*)fused, hipFuncAttributeMaxDynamicSharedMemorySize, 163840);
  fused<<<256, 512, 163840, stream>>>((const u16*)d_in[0], (const u16*)d_in[1], (const u16*)d_in[2], (const u16*)d_in[4],
                                      (const u16*)d_in[5], (const u16*)d_in[6], (const u16*)d_in[7], (const u16*)d_in[8],
                                      (const u16*)d_in[9], (const u16*)d_in[10], (char*)d_ws, (u16*)d_out);
}
```

```cpp
#include <hip/hip_runtime.h>
#include <hip/hip_bf16.h>
#include <stdint.h>

typedef unsigned int u32;
typedef unsigned short u16;
typedef __attribute__((ext_vector_type(8))) short s16x8;
typedef __attribute__((ext_vector_type(4))) short s16x4;
typedef __attribute__((ext_vector_type(8))) _Float16 h16x8;
typedef __attribute__((ext_vector_type(2))) _Float16 h16x2;
typedef __attribute__((ext_vector_type(2))) __bf16 b16x2;
typedef __attribute__((ext_vector_type(2))) float f32x2;
typedef __attribute__((ext_vector_type(4))) float f32x4;
typedef __attribute__((ext_vector_type(16))) float f32x16;
typedef __attribute__((ext_vector_type(2))) u32 u32x2;
typedef __attribute__((ext_vector_type(4))) u32 u32x4;
#define LDS_AS __attribute__((address_space(3)))
#define DI __device__ __forceinline__

static constexpr int SEQ = 2048, DM = 512;
#define KVC 1
static constexpr size_t WS_KV = (6u << 20) + 65536;
static constexpr int KV_CP = 36864;
static constexpr size_t WS_G = 65536;
static constexpr size_t WS_PQ = 1u << 20;
static constexpr size_t WS_BT = (4u << 20) + 4096;
static constexpr size_t WS_SYNC = 49152;
static constexpr size_t WS_FLAG = 131072;
typedef unsigned long long u64;
typedef __attribute__((address_space(1))) u64 gu64;
typedef __attribute__((address_space(1))) u32 gu32;
#define RLX_AGENT __ATOMIC_RELAXED, __HIP_MEMORY_SCOPE_AGENT
__device__ __forceinline__ u64 next_tag(u64 e) { return e * 6364136223846793005ull + 1442695040888963407ull; }

#define STAMP_ARG
#define STAMP_PASS
#define STAMP_DECL do {} while (0)
#define STAMP(i) do {} while (0)
#define CYC(i) do {} while (0)
#define CYCD(i, j) do {} while (0)
#define STAMP_FLUSH(wsp) do {} while (0)
#define POLL_SLEEP 2
#define ST_AUX 16
#define WAIT_VM(n) asm volatile("s_waitcnt vmcnt(" #n ")" ::: "memory")
#define WAIT_L0() __builtin_amdgcn_s_waitcnt(0xC07F)
#define RAW_BAR() __builtin_amdgcn_s_barrier()
#define SCHED() __builtin_amdgcn_sched_barrier(0)
#define LBAR() do { WAIT_L0(); asm volatile("" ::: "memory"); RAW_BAR(); asm volatile("" ::: "memory"); } while (0)
typedef LDS_AS char* lptr;
#define LD(T, p) (*(const LDS_AS T*)(p))
#define ST(T, p) (*(LDS_AS T*)(p))

DI u32 pk_bf16(float a, float b) { return __builtin_bit_cast(u32, __builtin_convertvector((f32x2){a, b}, b16x2)); }
DI u32 pk_f16(f32x2 v) { return __builtin_bit_cast(u32, __builtin_convertvector(v, h16x2)); }
DI float bf_lo(u32 v) { return __builtin_bit_cast(float, v << 16); }
DI float bf_hi(u32 v) { return __builtin_bit_cast(float, v & 0xffff0000u); }
DI float u2f(u32 v) { return __builtin_bit_cast(float, v); }
DI b16x2 u2b(u32 v) { return __builtin_bit_cast(b16x2, v); }
DI float rbf(float v) { return bf_lo(pk_bf16(v, v)); }
static constexpr float LOG2E = 1.44269504088896341f, EPS = 1e-6f;
DI float phi_f(float t) {
  const int ti = __builtin_bit_cast(int, t), ei = __builtin_bit_cast(int, EPS);
  const float lo = __builtin_bit_cast(float, min(ti, ei)), hi = __builtin_bit_cast(float, max(ti, ei));
  return hi + __builtin_amdgcn_exp2f(__builtin_fmaf(lo, LOG2E, -EPS * LOG2E));
}
DI float exp_f(float v) {
  const float p = v * LOG2E;
  const float r = __builtin_fmaf(v, 1.925963033500011e-8f, __builtin_fmaf(v, LOG2E, -p));
  return __builtin_amdgcn_exp2f(p) * __builtin_fmaf(r, 0.6931471805599453f, 1.f);
}
#define DPP_F(v, ctrl, rmask, bc) __builtin_bit_cast(float, __builtin_amdgcn_update_dpp(0, __builtin_bit_cast(int, v), ctrl, rmask, 0xf, bc))
DI float wave_sum(float r) {
  r += DPP_F(r, 0x111, 0xf, true); r += DPP_F(r, 0x112, 0xf, true); r += DPP_F(r, 0x114, 0xf, true); r += DPP_F(r, 0x118, 0xf, true);
  r += DPP_F(r, 0x142, 0xa, false); r += DPP_F(r, 0x143, 0xc, false);
  return r;
}
DI u32 wave_umax(u32 r) {
#define DPP_U(v, ctrl, rmask, bc) (u32)__builtin_amdgcn_update_dpp(0, (int)(v), ctrl, rmask, 0xf, bc)
  r = max(r, DPP_U(r, 0x111, 0xf, true)); r = max(r, DPP_U(r, 0x112, 0xf, true)); r = max(r, DPP_U(r, 0x114, 0xf, true));
  r = max(r, DPP_U(r, 0x118, 0xf, true)); r = max(r, DPP_U(r, 0x142, 0xa, false)); r = max(r, DPP_U(r, 0x143, 0xc, false));
  return (u32)__builtin_amdgcn_readlane((int)r, 63);
}
DI s16x4 tr_read(lptr p) { return __builtin_amdgcn_ds_read_tr16_b64_v4i16((LDS_AS s16x4*)p); }
DI s16x8 cat8(s16x4 a, s16x4 b) { return __builtin_shufflevector(a, b, 0, 1, 2, 3, 4, 5, 6, 7); }
typedef __amdgpu_buffer_rsrc_t rsrc_t;
DI rsrc_t mk_rsrc(const void* p, int bytes) { return __builtin_amdgcn_make_buffer_rsrc((void*)p, 0, bytes, 0x00020000); }
DI void store16(u32x4 v, rsrc_t r, int off) { __builtin_amdgcn_raw_buffer_store_b128(v, r, off, 0, ST_AUX); }
DI void blds16(rsrc_t r, lptr l, int voff, int soff) { __builtin_amdgcn_raw_ptr_buffer_load_lds(r, (LDS_AS void*)l, 16, voff, soff, 0, 0); }
#define CM_MUL(t, a, w) "v_pk_mul_f32 " t ", " a ", " w " op_sel:[1,1] op_sel_hi:[1,0]\n\t"
#define CM_FMA(r, a, w, t) "v_pk_fma_f32 " r ", " a ", " w ", " t " op_sel_hi:[0,1,1] neg_lo:[0,0,1]\n\t"
DI void cmul2(f32x2 (&r)[2], const f32x2 (&a)[2], const f32x2 (&w)[2]) {
  f32x2 r0, r1;
  asm(CM_MUL("%0", "%2", "%4") CM_MUL("%1", "%3", "%5") CM_FMA("%0", "%2", "%4", "%0") CM_FMA("%1", "%3", "%5", "%1")
      : "=&v"(r0), "=&v"(r1) : "v"(a[0]), "v"(a[1]), "v"(w[0]), "v"(w[1]));
  r[0] = r0; r[1] = r1;
}
DI void cmul4(f32x2 (&r)[2], const f32x2 (&a)[2], const f32x2 (&w)[2], f32x2 (&q)[2], const f32x2 (&b)[2], const f32x2 (&v)[2]) {
  f32x2 r0, r1, q0, q1;
  asm(CM_MUL("%0", "%4", "%6") CM_MUL("%1", "%5", "%7") CM_MUL("%2", "%8", "%10") CM_MUL("%3", "%9", "%11")
      CM_FMA("%0", "%4", "%6", "%0") CM_FMA("%1", "%5", "%7", "%1") CM_FMA("%2", "%8", "%10", "%2") CM_FMA("%3", "%9", "%11", "%3")
      : "=&v"(r0), "=&v"(r1), "=&v"(q0), "=&v"(q1)
      : "v"(a[0]), "v"(a[1]), "v"(w[0]), "v"(w[1]), "v"(b[0]), "v"(b[1]), "v"(v[0]), "v"(v[1]));
  r[0] = r0; r[1] = r1; q[0] = q0; q[1] = q1;
}
DI f32x2 cexp_p(float alpha, float theta, float p) {
  float m = __builtin_amdgcn_exp2f(alpha * p);
  float f = __builtin_amdgcn_fractf(theta * p);
  return (f32x2){m * __builtin_amdgcn_cosf(f), m * __builtin_amdgcn_sinf(f)};
}
#define MFMA16(a, b, c) __builtin_amdgcn_mfma_f32_16x16x32_bf16((a), (b), (c), 0, 0, 0)
#define MFMA16H(a, b, c) __builtin_amdgcn_mfma_f32_16x16x32_f16((a), (b), (c), 0, 0, 0)
#define MFMA32(a, b, c) __builtin_amdgcn_mfma_f32_32x32x16_bf16((a), (b), (c), 0, 0, 0)

static constexpr int STAGE = 40960;
static constexpr int P2_WO = 0, P2_KV = 65536, P2_OK = P2_KV + 17408, P2_BT = P2_KV + 18432, P2_EX = P2_BT + 8192;

DI u64 load_epoch(char* ws) {
  int vzero;
  asm volatile("v_mov_b32 %0, 0" : "=v"(vzero));
  return __hip_atomic_load((gu64*)(ws + WS_SYNC + vzero), RLX_AGENT);
}
DI u32 touch_pages(const char* ws, const u16* out, int rt, int lane) {
  const char* p = lane < 4 ? ws + (size_t)((0xf871u >> (4 * lane)) & 15) * (512u << 10) : (const char*)out + (size_t)rt * 65536;
  return lane < 5 ? *(const u32*)p : 0u;
}
DI void fetch_wo(const u16* __restrict__ wo, int ct, int w, int lane, lptr smem) {
  const rsrc_t rwo = mk_rsrc(wo, DM * DM * 2);
  const int sr = lane >> 3, sp = lane & 7, sc = (sp ^ (((sr >> 1) & 1) << 2)) << 4;
#pragma unroll
  for (int i = 0; i < 8; ++i) blds16(rwo, smem + P2_WO + (64 * w + 8 * i) * 128, sr * 1024 + sc, (64 * w + 8 * i) * 1024 + 128 * ct);
}

DI void gemm_role(const u16* __restrict__ x, const u16* __restrict__ wqkv, const u16* __restrict__ wo, int ct, char* __restrict__ ws, const u16* out, int rt, int tt, int hg, int pq4,
                  u64& epoch, lptr smem STAMP_ARG) {
  const int tid = threadIdx.x, lane = tid & 63;
  const int w = __builtin_amdgcn_readfirstlane(tid >> 6);
  const int wm = w >> 2, wh = w & 3, g = lane >> 4, l15 = lane & 15;
  const int sr = lane >> 3, sp = lane & 7;
  const rsrc_t rx = mk_rsrc(x, SEQ * DM * 2), rw = mk_rsrc(wqkv, DM * 1536 * 2);
  const int xv = sr * 1024 + ((sp ^ sr) << 4), xs0 = (tt * 128 + 16 * w) * 1024;
  const int fw = ((sr >> 1) & 1) | ((w & 1) << 1);
  const int wv = sr * 3072 + ((sp ^ (fw << 1)) << 4), ws0 = 8 * w * 3072 + hg * 128;
  const int ROT = tt;
  const rsrc_t rwo = mk_rsrc(wo, DM * DM * 2);
  const int ov = sr * 1024 + ((sp ^ (((sr >> 1) & 1) << 2)) << 4);
#define ISS(s, i) do { lptr sb = smem + ((s) & 3) * STAGE; \
    if ((s) >= 8) { if ((i) < 4) blds16(rwo, smem + P2_WO + (64 * w + 32 * ((s) - 8) + 8 * (i)) * 128, ov, (64 * w + 32 * ((s) - 8) + 8 * (i)) * 1024 + 128 * ct); } \
    else if ((i) < 2) blds16(rx, sb + w * 2048 + (i) * 1024, xv, xs0 + (((s) + ROT) & 7) * 128 + (i) * 8192); \
    else blds16(rw, sb + 16384 + ((i) - 2) * 8192 + w * 1024, wv, ws0 + (((s) + ROT) & 7) * 64 * 3072 + ((i) - 2) * 1024); } while (0)
#define ISSUE(s) do { ISS(s, 0); ISS(s, 1); ISS(s, 2); ISS(s, 3); ISS(s, 4); } while (0)
  gu64* sync_z = (gu64*)(ws + WS_SYNC + 128 + hg * 128);
  ISSUE(0);
  SCHED(); RAW_BAR(); SCHED();
  u32 pv2 = 0;
  {
    const int q = pq4;
    const int jx = w * 32 + (lane & 31);
    const int jw = min(w * 48 + lane, 383);
    __builtin_amdgcn_raw_ptr_buffer_load_lds(rw, (LDS_AS void*)(smem + 3 * STAGE + w * 2048), 4,
                                             (128 * (q & 3) + jw / 3) * 3072 + (jw % 3) * 1024, hg * 128, 0, 0);
    __builtin_amdgcn_raw_ptr_buffer_load_lds(rx, (LDS_AS void*)(smem + 3 * STAGE + w * 2048 + 256), 4,
                                             32 * (q >> 2) * 1024 + jx * 128, tt * 131072, 0, 0);
  }
  SCHED();
  ISSUE(1);
  SCHED();
  epoch = load_epoch(ws);
  const int xo = (64 * wm + l15) * 128 + ((g ^ (lane & 7)) << 4);
  const int q4 = l15 >> 2, p4 = lane & 3;
  const int wo_ = 16384 + (8 * g + q4) * 128 + ((wh ^ (((q4 >> 1) & 1) | ((g & 1) << 1))) << 5) + 8 * p4;
  f32x4 aq[4], ak[4], av[4];
#pragma unroll
  for (int i = 0; i < 4; ++i) { aq[i] = (f32x4){EPS, EPS, EPS, EPS}; ak[i] = aq[i]; av[i] = (f32x4){0, 0, 0, 0}; }
  s16x8 xa[4], wa[3], xb[4], wb[3];
#define LDWF(wf, s, ks, st) wf[st] = cat8(tr_read(smem + ((s) & 3) * STAGE + wo_ + (st) * 8192 + (ks) * 4096), tr_read(smem + ((s) & 3) * STAGE + wo_ + (st) * 8192 + (ks) * 4096 + 512))
#define LDXF(xf, s, ks, mt) xf[mt] = LD(s16x8, smem + ((s) & 3) * STAGE + ((xo ^ ((ks) * 64)) + (mt) * 2048))
#define LOADF(xf, wf, s, ks) do { LDWF(wf, s, ks, 0); LDWF(wf, s, ks, 1); LDWF(wf, s, ks, 2); \
    LDXF(xf, s, ks, 0); LDXF(xf, s, ks, 1); LDXF(xf, s, ks, 2); LDXF(xf, s, ks, 3); } while (0)
#define MM3(xf, wf, mt) do { aq[mt] = MFMA16(wf[0], xf[mt], aq[mt]); ak[mt] = MFMA16(xf[mt], wf[1], ak[mt]); av[mt] = MFMA16(xf[mt], wf[2], av[mt]); \
    asm volatile("" : "+v"(aq[mt]), "+v"(ak[mt]), "+v"(av[mt])); } while (0)
#define HALF(xf, wf, xn, wn, ls, lk, is) do { SCHED(); MM3(xf, wf, 0); SCHED(); \
    if ((ls) >= 0) { LDWF(wn, ls, lk, 0); LDXF(xn, ls, lk, 0); LDWF(wn, ls, lk, 1); } if ((is) >= 0) ISS(is, 0); SCHED(); MM3(xf, wf, 1); SCHED(); \
    if ((ls) >= 0) { LDWF(wn, ls, lk, 2); LDXF(xn, ls, lk, 1); LDXF(xn, ls, lk, 2); } if ((is) >= 0) ISS(is, 1); SCHED(); MM3(xf, wf, 2); SCHED(); \
    if ((ls) >= 0) LDXF(xn, ls, lk, 3); if ((is) >= 0) { ISS(is, 2); ISS(is, 3); } SCHED(); MM3(xf, wf, 3); SCHED(); \
    if ((is) >= 0) ISS(is, 4); SCHED(); } while (0)
#define STEP(s, vm, i1, i2) do { WAIT_L0(); HALF(xa, wa, xb, wb, s, 1, i1); \
    WAIT_L0(); WAIT_VM(vm); RAW_BAR(); HALF(xb, wb, xa, wa, (s) + 1, 0, i2); } while (0)
  STAMP(1);
  WAIT_VM(7); RAW_BAR();
  STAMP(2);
  LOADF(xa, wa, 0, 0);
  STEP(0, 5, 2, 3);
  STAMP(3);
  STEP(1, 10, 4, 5);
  STEP(2, 10, -1, 6);
  STEP(3, 10, -1, 7);
  STAMP(4); CYC(20);
  if (w == 0) pv2 = touch_pages(ws, out, rt, lane);
  STEP(4, 10, -1, -1);
  u64 ready = __hip_atomic_load(sync_z, RLX_AGENT);
  STEP(5, 5, -1, 8);
  if (w < 4) __builtin_amdgcn_s_setprio(2);
  STEP(6, 8, 9, -1);
  STAMP(5); CYC(21); CYCD(20, 21); CYC(21);
  {
    const u64 tag = next_tag(epoch);
#pragma unroll 1
    for (int spin = 0; ready != tag && spin < (1 << 22); ++spin) {
      __builtin_amdgcn_s_sleep(2); ready = __hip_atomic_load(sync_z, RLX_AGENT); __builtin_amdgcn_s_waitcnt(0x0F70);
    }
  }
  WAIT_L0(); HALF(xa, wa, xb, wb, 7, 1, -1); WAIT_L0(); SCHED();
#pragma unroll
  for (int mt = 0; mt < 4; ++mt) aq[mt] = MFMA16(wb[0], xb[mt], aq[mt]);
  asm volatile("" : "+v"(aq[0]), "+v"(aq[1]), "+v"(aq[2]), "+v"(aq[3]));
  SCHED();
#pragma unroll
  for (int mt = 0; mt < 4; ++mt) { ak[mt] = MFMA16(xb[mt], wb[1], ak[mt]); av[mt] = MFMA16(xb[mt], wb[2], av[mt]); }
  asm volatile("" : "+v"(ak[0]), "+v"(ak[1]), "+v"(ak[2]), "+v"(ak[3]), "+v"(av[0]), "+v"(av[1]), "+v"(av[2]), "+v"(av[3]));
  SCHED();
  asm volatile("" :: "v"(pv2));
  STAMP(6);
#undef ISS
#undef ISSUE
#undef LOADF
#undef MM3
#undef HALF
#undef LDWF
#undef LDXF
#undef STEP
  const int h = 4 * hg + wh;
  const rsrc_t rws = mk_rsrc(ws, 8 << 20);
  const int pqv = (g >> 1) * 512 + l15 * 16 + (g & 1) * 256, pqs = (int)WS_PQ + ((tt * 4 + 2 * wm) * 32 + h) * 1024;
#pragma unroll
  for (int pr = 0; pr < 2; ++pr) {
    const f32x4 q0 = aq[2 * pr], q1 = aq[2 * pr + 1];
    const auto s0 = __builtin_amdgcn_permlane16_swap(pk_bf16(phi_f(q0[0]), phi_f(q0[1])), pk_bf16(phi_f(q1[0]), phi_f(q1[1])), false, false);
    const auto s1 = __builtin_amdgcn_permlane16_swap(pk_bf16(phi_f(q0[2]), phi_f(q0[3])), pk_bf16(phi_f(q1[2]), phi_f(q1[3])), false, false);
    store16((u32x4){s0[0], s1[0], s0[1], s1[1]}, rws, pqv + pqs + pr * 32768);
  }
  SCHED();
  STAMP(7);
  f32x4 kvt = {0, 0, 0, 0}, zz = {0, 0, 0, 0};
  const u32x4 ones_u = {0x3f803f80u, 0x3f803f80u, 0x3f803f80u, 0x3f803f80u};
#pragma unroll
  for (int pr = 0; pr < 2; ++pr) {
    u32x4 pk, vv;
#pragma unroll
    for (int m = 0; m < 2; ++m) {
      const f32x4 kk = ak[2 * pr + m], v4 = av[2 * pr + m];
      pk[2 * m] = pk_bf16(phi_f(kk[0]), phi_f(kk[1])); pk[2 * m + 1] = pk_bf16(phi_f(kk[2]), phi_f(kk[3]));
      vv[2 * m] = pk_bf16(v4[0], v4[1]); vv[2 * m + 1] = pk_bf16(v4[2], v4[3]);
    }
    kvt = MFMA16(__builtin_bit_cast(s16x8, vv), __builtin_bit_cast(s16x8, pk), kvt);
    zz = MFMA16(__builtin_bit_cast(s16x8, ones_u), __builtin_bit_cast(s16x8, pk), zz);
  }
  lptr red = smem + 66560;
#pragma unroll
  for (int r = 0; r < 4; ++r) ST(float, red + (wm * 4 + wh) * 1088 + l15 * 4 + (4 * g + r) * 64) = kvt[r];
  if (g == 0) ST(float, red + (wm * 4 + wh) * 1088 + l15 * 4 + 1024) = zz[0];
  LBAR();
  STAMP(8);
  float* kvacc = (float*)(ws + WS_KV + (tt % KVC) * KV_CP) + hg * 1088 + tid;
  atomicAdd(kvacc, LD(float, red + tid * 4) + LD(float, red + 4352 + tid * 4));
  atomicAdd(kvacc + 512, LD(float, red + 2048 + tid * 4) + LD(float, red + 4352 + 2048 + tid * 4));
  if (tid < 64) atomicAdd(kvacc + 1024, LD(float, red + 4096 + tid * 4) + LD(float, red + 4352 + 4096 + tid * 4));
  STAMP(9); CYC(22); CYCD(21, 22);
}

static constexpr int TB_ROW = 544, TB_BUF = 16 * TB_ROW, TB_CH = 2 * TB_BUF;
static constexpr int KR_OFF = 4 * TB_CH, KR_CP = 4160, KR_CH = 2 * KR_CP;
static constexpr int U_OFF = KR_OFF + 4 * KR_CH, U_CH = (2048 + 240) * 2;
static constexpr int EX_OFF = U_OFF + 4 * U_CH;
static_assert(EX_OFF + 32768 <= 163840 && KR_OFF >= 65536, "LDS map");

DI void conv_role(const u16* __restrict__ x, const u16* __restrict__ lar, const u16* __restrict__ aim, const u16* __restrict__ cre,
                  const u16* __restrict__ cim, const u16* __restrict__ ldt, const u16* __restrict__ dsk, const u16* __restrict__ gw,
                  const u16* __restrict__ wo, char* __restrict__ ws, const u16* out, int rt, int cg, int ct, u64& epoch, lptr smem STAMP_ARG) {
  const int tid = threadIdx.x, lane = tid & 63;
  const int w = __builtin_amdgcn_readfirstlane(tid >> 6);
  const int ch = w & 3, half = w >> 2, g = lane >> 4, l15 = lane & 15;
  const int c = 4 * cg + ch;
  const rsrc_t rx = mk_rsrc(x, SEQ * DM * 2);
  const float dsk_f = bf_lo(dsk[c]);
  const bool clear = (cg & 15) == 0;
  u32x4 gx[4], gwv;
  u32 pv = 0;
  if (w < 4) {
    const int cw = 4 * cg + w;
    const u32 lar2 = *(const u32*)(lar + cw * 128 + 2 * lane), aim2 = *(const u32*)(aim + cw * 128 + 2 * lane);
    const u32 cre2 = *(const u32*)(cre + cw * 128 + 2 * lane), cim2 = *(const u32*)(cim + cw * 128 + 2 * lane);
    const u32 ldt_u = ldt[cw];
    if (lane < 8) ST(u32x4, smem + KR_OFF + w * KR_CH + (lane >> 2) * KR_CP + 4096 + 16 * (lane & 3)) = (u32x4){0u, 0u, 0u, 0u};
    SCHED(); RAW_BAR(); SCHED();
    const float dt = rbf(exp_f(bf_lo(ldt_u)));
    float al[2], th[2];
    al[0] = -rbf(exp_f(bf_lo(lar2))) * dt * LOG2E; al[1] = -rbf(exp_f(bf_hi(lar2))) * dt * LOG2E;
    th[0] = bf_lo(aim2) * dt * 0.15915494309189535f; th[1] = bf_hi(aim2) * dt * 0.15915494309189535f;
    STAMP(1);
    u32 ce = wave_umax(max(max(cre2 & 0x7f80u, (cre2 >> 16) & 0x7f80u), max(cim2 & 0x7f80u, (cim2 >> 16) & 0x7f80u))) >> 7;
    ce = ce < 1 ? 1 : (ce > 253 ? 253 : ce);
    const float cinv = __builtin_bit_cast(float, (254u - ce) << 23);
    const float kscale = 2.f * dt * __builtin_bit_cast(float, ce << 23);
    lptr tb = smem + w * TB_CH + lane * 8, tr = smem + w * TB_CH + 16 * g, kr = smem + KR_OFF + w * KR_CH;
#define PUT(buf, e, v) ST(u32x2, tb + (buf) * TB_BUF + (e) * TB_ROW) = (u32x2){pk_f16(v[0]), pk_f16(v[1])}
#define CONJ(v) ((f32x2){(v).x, -(v).y})
#define GETF(f, buf, row) do { lptr p_ = tr + (buf) * TB_BUF + (row) * TB_ROW; \
      _Pragma("unroll") for (int ks = 0; ks < 8; ++ks) f[ks] = LD(h16x8, p_ + 64 * ks); } while (0)
#define TILE(d, f) do { d = (f32x4){0, 0, 0, 0}; _Pragma("unroll") for (int ks = 0; ks < 8; ++ks) d = MFMA16H(f[ks], fx[ks], d); } while (0)
    f32x2 y[17][2], wB[2], w16[2], w32[2], xa[2], xb[2], aS[6][2], t[2], t2[2];
#pragma unroll
    for (int m = 0; m < 2; ++m) {
      y[1][m] = CONJ(cexp_p(al[m], th[m], 1.f)); w16[m] = cexp_p(al[m], th[m], 16.f); wB[m] = CONJ(cexp_p(al[m], th[m], 256.f));
      y[16][m] = CONJ(w16[m]);
    }
    xa[0] = (f32x2){bf_lo(cre2) * cinv, bf_lo(cim2) * cinv}; xa[1] = (f32x2){bf_hi(cre2) * cinv, bf_hi(cim2) * cinv};
    ST(u32x2, tb + TB_BUF) = (u32x2){0x3c00u, 0x3c00u};
    PUT(0, 0, xa); PUT(1, 1, y[1]);
    cmul4(xb, xa, w16, w32, w16, w16); cmul4(y[2], y[1], y[1], aS[0], wB, wB);
    PUT(0, 1, xb); PUT(1, 2, y[2]);
#pragma unroll
    for (int i = 1; i < 8; ++i) {
      cmul4(xa, xa, w32, xb, xb, w32);
      if (i < 7) cmul4(y[2 * i + 1], y[2 * i - 1], y[2], y[2 * i + 2], y[2 * i], y[2]); else cmul2(y[15], y[13], y[2]);
      PUT(0, 2 * i, xa); PUT(0, 2 * i + 1, xb); PUT(1, 2 * i + 1, y[2 * i + 1]);
      if (i < 7) PUT(1, 2 * i + 2, y[2 * i + 2]);
    }
    STAMP(2);
    h16x8 fx[8], fa[8], fb[8];
    f32x4 kd;
    const int gq = l15 >> 2, r3 = min(l15 & 3, 2), yk = 2044 - 16 * l15 - 4 * g;
    SCHED();
    GETF(fx, 0, l15); GETF(fa, 1, l15);
    SCHED();
    PUT(0, 0, wB);
#pragma unroll
    for (int m = 1; m < 9; m += 2) {
      cmul4(t, wB, y[2 * m], t2, wB, y[2 * m + 2]);
      if (m == 1) cmul4(aS[1], aS[0], wB, aS[2], aS[0], aS[0]);
      if (m == 3) cmul4(aS[3], aS[2], wB, aS[4], aS[2], aS[0]);
      if (m == 5) cmul2(aS[5], aS[4], wB);
      PUT(0, m, t); PUT(0, m + 1, t2);
    }
    SCHED();
    GETF(fb, 0, (int)((0x8766654443222100ull >> (4 * l15)) & 15));
    TILE(kd, fa);
    SCHED();
#pragma unroll
    for (int s = 0; s < 2; ++s) {
      PUT(1, 5 * s, aS[s]);
#pragma unroll
      for (int m = 1; m < 5; m += 2) { cmul4(t, aS[s], y[4 * m], t2, aS[s], y[4 * m + 4]); PUT(1, 5 * s + m, t); PUT(1, 5 * s + m + 1, t2); }
    }
    {
      const f32x4 k0 = kd * kscale;
      const u32 p32 = pk_bf16(k0[3], k0[2]), p10 = pk_bf16(k0[1], k0[0]);
      ST(u32x2, kr + 2 * yk) = (u32x2){p32, p10};
      ST(u16, kr + KR_CP + 2 * yk + 2) = (u16)p32;
      ST(u32, kr + KR_CP + 2 * yk + 4) = pk_bf16(k0[2], k0[1]);
      ST(u16, kr + KR_CP + 2 * yk + 8) = (u16)(p10 >> 16);
    }
    SCHED();
    GETF(fa, 1, 5 * (gq >> 1) + 2 * (gq & 1) + r3);
    TILE(kd, fb);
    SCHED();
#pragma unroll
    for (int s = 2; s < 6; ++s) {
      cmul4(t, aS[s], y[8], t2, aS[s], y[16]);
      PUT(0, 3 * (s - 2), aS[s]); PUT(0, 3 * (s - 2) + 1, t); PUT(0, 3 * (s - 2) + 2, t2);
    }
    {
      const f32x4 k1 = kd * kscale;
      const u32 q10 = pk_bf16(k1[1], k1[0]);
      ST(u32x2, kr + 2 * (yk - 256)) = (u32x2){pk_bf16(k1[3], k1[2]), q10};
      ST(u32x2, kr + KR_CP + 2 * (yk - 256)) = (u32x2){pk_bf16(k1[3], k1[3]), pk_bf16(k1[2], k1[1])};
      if (lane == 0) ST(u16, kr + KR_CP + 2 * 1792) = (u16)q10;
    }
    SCHED();
    GETF(fb, 0, 3 * gq + r3);
    TILE(kd, fa);
    STAMP(3);
    {
      const f32x4 k0 = kd * kscale;
      const int y2 = 2040 - (512 + 256 * (g >> 1) + 16 * l15 + 8 * (g & 1));
      const u32 b2 = pk_bf16(k0[2], k0[2]), b1 = pk_bf16(k0[1], k0[1]), b0 = pk_bf16(k0[0], k0[0]);
      ST(u32x4, kr + 2 * y2) = (u32x4){b2, b1, pk_bf16(k0[1], k0[0]), b0};
      ST(u32x4, kr + KR_CP + 2 * y2) = (u32x4){b2, pk_bf16(k0[2], k0[1]), b1, b0};
    }
    TILE(kd, fb);
    {
      const f32x4 k1 = kd * kscale;
      const int y3 = 2032 - (1024 + 256 * g + 16 * l15);
      const u32 a2 = pk_bf16(k1[2], k1[2]), a1 = pk_bf16(k1[1], k1[1]), a0 = pk_bf16(k1[0], k1[0]);
      ST(u32x4, kr + 2 * y3) = (u32x4){a2, a2, a1, a1};
      ST(u32x4, kr + 2 * y3 + 16) = (u32x4){a1, pk_bf16(k1[1], k1[0]), a0, a0};
      ST(u32x4, kr + KR_CP + 2 * y3) = (u32x4){a2, a2, pk_bf16(k1[2], k1[1]), a1};
      ST(u32x4, kr + KR_CP + 2 * y3 + 16) = (u32x4){a1, a1, a0, a0};
    }
#undef PUT
#undef GETF
#undef TILE
#pragma unroll
    for (int r = 0; r < 4; ++r) gx[r] = __builtin_amdgcn_raw_buffer_load_b128(rx, lane * 16 + 1024 * r, (16 * cg + 4 * w) * 1024, 0);
    gwv = *(const u32x4*)(gw + 8 * lane);
    epoch = load_epoch(ws);
    if (w == 0) pv = touch_pages(ws, out, rt, lane);
    STAMP(4);
  } else {
    const int t2 = (tid + 16 * cg) & 255;
    if (clear) {
      const rsrc_t rk = mk_rsrc(ws, 8 << 20);
      const u32x4 z4 = {0u, 0u, 0u, 0u};
#pragma unroll
      for (int cp = 0; cp < KVC; ++cp) {
        store16(z4, rk, t2 * 16 + (int)WS_KV + cp * KV_CP + (cg >> 4) * 4352);
        if (t2 < 16) store16(z4, rk, t2 * 16 + (int)WS_KV + cp * KV_CP + (cg >> 4) * 4352 + 4096);
      }
    }
    SCHED(); RAW_BAR(); SCHED();
    u32x2 xr[8];
#pragma unroll
    for (int r = 0; r < 8; ++r) xr[r] = __builtin_amdgcn_raw_buffer_load_b64(rx, t2 * 4096 + (r & 3) * 1024, 8 * cg + (r >> 2) * (1024 * 1024), 0);
    epoch = load_epoch(ws);
    STAMP(1);
    if (lane < 30) ST(u32x4, smem + U_OFF + (w - 4) * U_CH + lane * 16) = (u32x4){0u, 0u, 0u, 0u};
#pragma unroll
    for (int hf = 0; hf < 2; ++hf) {
      lptr ub = smem + U_OFF + 480 + t2 * 8 + hf * 2048;
      const u32x2 x0 = xr[4 * hf], x1 = xr[4 * hf + 1], x2 = xr[4 * hf + 2], x3 = xr[4 * hf + 3];
      ST(u32x2, ub + 0 * U_CH) = (u32x2){__builtin_amdgcn_perm(x1.x, x0.x, 0x05040100u), __builtin_amdgcn_perm(x3.x, x2.x, 0x05040100u)};
      ST(u32x2, ub + 1 * U_CH) = (u32x2){__builtin_amdgcn_perm(x1.x, x0.x, 0x07060302u), __builtin_amdgcn_perm(x3.x, x2.x, 0x07060302u)};
      ST(u32x2, ub + 2 * U_CH) = (u32x2){__builtin_amdgcn_perm(x1.y, x0.y, 0x05040100u), __builtin_amdgcn_perm(x3.y, x2.y, 0x05040100u)};
      ST(u32x2, ub + 3 * U_CH) = (u32x2){__builtin_amdgcn_perm(x1.y, x0.y, 0x07060302u), __builtin_amdgcn_perm(x3.y, x2.y, 0x07060302u)};
    }
    WAIT_VM(0);
    STAMP(4);
  }
  lptr ka[8];
  {
    const int yb = 2031 - l15 + 8 * g - 128 * half - 96 - 1792;
    lptr k7 = smem + KR_OFF + ch * KR_CH + (yb & 1) * KR_CP + ((yb + 1) >> 1) * 4;
#pragma unroll
    for (int n = 0; n < 8; ++n) { ka[n] = k7 + 512 * (7 - n); asm volatile("" : "+v"(ka[n])); }
  }
  lptr ua = smem + U_OFF + ch * U_CH + (8 * g + 16 * l15 + 224 - 128 * half - 96) * 2;
  STAMP(6);
  LBAR();
  if (clear && tid == 256) __hip_atomic_store((gu64*)(ws + WS_SYNC + 128 + (cg >> 4) * 128), next_tag(epoch), RLX_AGENT);
  if (half) { fetch_wo(wo, ct, 2 * ch, lane, smem); fetch_wo(wo, ct, 2 * ch + 1, lane, smem); }
  STAMP(7); CYC(20);
  f32x4 acc[8];
#pragma unroll
  for (int i = 0; i < 8; ++i) acc[i] = (f32x4){0, 0, 0, 0};
  {
    u32x4 A0[8], A1[8]; s16x8 B0[8], B1[8];
#define LOADC(A, B, rr) do { _Pragma("unroll") for (int n = 0; n < 8; ++n) { \
      lptr kp = ka[n] + 64 * (3 - (rr)); \
      A[n] = (u32x4){LD(u32, kp), LD(u32, kp + 4), LD(u32, kp + 8), LD(u32, kp + 12)}; \
      B[n] = LD(s16x8, ua + 64 * (3 - (rr)) + 512 * n); } } while (0)
#define MMC(A, B) do { _Pragma("unroll") for (int nu = 0; nu < 8; ++nu) _Pragma("unroll") for (int mu = 0; mu < 8 - nu; ++mu) \
      acc[nu + mu] = MFMA16(__builtin_bit_cast(s16x8, A[nu]), B[mu], acc[nu + mu]); } while (0)
#define PINC() do { asm volatile("" : "+v"(acc[0]), "+v"(acc[1]), "+v"(acc[2]), "+v"(acc[3]), "+v"(acc[4]), "+v"(acc[5]), "+v"(acc[6]), "+v"(acc[7])); SCHED(); } while (0)
#define MIXC() do { _Pragma("unroll") for (int q = 0; q < 12; ++q) { __builtin_amdgcn_sched_group_barrier(0x8, 3, 0); __builtin_amdgcn_sched_group_barrier(0x100, 2, 0); } } while (0)
    LOADC(A0, B0, 0);
    SCHED();
    WAIT_L0(); SCHED(); LOADC(A1, B1, 1); MMC(A0, B0); MIXC(); PINC();
    WAIT_L0(); SCHED(); LOADC(A0, B0, 2); MMC(A1, B1); MIXC(); PINC();
    WAIT_L0(); SCHED(); LOADC(A1, B1, 3); MMC(A0, B0); MIXC(); PINC();
    WAIT_L0(); SCHED(); MMC(A1, B1); PINC();
#undef LOADC
#undef MMC
  }
  STAMP(8); CYC(21); CYCD(20, 21);
  {
    lptr ex = smem + EX_OFF + w * 4096 + lane * 16;
    f32x4 keep[4];
    if (half) {
#pragma unroll
      for (int i = 0; i < 4; ++i) { ST(f32x4, ex + 1024 * i) = acc[i]; keep[i] = acc[4 + i]; }
    } else {
#pragma unroll
      for (int i = 0; i < 4; ++i) { ST(f32x4, ex + 1024 * i) = acc[4 + i]; keep[i] = acc[i]; }
    }
    const int t0 = 1024 * half + 16 * l15 + 4 * g;
    const rsrc_t rws = mk_rsrc(ws, 8 << 20);
    lptr up = smem + U_OFF + ch * U_CH + 480 + 2 * t0;
#pragma unroll
    for (int i = 0; i < 4; ++i) {
      const u32x2 uu = LD(u32x2, up + 512 * i);
      keep[i] += (f32x4){bf_lo(uu.x), bf_hi(uu.x), bf_lo(uu.y), bf_hi(uu.y)} * dsk_f;
    }
    asm volatile("" :: "v"(pv));
    if (w < 4) {
      float sg[4];
#pragma unroll
      for (int r = 0; r < 4; ++r) {
        float s0 = 0.f;
#pragma unroll
        for (int i = 0; i < 4; ++i) s0 = __builtin_amdgcn_fdot2_f32_bf16(u2b(gx[r][i]), u2b(gwv[i]), s0, false);
        sg[r] = rbf(__builtin_amdgcn_rcpf(1.f + __builtin_amdgcn_exp2f(-LOG2E * rbf(wave_sum(s0)))));
      }
      if (lane == 63) store16(__builtin_bit_cast(u32x4, (f32x4){sg[0], sg[1], sg[2], sg[3]}), rws, (int)WS_G + (16 * cg + 4 * w) * 4 + (lane - 63));
    }
    LBAR();
    lptr ei = smem + EX_OFF + (w ^ 4) * 4096 + lane * 16;
    u32x2 yb[4];
#pragma unroll
    for (int i = 0; i < 4; ++i) {
      const f32x4 y = keep[i] + LD(f32x4, ei + 1024 * i);
      yb[i] = (u32x2){pk_bf16(y[0], y[1]), pk_bf16(y[2], y[3])};
    }
#pragma unroll
    for (int i = 0; i < 2; ++i) {
      const auto sx = __builtin_amdgcn_permlane16_swap(yb[2 * i].x, yb[2 * i + 1].x, false, false);
      const auto sy = __builtin_amdgcn_permlane16_swap(yb[2 * i].y, yb[2 * i + 1].y, false, false);
      store16((u32x4){sx[0], sy[0], sx[1], sy[1]}, rws, (int)WS_BT + c * 4096 + 2048 * half + 32 * l15 + 16 * (g >> 1) + 512 * (g & 1) + 1024 * i);
    }
  }
  STAMP(9);
}

struct Tags { u64 a, b; };
DI Tags poll_load(char* ws, int off, int lane) {
  gu64* p = (gu64*)(ws + WS_FLAG + off) + lane;
  return Tags{__hip_atomic_load(p, RLX_AGENT), __hip_atomic_load(p + 64, RLX_AGENT)};
}
DI void poll_wait(char* ws, int off, int lane, u64 tag, Tags& f, u64 ma, u64 mb) {
#pragma unroll 1
  for (int spin = 0; spin < (1 << 20); ++spin) {
    if ((__ballot(f.a != tag) & ma) == 0 && (__ballot(f.b != tag) & mb) == 0) break;
    __builtin_amdgcn_s_sleep(POLL_SLEEP);
    f = poll_load(ws, off, lane);
    __builtin_amdgcn_s_waitcnt(0x0F70);
  }
}

DI void out_role(char* __restrict__ ws, u16* __restrict__ out, int rt, int ct, u64 tag, lptr smem STAMP_ARG) {
  const int tid = threadIdx.x, lane = tid & 63;
  const int w = __builtin_amdgcn_readfirstlane(tid >> 6);
  const int th = w & 1, kq = w >> 1, hh = lane >> 5, l31 = lane & 31;
  const rsrc_t rws = mk_rsrc(ws, 8 << 20);
  const int foff = (blockIdx.x & 31) * 2048;
  Tags fl = {0, 0};
  if (w == 0) { fl = poll_load(ws, foff, lane); poll_wait(ws, foff, lane, tag, fl, ~0ull, ~0ull); }
  RAW_BAR();
  STAMP(11);
  if (w == 1) fl = poll_load(ws, foff + 1024, lane);
  f32x4 kvr[KVC][5];
#pragma unroll
  for (int cp = 0; cp < KVC; ++cp) {
#pragma unroll
    for (int i = 0; i < 4; ++i) kvr[cp][i] = __builtin_bit_cast(f32x4, __builtin_amdgcn_raw_buffer_load_b128(rws, tid * 16, (int)WS_KV + cp * KV_CP + 8192 * i, 16));
    kvr[cp][4] = __builtin_bit_cast(f32x4, __builtin_amdgcn_raw_buffer_load_b128(rws, (tid & 127) * 16, (int)WS_KV + cp * KV_CP + 32768, 16));
  }
  SCHED(); RAW_BAR(); SCHED();
  u32x4 pf[8];
#define LOAD_PQ(i0, i1) do { _Pragma("unroll") for (int i = i0; i < i1; ++i) \
    pf[i] = __builtin_amdgcn_raw_buffer_load_b128(rws, lane * 16, (int)WS_PQ + ((2 * rt + th) * 32 + 8 * kq + i) * 1024, 16); } while (0)
  LOAD_PQ(0, 3);
  STAMP(12);
#pragma unroll
  for (int i = 0; i < 5; ++i) {
    f32x4 v = kvr[0][i];
#pragma unroll
    for (int cp = 1; cp < KVC; ++cp) v += kvr[cp][i];
    ST(u32x2, smem + P2_KV + (i < 4 ? tid + 512 * i : 2048 + (tid & 127)) * 8) = (u32x2){pk_bf16(v[0], v[1]), pk_bf16(v[2], v[3])};
  }
  f32x16 acc[2];
#pragma unroll
  for (int i = 0; i < 16; ++i) { acc[0][i] = 0.f; acc[1][i] = 0.f; }
  f32x16 cinit = acc[0];
  cinit[8] = EPS;
  lptr kva = smem + P2_KV + 8 * kq * 544 + min(l31, 16) * 32 + hh * 16;
  const int blk = (lane >> 4) & 1, q4 = (lane & 15) >> 2, p4 = lane & 3;
  const int sw = ((q4 >> 1) & 1) << 6;
  lptr wa0 = smem + P2_WO + (128 * kq + 4 * hh + q4) * 128 + blk * 32 + p4 * 8 + sw;
  lptr wa1 = smem + P2_WO + (128 * kq + 4 * hh + q4) * 128 + blk * 32 + p4 * 8 + (64 ^ sw);
  STAMP(13);
  if (w == 1) { const u32 ok = __all(fl.a == tag && fl.b == tag); if (lane == 0) ST(u32, smem + P2_OK) = ok; }
  LBAR();
  LOAD_PQ(3, 8);
#undef LOAD_PQ
  const bool early = __builtin_amdgcn_readfirstlane(LD(u32, smem + P2_OK)) != 0;
  u32x4 btr; float gt;
#define LOAD_B() do { \
    btr = __builtin_amdgcn_raw_buffer_load_b128(rws, (lane >> 2) * 4096 + (lane & 3) * 16, (int)WS_BT + (64 * ct + 16 * kq) * 4096 + 128 * rt + 64 * th, 16); \
    gt = u2f(__builtin_amdgcn_raw_buffer_load_b32(rws, l31 * 4, (int)WS_G + (64 * rt + 32 * th) * 4, 16)); } while (0)
  if (early) LOAD_B();
  STAMP(14);
  {
    f32x16 d[2]; s16x8 kf[8], wf[2][2]; u32x4 of[2];
#define OMM(i) do { d[(i) & 1] = MFMA32(kf[i], __builtin_bit_cast(s16x8, pf[i]), cinit); asm volatile("" : "+v"(d[(i) & 1])); } while (0)
#define NORM(i) do { const f32x16& dd = d[(i) & 1]; const float inv = __builtin_amdgcn_rcpf(dd[8]); \
      of[(i) & 1] = (u32x4){pk_bf16(dd[0] * inv, dd[1] * inv), pk_bf16(dd[2] * inv, dd[3] * inv), pk_bf16(dd[4] * inv, dd[5] * inv), pk_bf16(dd[6] * inv, dd[7] * inv)}; \
      asm volatile("" : "+v"(of[(i) & 1]) : "v"(dd)); } while (0)
#define LDW(i) do { wf[(i) & 1][0] = cat8(tr_read(wa0 + (i) * 2048), tr_read(wa0 + (i) * 2048 + 1024)); \
      wf[(i) & 1][1] = cat8(tr_read(wa1 + (i) * 2048), tr_read(wa1 + (i) * 2048 + 1024)); } while (0)
#define AMM(i) do { acc[0] = MFMA32(wf[(i) & 1][0], __builtin_bit_cast(s16x8, of[(i) & 1]), acc[0]); \
      acc[1] = MFMA32(wf[(i) & 1][1], __builtin_bit_cast(s16x8, of[(i) & 1]), acc[1]); asm volatile("" : "+v"(acc[0]), "+v"(acc[1])); } while (0)
#pragma unroll
    for (int i = 0; i < 8; ++i) kf[i] = LD(s16x8, kva + i * 544);
    LDW(0);
    SCHED();
    OMM(0); OMM(1); SCHED(); NORM(0); SCHED();
#pragma unroll
    for (int i = 0; i < 8; ++i) {
      if (i + 1 < 8) LDW(i + 1);
      SCHED();
      AMM(i);
      if (i + 2 < 8) OMM(i + 2);
      SCHED();
      if (i + 1 < 8) NORM(i + 1);
      SCHED();
    }
#undef OMM
#undef NORM
#undef LDW
#undef AMM
  }
  STAMP(15);
#pragma unroll
  for (int q = 0; q < 4; ++q) {
    lptr ex = smem + P2_EX + ((2 * q + th) * 4 + kq) * 2048 + lane * 16;
    const f32x16& s = acc[q >> 1];
    ST(f32x4, ex) = (f32x4){s[8 * (q & 1)], s[8 * (q & 1) + 1], s[8 * (q & 1) + 2], s[8 * (q & 1) + 3]};
    ST(f32x4, ex + 1024) = (f32x4){s[8 * (q & 1) + 4], s[8 * (q & 1) + 5], s[8 * (q & 1) + 6], s[8 * (q & 1) + 7]};
  }
  if (!early && w == 1) poll_wait(ws, foff + 1024, lane, tag, fl, ~0ull, ~0ull);
  LBAR();
  if (!early) LOAD_B();
#undef LOAD_B
  if (blockIdx.x == 0 && tid == 0) __hip_atomic_store((gu64*)(ws + WS_SYNC), tag, RLX_AGENT);
  STAMP(16);
  lptr ei = smem + P2_EX + w * 8192 + lane * 16;
  ST(u32x4, smem + P2_BT + w * 1024 + lane * 16) = btr;
  const float g1 = rbf(1.f - gt);
  lptr ba = smem + P2_BT + w * 1024 + (4 * hh + q4) * 64 + blk * 32 + p4 * 8;
  u32x2 res[2];
#pragma unroll
  for (int i = 0; i < 2; ++i) {
    const f32x4 o = (LD(f32x4, ei + 1024 * i) + LD(f32x4, ei + 2048 + 1024 * i)) + (LD(f32x4, ei + 4096 + 1024 * i) + LD(f32x4, ei + 6144 + 1024 * i));
    const u32x2 bb = __builtin_bit_cast(u32x2, tr_read(ba + i * 512));
    res[i] = (u32x2){pk_bf16(gt * o[0] + g1 * bf_lo(bb.x), gt * o[1] + g1 * bf_hi(bb.x)), pk_bf16(gt * o[2] + g1 * bf_lo(bb.y), gt * o[3] + g1 * bf_hi(bb.y))};
  }
  const auto sx = __builtin_amdgcn_permlane32_swap(res[0].x, res[1].x, false, false);
  const auto sy = __builtin_amdgcn_permlane32_swap(res[0].y, res[1].y, false, false);
  *(u32x4*)(out + (size_t)(64 * rt + 32 * th + l31) * DM + 64 * ct + 16 * kq + 8 * hh) = (u32x4){sx[0], sy[0], sx[1], sy[1]};
  STAMP(17);
}

__global__ __launch_bounds__(512, 2) void fused(const u16* __restrict__ x, const u16* __restrict__ wqkv, const u16* __restrict__ wo,
                                                const u16* __restrict__ lar, const u16* __restrict__ aim, const u16* __restrict__ cre,
                                                const u16* __restrict__ cim, const u16* __restrict__ ldt, const u16* __restrict__ dsk,
                                                const u16* __restrict__ gw, char* __restrict__ ws, u16* __restrict__ out) {
  extern __shared__ __attribute__((aligned(16))) char smem_[];
  lptr smem = (lptr)smem_;
  STAMP_DECL; STAMP(0);
  asm volatile("" :: "s"(x), "s"(wqkv), "s"(wo), "s"(lar), "s"(aim), "s"(cre), "s"(cim), "s"(ldt), "s"(dsk), "s"(gw), "s"(ws), "s"(out));
  const int b = blockIdx.x, xcd = b & 7, slot = b >> 3, idx = slot >> 1;
  const int rt = 8 * (xcd >> 1) + (slot & 7), ct = 4 * (xcd & 1) + (slot >> 3);
  const int tt = 4 * (xcd >> 1) + (idx & 3), hg = 4 * (xcd & 1) + (idx >> 2);
  u64 epoch;
  if (__builtin_expect(slot & 1, 0)) conv_role(x, lar, aim, cre, cim, ldt, dsk, gw, wo, ws, out, rt, xcd * 16 + idx, ct, epoch, smem STAMP_PASS);
  else gemm_role(x, wqkv, wo, ct, ws, out, rt, tt, hg, idx, epoch, smem STAMP_PASS);
  const int lane = threadIdx.x & 63, w = __builtin_amdgcn_readfirstlane(threadIdx.x >> 6);
  const u64 tag = next_tag(epoch);
  SCHED();
  WAIT_VM(0);
  __builtin_amdgcn_s_setprio(0);
  STAMP(10);
  LBAR();
  if (threadIdx.x < 32) __hip_atomic_store((gu64*)(ws + WS_FLAG + threadIdx.x * 2048) + ((slot & 1) ? 128 + xcd * 16 + idx : tt * 8 + hg), tag, RLX_AGENT);
  out_role(ws, out, rt, ct, tag, smem STAMP_PASS);
  STAMP_FLUSH(ws);
}

extern "C" void kernel_launch(void* const* d_in, const int* in_sizes, int n_in, void* d_out, int out_size, void* d_ws,
                              size_t ws_size, hipStream_t stream) {
  (void)in_sizes; (void)n_in; (void)out_size; (void)ws_size;
  (void)hipFuncSetAttribute((const void*)fused, hipFuncAttributeMaxDynamicSharedMemorySize, 163840);
  fused<<<256, 512, 163840, stream>>>((const u16*)d_in[0], (const u16*)d_in[1], (const u16*)d_in[2], (const u16*)d_in[4],
                                      (const u16*)d_in[5], (const u16*)d_in[6], (const u16*)d_in[7], (const u16*)d_in[8],
                                      (const u16*)d_in[9], (const u16*)d_in[10], (char*)d_ws, (u16*)d_out);
}
```

```cpp
#include <hip/hip_runtime.h>
#include <hip/hip_bf16.h>
#include <stdint.h>

typedef unsigned int u32;
typedef unsigned short u16;
typedef __attribute__((ext_vector_type(8))) short s16x8;
typedef __attribute__((ext_vector_type(4))) short s16x4;
typedef __attribute__((ext_vector_type(8))) _Float16 h16x8;
typedef __attribute__((ext_vector_type(2))) _Float16 h16x2;
typedef __attribute__((ext_vector_type(2))) __bf16 b16x2;
typedef __attribute__((ext_vector_type(2))) float f32x2;
typedef __attribute__((ext_vector_type(4))) float f32x4;
typedef __attribute__((ext_vector_type(16))) float f32x16;
typedef __attribute__((ext_vector_type(2))) u32 u32x2;
typedef __attribute__((ext_vector_type(4))) u32 u32x4;
#define LDS_AS __attribute__((address_space(3)))
#define DI __device__ __forceinline__

static constexpr int SEQ = 2048, DM = 512;
#define KVC 1
static constexpr size_t WS_KV = (6u << 20) + 65536;
static constexpr int KV_CP = 36864;
static constexpr size_t WS_G = 65536;
static constexpr size_t WS_PQ = 1u << 20;
static constexpr size_t WS_BT = (4u << 20) + 4096;
static constexpr int WS_USED = 8 << 20;
#define SYNCO 49152
static constexpr size_t WS_SYNC = SYNCO;
static constexpr size_t WS_FLAG = 131072;
typedef unsigned long long u64;
typedef __attribute__((address_space(1))) u64 gu64;
typedef __attribute__((address_space(1))) u32 gu32;
#define RLX_AGENT __ATOMIC_RELAXED, __HIP_MEMORY_SCOPE_AGENT
__device__ __forceinline__ u64 next_tag(u64 e) { return e * 6364136223846793005ull + 1442695040888963407ull; }

#define STAMP_ARG
#define STAMP_PASS
#define STAMP_DECL do {} while (0)
#define STAMP(i) do {} while (0)
#define CYC(i) do {} while (0)
#define CYCD(i, j) do {} while (0)
#define STAMP_FLUSH(wsp) do {} while (0)
#define MIXM 2
#define POLL_SLEEP 2
#define ST_AUX 16
#define WAIT_VM(n) asm volatile("s_waitcnt vmcnt(" #n ")" ::: "memory")
#define WAIT_L0() __builtin_amdgcn_s_waitcnt(0xC07F)
#define RAW_BAR() __builtin_amdgcn_s_barrier()
#define SCHED() __builtin_amdgcn_sched_barrier(0)
#define LBAR() do { WAIT_L0(); asm volatile("" ::: "memory"); RAW_BAR(); asm volatile("" ::: "memory"); } while (0)
typedef LDS_AS char* lptr;
#define LD(T, p) (*(const LDS_AS T*)(p))
#define ST(T, p) (*(LDS_AS T*)(p))

DI u32 pk_bf16(float a, float b) { return __builtin_bit_cast(u32, __builtin_convertvector((f32x2){a, b}, b16x2)); }
DI u32 pk_f16(f32x2 v) { return __builtin_bit_cast(u32, __builtin_convertvector(v, h16x2)); }
DI float bf_lo(u32 v) { return __builtin_bit_cast(float, v << 16); }
DI float bf_hi(u32 v) { return __builtin_bit_cast(float, v & 0xffff0000u); }
DI float u2f(u32 v) { return __builtin_bit_cast(float, v); }
DI b16x2 u2b(u32 v) { return __builtin_bit_cast(b16x2, v); }
DI float rbf(float v) { return bf_lo(pk_bf16(v, v)); }
static constexpr float LOG2E = 1.44269504088896341f, EPS = 1e-6f;
DI float phi_f(float t) {
  const float hi = __builtin_bit_cast(float, max(__builtin_bit_cast(int, t), __builtin_bit_cast(int, EPS)));
  return hi + __builtin_amdgcn_fmed3f(__builtin_amdgcn_exp2f(__builtin_fmaf(t, LOG2E, -EPS * LOG2E)), 0.f, 1.f);
}
DI float exp_f(float v) {
  const float p = v * LOG2E;
  const float r = __builtin_fmaf(v, 1.925963033500011e-8f, __builtin_fmaf(v, LOG2E, -p));
  return __builtin_amdgcn_exp2f(p) * __builtin_fmaf(r, 0.6931471805599453f, 1.f);
}
#define DPP_F(v, ctrl, rmask, bc) __builtin_bit_cast(float, __builtin_amdgcn_update_dpp(0, __builtin_bit_cast(int, v), ctrl, rmask, 0xf, bc))
DI float wave_sum(float r) {
  r += DPP_F(r, 0x111, 0xf, true); r += DPP_F(r, 0x112, 0xf, true); r += DPP_F(r, 0x114, 0xf, true); r += DPP_F(r, 0x118, 0xf, true);
  r += DPP_F(r, 0x142, 0xa, false); r += DPP_F(r, 0x143, 0xc, false);
  return r;
}
DI u32 wave_umax(u32 r) {
#define DPP_U(v, ctrl, rmask, bc) (u32)__builtin_amdgcn_update_dpp(0, (int)(v), ctrl, rmask, 0xf, bc)
  r = max(r, DPP_U(r, 0x111, 0xf, true)); r = max(r, DPP_U(r, 0x112, 0xf, true)); r = max(r, DPP_U(r, 0x114, 0xf, true));
  r = max(r, DPP_U(r, 0x118, 0xf, true)); r = max(r, DPP_U(r, 0x142, 0xa, false)); r = max(r, DPP_U(r, 0x143, 0xc, false));
  return (u32)__builtin_amdgcn_readlane((int)r, 63);
}
DI s16x4 tr_read(lptr p) { return __builtin_amdgcn_ds_read_tr16_b64_v4i16((LDS_AS s16x4*)p); }
DI s16x8 cat8(s16x4 a, s16x4 b) { return __builtin_shufflevector(a, b, 0, 1, 2, 3, 4, 5, 6, 7); }
typedef __amdgpu_buffer_rsrc_t rsrc_t;
DI rsrc_t mk_rsrc(const void* p, int bytes) { return __builtin_amdgcn_make_buffer_rsrc((void*)p, 0, bytes, 0x00020000); }
DI void store16(u32x4 v, rsrc_t r, int off) { __builtin_amdgcn_raw_buffer_store_b128(v, r, off, 0, ST_AUX); }
DI void blds16(rsrc_t r, lptr l, int voff, int soff) { __builtin_amdgcn_raw_ptr_buffer_load_lds(r, (LDS_AS void*)l, 16, voff, soff, 0, 0); }
#define CM_MUL(t, a, w) "v_pk_mul_f32 " t ", " a ", " w " op_sel:[1,1] op_sel_hi:[1,0]\n\t"
#define CM_FMA(r, a, w, t) "v_pk_fma_f32 " r ", " a ", " w ", " t " op_sel_hi:[0,1,1] neg_lo:[0,0,1]\n\t"
DI void cmul2(f32x2 (&r)[2], const f32x2 (&a)[2], const f32x2 (&w)[2]) {
  f32x2 r0, r1;
  asm(CM_MUL("%0", "%2", "%4") CM_MUL("%1", "%3", "%5") CM_FMA("%0", "%2", "%4", "%0") CM_FMA("%1", "%3", "%5", "%1")
      : "=&v"(r0), "=&v"(r1) : "v"(a[0]), "v"(a[1]), "v"(w[0]), "v"(w[1]));
  r[0] = r0; r[1] = r1;
}
DI void cmul4(f32x2 (&r)[2], const f32x2 (&a)[2], const f32x2 (&w)[2], f32x2 (&q)[2], const f32x2 (&b)[2], const f32x2 (&v)[2]) {
  f32x2 r0, r1, q0, q1;
  asm(CM_MUL("%0", "%4", "%6") CM_MUL("%1", "%5", "%7") CM_MUL("%2", "%8", "%10") CM_MUL("%3", "%9", "%11")
      CM_FMA("%0", "%4", "%6", "%0") CM_FMA("%1", "%5", "%7", "%1") CM_FMA("%2", "%8", "%10", "%2") CM_FMA("%3", "%9", "%11", "%3")
      : "=&v"(r0), "=&v"(r1), "=&v"(q0), "=&v"(q1)
      : "v"(a[0]), "v"(a[1]), "v"(w[0]), "v"(w[1]), "v"(b[0]), "v"(b[1]), "v"(v[0]), "v"(v[1]));
  r[0] = r0; r[1] = r1; q[0] = q0; q[1] = q1;
}
DI f32x2 cexp_p(float alpha, float theta, float p) {
  float m = __builtin_amdgcn_exp2f(alpha * p);
  float f = __builtin_amdgcn_fractf(theta * p);
  return (f32x2){m * __builtin_amdgcn_cosf(f), m * __builtin_amdgcn_sinf(f)};
}
#define MFMA16(a, b, c) __builtin_amdgcn_mfma_f32_16x16x32_bf16((a), (b), (c), 0, 0, 0)
#define MFMA16H(a, b, c) __builtin_amdgcn_mfma_f32_16x16x32_f16((a), (b), (c), 0, 0, 0)
#define MFMA32(a, b, c) __builtin_amdgcn_mfma_f32_32x32x16_bf16((a), (b), (c), 0, 0, 0)

static constexpr int STAGE = 40960;
static constexpr int WOQ = 17408;
static constexpr int P2_WO = 0, P2_KV = 4 * WOQ, P2_OK = P2_KV + 17408, P2_BT = P2_KV + 18432, P2_EX = P2_BT + 8192;

DI u64 load_epoch(char* ws) {
  int vzero;
  asm volatile("v_mov_b32 %0, 0" : "=v"(vzero));
  return __hip_atomic_load((gu64*)(ws + WS_SYNC + vzero), RLX_AGENT);
}
DI u32 touch_pages(const char* ws, const u16* out, int rt, int lane) {
  const char* p = lane < 4 ? ws + (size_t)((0xf871u >> (4 * lane)) & 15) * (512u << 10) : (const char*)out + (size_t)rt * 65536;
  return lane < 5 ? *(const u32*)p : 0u;
}
DI void fetch_wo(const u16* __restrict__ wo, int ct, int q, int lane, lptr smem) {
  const rsrc_t rwo = mk_rsrc(wo, DM * DM * 2);
  const int sr = lane >> 3, sp = lane & 7, sc = (sp ^ (((sr >> 1) & 1) << 2)) << 4;
#pragma unroll
  for (int i = 0; i < 16; ++i) blds16(rwo, smem + P2_WO + q * WOQ + 1024 * i, sr * 1024 + sc, (128 * q + 8 * i) * 1024 + 128 * ct);
}

DI void gemm_role(const u16* __restrict__ x, const u16* __restrict__ wqkv, const u16* __restrict__ wo, int ct, char* __restrict__ ws, const u16* out, int rt, int tt, int hg,
                  u64& epoch, lptr smem STAMP_ARG) {
  const int tid = threadIdx.x, lane = tid & 63;
  const int w = __builtin_amdgcn_readfirstlane(tid >> 6);
  const int wm = w >> 2, wh = w & 3, g = lane >> 4, l15 = lane & 15;
  const int sr = lane >> 3, sp = lane & 7;
  const rsrc_t rx = mk_rsrc(x, SEQ * DM * 2), rw = mk_rsrc(wqkv, DM * 1536 * 2);
  const int xv = sr * 1024 + ((sp ^ sr) << 4), xs0 = (tt * 128 + 16 * w) * 1024;
  const int fw = ((sr >> 1) & 1) | ((w & 1) << 1);
  const int wv = sr * 3072 + ((sp ^ (fw << 1)) << 4), ws0 = 8 * w * 3072 + hg * 128;
  const int ROT = 2 * (tt & 3) + ((tt >> 2) & 1);
  const rsrc_t rwo = mk_rsrc(wo, DM * DM * 2);
  const int ov = sr * 1024 + ((sp ^ (((sr >> 1) & 1) << 2)) << 4);
#define ISS(s, i) do { lptr sb = smem + ((s) & 3) * STAGE; \
    if ((s) >= 8) { if ((i) < 4) blds16(rwo, smem + P2_WO + (w >> 1) * WOQ + (64 * (w & 1) + 32 * ((s) - 8) + 8 * (i)) * 128, ov, (64 * w + 32 * ((s) - 8) + 8 * (i)) * 1024 + 128 * ct); } \
    else if ((i) < 2) blds16(rx, sb + w * 2048 + (i) * 1024, xv, xs0 + (((s) + ROT) & 7) * 128 + (i) * 8192); \
    else blds16(rw, sb + 16384 + ((i) - 2) * 8192 + w * 1024, wv, ws0 + (((s) + ROT) & 7) * 64 * 3072 + ((i) - 2) * 1024); } while (0)
#define ISSUE(s) do { ISS(s, 0); ISS(s, 1); ISS(s, 2); ISS(s, 3); ISS(s, 4); } while (0)
  gu64* sync_z = (gu64*)(ws + WS_SYNC + 128 + hg * 128);
  ISSUE(0);
  STAMP(19);
  SCHED(); RAW_BAR(); SCHED();
  u32 pv2 = 0;
  SCHED();
  ISSUE(1);
  SCHED();
  epoch = load_epoch(ws);
  const rsrc_t rws = mk_rsrc(ws, WS_USED);
  const bool clear = tt == 0 && w == 7;
  if (clear) {
    const u32x4 z4 = {0u, 0u, 0u, 0u};
#pragma unroll
    for (int i = 0; i < 4; ++i) store16(z4, rws, lane * 16 + (int)WS_KV + hg * 4352 + 1024 * i);
    store16(z4, rws, (lane & 15) * 16 + (int)WS_KV + hg * 4352 + 4096);
  }
  SCHED();
  const int xo = (64 * wm + l15) * 128 + ((g ^ (lane & 7)) << 4);
  const int q4 = l15 >> 2, p4 = lane & 3;
  const int wo_ = 16384 + (8 * g + q4) * 128 + ((wh ^ (((q4 >> 1) & 1) | ((g & 1) << 1))) << 5) + 8 * p4;
  f32x4 aq[4], ak[4], av[4];
#pragma unroll
  for (int i = 0; i < 4; ++i) { aq[i] = (f32x4){EPS, EPS, EPS, EPS}; ak[i] = aq[i]; av[i] = (f32x4){0, 0, 0, 0}; }
  s16x8 xa[4], wa[3], xb[4], wb[3];
#define LDWF(wf, s, ks, st) wf[st] = cat8(tr_read(smem + ((s) & 3) * STAGE + wo_ + (st) * 8192 + (ks) * 4096), tr_read(smem + ((s) & 3) * STAGE + wo_ + (st) * 8192 + (ks) * 4096 + 512))
#define LDXF(xf, s, ks, mt) xf[mt] = LD(s16x8, smem + ((s) & 3) * STAGE + ((xo ^ ((ks) * 64)) + (mt) * 2048))
#define LOADF(xf, wf, s, ks) do { LDWF(wf, s, ks, 0); LDWF(wf, s, ks, 1); LDWF(wf, s, ks, 2); \
    LDXF(xf, s, ks, 0); LDXF(xf, s, ks, 1); LDXF(xf, s, ks, 2); LDXF(xf, s, ks, 3); } while (0)
#define MM3(xf, wf, mt) do { aq[mt] = MFMA16(wf[0], xf[mt], aq[mt]); ak[mt] = MFMA16(xf[mt], wf[1], ak[mt]); av[mt] = MFMA16(xf[mt], wf[2], av[mt]); \
    asm volatile("" : "+v"(aq[mt]), "+v"(ak[mt]), "+v"(av[mt])); } while (0)
#define HALF(xf, wf, xn, wn, ls, lk, is) do { SCHED(); MM3(xf, wf, 0); SCHED(); \
    if ((ls) >= 0) { LDWF(wn, ls, lk, 0); LDXF(xn, ls, lk, 0); LDWF(wn, ls, lk, 1); } if ((is) >= 0) ISS(is, 0); SCHED(); MM3(xf, wf, 1); SCHED(); \
    if ((ls) >= 0) { LDWF(wn, ls, lk, 2); LDXF(xn, ls, lk, 1); LDXF(xn, ls, lk, 2); } if ((is) >= 0) ISS(is, 1); SCHED(); MM3(xf, wf, 2); SCHED(); \
    if ((ls) >= 0) LDXF(xn, ls, lk, 3); if ((is) >= 0) { ISS(is, 2); ISS(is, 3); } SCHED(); MM3(xf, wf, 3); SCHED(); \
    if ((is) >= 0) ISS(is, 4); SCHED(); } while (0)
#define STEP(s, vm, i1, i2) do { WAIT_L0(); HALF(xa, wa, xb, wb, s, 1, i1); \
    WAIT_L0(); WAIT_VM(vm); RAW_BAR(); HALF(xb, wb, xa, wa, (s) + 1, 0, i2); } while (0)
  STAMP(1);
  if (clear) WAIT_VM(11); else WAIT_VM(6);
  RAW_BAR();
  STAMP(2);
  LOADF(xa, wa, 0, 0);
  STEP(0, 5, 2, 3);
  if (clear && lane == 0) __hip_atomic_store(sync_z, next_tag(epoch), RLX_AGENT);
  STAMP(3);
  STEP(1, 10, 4, 5);
  STEP(2, 10, -1, 6);
  STEP(3, 10, -1, 7);
  STAMP(4); CYC(20);
  if (w == 0) pv2 = touch_pages(ws, out, rt, lane);
  STEP(4, 10, -1, -1);
  u64 ready = __hip_atomic_load(sync_z, RLX_AGENT);
  STEP(5, 5, -1, 8);
  if (w < 4) __builtin_amdgcn_s_setprio(2);
  STEP(6, 8, 9, -1);
  STAMP(5); CYC(21); CYCD(20, 21); CYC(21);
  {
    const u64 tag = next_tag(epoch);
#pragma unroll 1
    for (int spin = 0; ready != tag && spin < (1 << 22); ++spin) {
      __builtin_amdgcn_s_sleep(2); ready = __hip_atomic_load(sync_z, RLX_AGENT); __builtin_amdgcn_s_waitcnt(0x0F70);
    }
  }
  WAIT_L0(); HALF(xa, wa, xb, wb, 7, 1, -1); WAIT_L0(); SCHED();
#pragma unroll
  for (int mt = 0; mt < 4; ++mt) aq[mt] = MFMA16(wb[0], xb[mt], aq[mt]);
  asm volatile("" : "+v"(aq[0]), "+v"(aq[1]), "+v"(aq[2]), "+v"(aq[3]));
  SCHED();
#pragma unroll
  for (int mt = 0; mt < 4; ++mt) { ak[mt] = MFMA16(xb[mt], wb[1], ak[mt]); av[mt] = MFMA16(xb[mt], wb[2], av[mt]); }
  asm volatile("" : "+v"(ak[0]), "+v"(ak[1]), "+v"(ak[2]), "+v"(ak[3]), "+v"(av[0]), "+v"(av[1]), "+v"(av[2]), "+v"(av[3]));
  SCHED();
  asm volatile("" :: "v"(pv2));
  STAMP(6);
#undef ISS
#undef ISSUE
#undef LOADF
#undef MM3
#undef HALF
#undef LDWF
#undef LDXF
#undef STEP
  const int h = 4 * hg + wh;
  const int pqv = (g >> 1) * 512 + l15 * 16 + (g & 1) * 256, pqs = (int)WS_PQ + ((tt * 4 + 2 * wm) * 32 + h) * 1024;
#pragma unroll
  for (int pr = 0; pr < 2; ++pr) {
    const f32x4 q0 = aq[2 * pr], q1 = aq[2 * pr + 1];
    const auto s0 = __builtin_amdgcn_permlane16_swap(pk_bf16(phi_f(q0[0]), phi_f(q0[1])), pk_bf16(phi_f(q1[0]), phi_f(q1[1])), false, false);
    const auto s1 = __builtin_amdgcn_permlane16_swap(pk_bf16(phi_f(q0[2]), phi_f(q0[3])), pk_bf16(phi_f(q1[2]), phi_f(q1[3])), false, false);
    store16((u32x4){s0[0], s1[0], s0[1], s1[1]}, rws, pqv + pqs + pr * 32768);
  }
  SCHED();
  STAMP(7);
  f32x4 kvt = {0, 0, 0, 0}, zz = {0, 0, 0, 0};
  const u32x4 ones_u = {0x3f803f80u, 0x3f803f80u, 0x3f803f80u, 0x3f803f80u};
#pragma unroll
  for (int pr = 0; pr < 2; ++pr) {
    u32x4 pk, vv;
#pragma unroll
    for (int m = 0; m < 2; ++m) {
      const f32x4 kk = ak[2 * pr + m], v4 = av[2 * pr + m];
      pk[2 * m] = pk_bf16(phi_f(kk[0]), phi_f(kk[1])); pk[2 * m + 1] = pk_bf16(phi_f(kk[2]), phi_f(kk[3]));
      vv[2 * m] = pk_bf16(v4[0], v4[1]); vv[2 * m + 1] = pk_bf16(v4[2], v4[3]);
    }
    kvt = MFMA16(__builtin_bit_cast(s16x8, vv), __builtin_bit_cast(s16x8, pk), kvt);
    zz = MFMA16(__builtin_bit_cast(s16x8, ones_u), __builtin_bit_cast(s16x8, pk), zz);
  }
  lptr red = smem + 4 * WOQ;
#pragma unroll
  for (int r = 0; r < 4; ++r) ST(float, red + (wm * 4 + wh) * 1088 + l15 * 4 + (4 * g + r) * 64) = kvt[r];
  if (g == 0) ST(float, red + (wm * 4 + wh) * 1088 + l15 * 4 + 1024) = zz[0];
  LBAR();
  STAMP(8);
  float* kvacc = (float*)(ws + WS_KV + (tt % KVC) * KV_CP) + hg * 1088 + tid;
  atomicAdd(kvacc, LD(float, red + tid * 4) + LD(float, red + 4352 + tid * 4));
  atomicAdd(kvacc + 512, LD(float, red + 2048 + tid * 4) + LD(float, red + 4352 + 2048 + tid * 4));
  if (tid < 64) atomicAdd(kvacc + 1024, LD(float, red + 4096 + tid * 4) + LD(float, red + 4352 + 4096 + tid * 4));
  STAMP(9); CYC(22); CYCD(21, 22);
}

static constexpr int TB_ROW = 544, TB_BUF = 16 * TB_ROW, TB_CH = 2 * TB_BUF;
static constexpr int KR_OFF = 4 * TB_CH, KR_CP = 4160, KR_CH = 2 * KR_CP;
static constexpr int U_OFF = KR_OFF + 4 * KR_CH, U_CH = (2048 + 240) * 2;
static constexpr int EX_OFF = U_OFF + 4 * U_CH;
static_assert(EX_OFF + 32768 <= 163840 && TB_CH == WOQ && P2_EX + 65536 <= 163840, "LDS map");

DI void conv_role(const u16* __restrict__ x, u32 (&pr)[5], u32x2 (&xr)[8], u32 pf, const u16* __restrict__ dsk, const u16* __restrict__ gw,
                  const u16* __restrict__ wo, char* __restrict__ ws, const u16* out, int rt, int cg, int ct, u64& epoch, lptr smem STAMP_ARG) {
  const int tid = threadIdx.x, lane = tid & 63;
  const int w = __builtin_amdgcn_readfirstlane(tid >> 6);
  const int ch = w & 3, half = w >> 2, g = lane >> 4, l15 = lane & 15;
  const int c = 4 * cg + ch;
  const rsrc_t rx = mk_rsrc(x, SEQ * DM * 2);
  u32 dsk2, pv = 0;
  u32x4 gx[4], gwv;
  const rsrc_t rws = mk_rsrc(ws, WS_USED);
  f32x4 acc[8];
#pragma unroll
  for (int i = 0; i < 8; ++i) acc[i] = (f32x4){0, 0, 0, 0};
#define PINC() do { asm volatile("" : "+v"(acc[0]), "+v"(acc[1]), "+v"(acc[2]), "+v"(acc[3]), "+v"(acc[4]), "+v"(acc[5]), "+v"(acc[6]), "+v"(acc[7])); SCHED(); } while (0)
  const int t0 = 16 * l15 + 4 * g;
  auto put_b = [&](int T, f32x4 y0, f32x4 y1) {
    lptr up = smem + U_OFF + ch * U_CH + 480 + 2 * t0 + 512 * T;
    u32 dd = dsk2;
    asm volatile("" : "+v"(dd));
    const float dsk_f = (c & 1) ? bf_hi(dd) : bf_lo(dd);
    const u32x2 u0 = LD(u32x2, up), u1 = LD(u32x2, up + 512);
    y0 += (f32x4){bf_lo(u0.x), bf_hi(u0.x), bf_lo(u0.y), bf_hi(u0.y)} * dsk_f;
    y1 += (f32x4){bf_lo(u1.x), bf_hi(u1.x), bf_lo(u1.y), bf_hi(u1.y)} * dsk_f;
    const auto sx = __builtin_amdgcn_permlane16_swap(pk_bf16(y0[0], y0[1]), pk_bf16(y1[0], y1[1]), false, false);
    const auto sy = __builtin_amdgcn_permlane16_swap(pk_bf16(y0[2], y0[3]), pk_bf16(y1[2], y1[3]), false, false);
    store16((u32x4){sx[0], sy[0], sx[1], sy[1]}, rws, (int)WS_BT + c * 4096 + 32 * l15 + 16 * (g >> 1) + 512 * (g & 1) + 512 * T);
  };
  if (w < 4) {
    const int cw = 4 * cg + w;
    if (lane < 8) ST(u32x4, smem + KR_OFF + w * KR_CH + (lane >> 2) * KR_CP + 4096 + 16 * (lane & 3)) = (u32x4){0u, 0u, 0u, 0u};
    __builtin_amdgcn_s_setprio(2);
    asm volatile("s_waitcnt vmcnt(0)" : "+v"(pr[0]), "+v"(pr[1]), "+v"(pr[2]), "+v"(pr[3]), "+v"(pr[4]));
    const u32 lar2 = pr[0], aim2 = pr[1], cre2 = pr[2], cim2 = pr[3], ldt_u = pr[4];
    const float dt = rbf(exp_f(bf_lo(ldt_u)));
    float al[2], th[2];
    al[0] = -rbf(exp_f(bf_lo(lar2))) * dt * LOG2E; al[1] = -rbf(exp_f(bf_hi(lar2))) * dt * LOG2E;
    th[0] = bf_lo(aim2) * dt * 0.15915494309189535f; th[1] = bf_hi(aim2) * dt * 0.15915494309189535f;
    STAMP(1);
    u32 ce = wave_umax(max(max(cre2 & 0x7f80u, (cre2 >> 16) & 0x7f80u), max(cim2 & 0x7f80u, (cim2 >> 16) & 0x7f80u))) >> 7;
    ce = ce < 1 ? 1 : (ce > 253 ? 253 : ce);
    const float cinv = __builtin_bit_cast(float, (254u - ce) << 23);
    const float kscale = 2.f * dt * __builtin_bit_cast(float, ce << 23);
    lptr tb = smem + w * TB_CH + lane * 8, tr = smem + w * TB_CH + 16 * g, kr = smem + KR_OFF + w * KR_CH;
#define PUT(buf, e, v) ST(u32x2, tb + (buf) * TB_BUF + (e) * TB_ROW) = (u32x2){pk_f16(v[0]), pk_f16(v[1])}
#define CONJ(v) ((f32x2){(v).x, -(v).y})
#define GETF(f, buf, row) do { lptr p_ = tr + (buf) * TB_BUF + (row) * TB_ROW; \
      _Pragma("unroll") for (int ks = 0; ks < 8; ++ks) f[ks] = LD(h16x8, p_ + 64 * ks); } while (0)
#define TILE(d, f) do { d = (f32x4){0, 0, 0, 0}; _Pragma("unroll") for (int ks = 0; ks < 8; ++ks) d = MFMA16H(f[ks], fx[ks], d); } while (0)
    f32x2 y[17][2], wB[2], w16[2], w32[2], xa[2], xb[2], aS[6][2], t[2], t2[2];
#pragma unroll
    for (int m = 0; m < 2; ++m) {
      y[1][m] = CONJ(cexp_p(al[m], th[m], 1.f)); w16[m] = cexp_p(al[m], th[m], 16.f); wB[m] = CONJ(cexp_p(al[m], th[m], 256.f));
      y[16][m] = CONJ(w16[m]);
    }
    xa[0] = (f32x2){bf_lo(cre2) * cinv, bf_lo(cim2) * cinv}; xa[1] = (f32x2){bf_hi(cre2) * cinv, bf_hi(cim2) * cinv};
    ST(u32x2, tb + TB_BUF) = (u32x2){0x3c00u, 0x3c00u};
    PUT(0, 0, xa); PUT(1, 1, y[1]);
    cmul4(xb, xa, w16, w32, w16, w16); cmul4(y[2], y[1], y[1], aS[0], wB, wB);
    PUT(0, 1, xb); PUT(1, 2, y[2]);
#pragma unroll
    for (int i = 1; i < 8; ++i) {
      cmul4(xa, xa, w32, xb, xb, w32);
      if (i < 7) cmul4(y[2 * i + 1], y[2 * i - 1], y[2], y[2 * i + 2], y[2 * i], y[2]); else cmul2(y[15], y[13], y[2]);
      PUT(0, 2 * i, xa); PUT(0, 2 * i + 1, xb); PUT(1, 2 * i + 1, y[2 * i + 1]);
      if (i < 7) PUT(1, 2 * i + 2, y[2 * i + 2]);
    }
    STAMP(2);
    h16x8 fx[8], fa[8], fb[8];
    f32x4 kd;
    const int gq = l15 >> 2, r3 = min(l15 & 3, 2), yk = 2044 - 16 * l15 - 4 * g;
    SCHED();
    GETF(fx, 0, l15); GETF(fa, 1, l15);
    SCHED();
    PUT(0, 0, wB);
#pragma unroll
    for (int m = 1; m < 9; m += 2) {
      cmul4(t, wB, y[2 * m], t2, wB, y[2 * m + 2]);
      if (m == 1) cmul4(aS[1], aS[0], wB, aS[2], aS[0], aS[0]);
      if (m == 3) cmul4(aS[3], aS[2], wB, aS[4], aS[2], aS[0]);
      if (m == 5) cmul2(aS[5], aS[4], wB);
      PUT(0, m, t); PUT(0, m + 1, t2);
    }
    SCHED();
    GETF(fb, 0, (int)((0x8766654443222100ull >> (4 * l15)) & 15));
    TILE(kd, fa);
    SCHED();
#pragma unroll
    for (int s = 0; s < 2; ++s) {
      PUT(1, 5 * s, aS[s]);
#pragma unroll
      for (int m = 1; m < 5; m += 2) { cmul4(t, aS[s], y[4 * m], t2, aS[s], y[4 * m + 4]); PUT(1, 5 * s + m, t); PUT(1, 5 * s + m + 1, t2); }
    }
    {
      const f32x4 k0 = kd * kscale;
      const u32 p32 = pk_bf16(k0[3], k0[2]), p10 = pk_bf16(k0[1], k0[0]);
      ST(u32x2, kr + 2 * yk) = (u32x2){p32, p10};
      ST(u16, kr + KR_CP + 2 * yk + 2) = (u16)p32;
      ST(u32, kr + KR_CP + 2 * yk + 4) = pk_bf16(k0[2], k0[1]);
      ST(u16, kr + KR_CP + 2 * yk + 8) = (u16)(p10 >> 16);
    }
    SCHED();
    GETF(fa, 1, 5 * (gq >> 1) + 2 * (gq & 1) + r3);
    TILE(kd, fb);
    SCHED();
#pragma unroll
    for (int s = 2; s < 6; ++s) {
      cmul4(t, aS[s], y[8], t2, aS[s], y[16]);
      PUT(0, 3 * (s - 2), aS[s]); PUT(0, 3 * (s - 2) + 1, t); PUT(0, 3 * (s - 2) + 2, t2);
    }
    {
      const f32x4 k1 = kd * kscale;
      const u32 q10 = pk_bf16(k1[1], k1[0]);
      ST(u32x2, kr + 2 * (yk - 256)) = (u32x2){pk_bf16(k1[3], k1[2]), q10};
      ST(u32x2, kr + KR_CP + 2 * (yk - 256)) = (u32x2){pk_bf16(k1[3], k1[3]), pk_bf16(k1[2], k1[1])};
      if (lane == 0) ST(u16, kr + KR_CP + 2 * 1792) = (u16)q10;
    }
    SCHED();
    GETF(fb, 0, 3 * gq + r3);
    TILE(kd, fa);
    STAMP(3);
    {
      const f32x4 k0 = kd * kscale;
      const int y2 = 2040 - (512 + 256 * (g >> 1) + 16 * l15 + 8 * (g & 1));
      const u32 b2 = pk_bf16(k0[2], k0[2]), b1 = pk_bf16(k0[1], k0[1]), b0 = pk_bf16(k0[0], k0[0]);
      ST(u32x4, kr + 2 * y2) = (u32x4){b2, b1, pk_bf16(k0[1], k0[0]), b0};
      ST(u32x4, kr + KR_CP + 2 * y2) = (u32x4){b2, pk_bf16(k0[2], k0[1]), b1, b0};
    }
    TILE(kd, fb);
    {
      const f32x4 k1 = kd * kscale;
      const int y3 = 2032 - (1024 + 256 * g + 16 * l15);
      const u32 a2 = pk_bf16(k1[2], k1[2]), a1 = pk_bf16(k1[1], k1[1]), a0 = pk_bf16(k1[0], k1[0]);
      ST(u32x4, kr + 2 * y3) = (u32x4){a2, a2, a1, a1};
      ST(u32x4, kr + 2 * y3 + 16) = (u32x4){a1, pk_bf16(k1[1], k1[0]), a0, a0};
      ST(u32x4, kr + KR_CP + 2 * y3) = (u32x4){a2, a2, pk_bf16(k1[2], k1[1]), a1};
      ST(u32x4, kr + KR_CP + 2 * y3 + 16) = (u32x4){a1, a1, a0, a0};
    }
#undef PUT
#undef GETF
#undef TILE
    SCHED();
    __builtin_amdgcn_s_setprio(0);
    WAIT_L0(); SCHED();
    fetch_wo(wo, ct, ch, lane, smem);
    STAMP(4);
  } else {
    const int t2 = (tid + 16 * cg) & 255;
    if (lane < 30) ST(u32x4, smem + U_OFF + ch * U_CH + lane * 16) = (u32x4){0u, 0u, 0u, 0u};
    STAMP(1);
#pragma unroll
    for (int hf = 0; hf < 2; ++hf) {
      lptr ub = smem + U_OFF + 480 + t2 * 8 + hf * 2048;
      if (hf == 0) asm volatile("s_waitcnt vmcnt(4)" : "+v"(xr[0]), "+v"(xr[1]), "+v"(xr[2]), "+v"(xr[3]) : "v"(pf));
      else asm volatile("s_waitcnt vmcnt(0)" : "+v"(xr[4]), "+v"(xr[5]), "+v"(xr[6]), "+v"(xr[7]));
      const u32x2 x0 = xr[4 * hf], x1 = xr[4 * hf + 1], x2 = xr[4 * hf + 2], x3 = xr[4 * hf + 3];
      ST(u32x2, ub + 0 * U_CH) = (u32x2){__builtin_amdgcn_perm(x1.x, x0.x, 0x05040100u), __builtin_amdgcn_perm(x3.x, x2.x, 0x05040100u)};
      ST(u32x2, ub + 1 * U_CH) = (u32x2){__builtin_amdgcn_perm(x1.x, x0.x, 0x07060302u), __builtin_amdgcn_perm(x3.x, x2.x, 0x07060302u)};
      ST(u32x2, ub + 2 * U_CH) = (u32x2){__builtin_amdgcn_perm(x1.y, x0.y, 0x05040100u), __builtin_amdgcn_perm(x3.y, x2.y, 0x05040100u)};
      ST(u32x2, ub + 3 * U_CH) = (u32x2){__builtin_amdgcn_perm(x1.y, x0.y, 0x07060302u), __builtin_amdgcn_perm(x3.y, x2.y, 0x07060302u)};
      SCHED();
    }
    STAMP(4);
  }
  lptr ka[8];
  {
    const int yb = 2031 - l15 + 8 * g - 128 * half - 96 - 1792;
    lptr k7 = smem + KR_OFF + ch * KR_CH + (yb & 1) * KR_CP + ((yb + 1) >> 1) * 4;
#pragma unroll
    for (int n = 0; n < 8; ++n) { ka[n] = k7 + 512 * (7 - n); asm volatile("" : "+v"(ka[n])); }
  }
  lptr ua = smem + U_OFF + ch * U_CH + (8 * g + 16 * l15 + 224 - 128 * half - 96) * 2;
  asm volatile("" : "+v"(ua));
  STAMP(6);
  LBAR();
  STAMP(7); CYC(20);
  {
    u32x4 A0[8], A1[8]; s16x8 B0[8], B1[8];
#define LOADC(A, B, rr) do { _Pragma("unroll") for (int n = 0; n < 8; ++n) { \
      lptr kp = ka[n] + 64 * (3 - (rr)); \
      A[n] = (u32x4){LD(u32, kp), LD(u32, kp + 4), LD(u32, kp + 8), LD(u32, kp + 12)}; \
      B[n] = LD(s16x8, ua + 64 * (3 - (rr)) + 512 * n); } } while (0)
#define MMC(A, B) do { _Pragma("unroll") for (int nu = 0; nu < 8; ++nu) _Pragma("unroll") for (int mu = 0; mu < 8 - nu; ++mu) \
      acc[nu + mu] = MFMA16(__builtin_bit_cast(s16x8, A[nu]), B[mu], acc[nu + mu]); } while (0)
#define MIXC() do { _Pragma("unroll") for (int q = 0; q < 12; ++q) { __builtin_amdgcn_sched_group_barrier(0x8, MIXM, 0); __builtin_amdgcn_sched_group_barrier(0x100, 2, 0); } \
      __builtin_amdgcn_sched_group_barrier(0x8, 36 - 12 * MIXM, 0); } while (0)
    LOADC(A0, B0, 0);
    SCHED();
    dsk2 = *(const u32*)(dsk + (c & ~1));
    epoch = load_epoch(ws);
    if (half) {
#pragma unroll
      for (int r = 0; r < 4; ++r) gx[r] = __builtin_amdgcn_raw_buffer_load_b128(rx, lane * 16 + 1024 * r, (16 * cg + 4 * ch) * 1024, 0);
      gwv = *(const u32x4*)(gw + 8 * lane);
      pv = touch_pages(ws, out, rt, lane);
    }
    SCHED();
    WAIT_L0(); SCHED(); LOADC(A1, B1, 1); MMC(A0, B0); MIXC(); PINC();
    WAIT_L0(); SCHED(); LOADC(A0, B0, 2); MMC(A1, B1); MIXC(); PINC();
    WAIT_L0(); SCHED(); LOADC(A1, B1, 3); MMC(A0, B0); MIXC(); PINC();
    WAIT_L0(); SCHED(); MMC(A1, B1); PINC();
#undef LOADC
#undef MMC
#undef MIXC
#undef PINC
  }
  STAMP(8); CYC(21); CYCD(20, 21);
  {
    lptr ex = smem + EX_OFF + w * 4096 + lane * 16, ei = smem + EX_OFF + (w ^ 4) * 4096 + lane * 16;
    if (half) {
#pragma unroll
      for (int i = 0; i < 4; ++i) ST(f32x4, ex + 1024 * i) = acc[4 + i];
      LBAR();
      put_b(0, acc[0] + LD(f32x4, ei), acc[1] + LD(f32x4, ei + 1024));
      put_b(2, acc[2] + LD(f32x4, ei + 2048), acc[3] + LD(f32x4, ei + 3072));
      asm volatile("" :: "v"(pv));
      float sg[4];
#pragma unroll
      for (int r = 0; r < 4; ++r) {
        float s0 = 0.f;
#pragma unroll
        for (int i = 0; i < 4; ++i) s0 = __builtin_amdgcn_fdot2_f32_bf16(u2b(gx[r][i]), u2b(gwv[i]), s0, false);
        sg[r] = rbf(__builtin_amdgcn_rcpf(1.f + __builtin_amdgcn_exp2f(-LOG2E * rbf(wave_sum(s0)))));
      }
      if (lane == 63) store16(__builtin_bit_cast(u32x4, (f32x4){sg[0], sg[1], sg[2], sg[3]}), rws, (int)WS_G + (16 * cg + 4 * ch) * 4 + (lane - 63));
    } else {
#pragma unroll
      for (int i = 0; i < 4; ++i) ST(f32x4, ex + 1024 * i) = acc[i];
      LBAR();
      put_b(4, acc[4] + LD(f32x4, ei), acc[5] + LD(f32x4, ei + 1024));
      put_b(6, acc[6] + LD(f32x4, ei + 2048), acc[7] + LD(f32x4, ei + 3072));
      asm volatile(".Lfused_code_tail:");
    }
  }
  STAMP(9);
}

struct Tags { u64 a, b; };
DI Tags poll_load(char* ws, int off, int lane) {
  gu64* p = (gu64*)(ws + WS_FLAG + off) + lane;
  return Tags{__hip_atomic_load(p, RLX_AGENT), __hip_atomic_load(p + 64, RLX_AGENT)};
}
DI void poll_wait(char* ws, int off, int lane, u64 tag, Tags& f, u64 ma, u64 mb) {
#pragma unroll 1
  for (int spin = 0; spin < (1 << 20); ++spin) {
    if ((__ballot(f.a != tag) & ma) == 0 && (__ballot(f.b != tag) & mb) == 0) break;
    __builtin_amdgcn_s_sleep(POLL_SLEEP);
    f = poll_load(ws, off, lane);
    __builtin_amdgcn_s_waitcnt(0x0F70);
  }
}

DI void out_role(char* __restrict__ ws, u16* __restrict__ out, int rt, int ct, u64 tag, Tags fl, lptr smem STAMP_ARG) {
  const int tid = threadIdx.x, lane = tid & 63;
  const int w = __builtin_amdgcn_readfirstlane(tid >> 6);
  const int th = w & 1, kq = w >> 1, hh = lane >> 5, l31 = lane & 31;
  const rsrc_t rws = mk_rsrc(ws, WS_USED);
  const int foff = (blockIdx.x & 31) * 2048;
  if (w == 0) poll_wait(ws, foff, lane, tag, fl, ~0ull, ~0ull);
  RAW_BAR();
  STAMP(11);
  if (w == 1) fl = poll_load(ws, foff + 1024, lane);
  f32x4 kvr[KVC][5];
#pragma unroll
  for (int cp = 0; cp < KVC; ++cp) {
#pragma unroll
    for (int i = 0; i < 4; ++i) kvr[cp][i] = __builtin_bit_cast(f32x4, __builtin_amdgcn_raw_buffer_load_b128(rws, tid * 16, (int)WS_KV + cp * KV_CP + 8192 * i, 16));
    kvr[cp][4] = __builtin_bit_cast(f32x4, __builtin_amdgcn_raw_buffer_load_b128(rws, (tid & 127) * 16, (int)WS_KV + cp * KV_CP + 32768, 16));
  }
  SCHED(); RAW_BAR(); SCHED();
  u32x4 pf[8];
#define LOAD_PQ(i0, i1) do { _Pragma("unroll") for (int i = i0; i < i1; ++i) \
    pf[i] = __builtin_amdgcn_raw_buffer_load_b128(rws, lane * 16, (int)WS_PQ + ((2 * rt + th) * 32 + 8 * kq + i) * 1024, 16); } while (0)
  LOAD_PQ(0, 3);
  STAMP(12);
#pragma unroll
  for (int i = 0; i < 5; ++i) {
    f32x4 v = kvr[0][i];
#pragma unroll
    for (int cp = 1; cp < KVC; ++cp) v += kvr[cp][i];
    ST(u32x2, smem + P2_KV + (i < 4 ? tid + 512 * i : 2048 + (tid & 127)) * 8) = (u32x2){pk_bf16(v[0], v[1]), pk_bf16(v[2], v[3])};
  }
  f32x16 acc[2];
#pragma unroll
  for (int i = 0; i < 16; ++i) { acc[0][i] = 0.f; acc[1][i] = 0.f; }
  f32x16 cinit = acc[0];
  cinit[8] = EPS;
  lptr kva = smem + P2_KV + 8 * kq * 544 + min(l31, 16) * 32 + hh * 16;
  const int blk = (lane >> 4) & 1, q4 = (lane & 15) >> 2, p4 = lane & 3;
  const int sw = ((q4 >> 1) & 1) << 6;
  lptr wa0 = smem + P2_WO + kq * WOQ + (4 * hh + q4) * 128 + blk * 32 + p4 * 8 + sw;
  lptr wa1 = smem + P2_WO + kq * WOQ + (4 * hh + q4) * 128 + blk * 32 + p4 * 8 + (64 ^ sw);
  STAMP(13);
  if (w == 1) { const u32 ok = __all(fl.a == tag && fl.b == tag); if (lane == 0) ST(u32, smem + P2_OK) = ok; }
  LBAR();
  LOAD_PQ(3, 8);
#undef LOAD_PQ
  const bool early = __builtin_amdgcn_readfirstlane(LD(u32, smem + P2_OK)) != 0;
  u32x4 btr; float gt;
#define LOAD_B() do { \
    btr = __builtin_amdgcn_raw_buffer_load_b128(rws, (lane >> 2) * 4096 + (lane & 3) * 16, (int)WS_BT + (64 * ct + 16 * kq) * 4096 + 128 * rt + 64 * th, 16); \
    gt = u2f(__builtin_amdgcn_raw_buffer_load_b32(rws, l31 * 4, (int)WS_G + (64 * rt + 32 * th) * 4, 16)); } while (0)
  if (early) LOAD_B();
  STAMP(14);
  {
    f32x16 d[2]; s16x8 kf[8], wf[2][2]; u32x4 of[2];
#define OMM(i) do { d[(i) & 1] = MFMA32(kf[i], __builtin_bit_cast(s16x8, pf[i]), cinit); asm volatile("" : "+v"(d[(i) & 1])); } while (0)
#define NORM(i) do { const f32x16& dd = d[(i) & 1]; const float inv = __builtin_amdgcn_rcpf(dd[8]); \
      of[(i) & 1] = (u32x4){pk_bf16(dd[0] * inv, dd[1] * inv), pk_bf16(dd[2] * inv, dd[3] * inv), pk_bf16(dd[4] * inv, dd[5] * inv), pk_bf16(dd[6] * inv, dd[7] * inv)}; \
      asm volatile("" : "+v"(of[(i) & 1]) : "v"(dd)); } while (0)
#define LDW(i) do { wf[(i) & 1][0] = cat8(tr_read(wa0 + (i) * 2048), tr_read(wa0 + (i) * 2048 + 1024)); \
      wf[(i) & 1][1] = cat8(tr_read(wa1 + (i) * 2048), tr_read(wa1 + (i) * 2048 + 1024)); } while (0)
#define AMM(i) do { acc[0] = MFMA32(wf[(i) & 1][0], __builtin_bit_cast(s16x8, of[(i) & 1]), acc[0]); \
      acc[1] = MFMA32(wf[(i) & 1][1], __builtin_bit_cast(s16x8, of[(i) & 1]), acc[1]); asm volatile("" : "+v"(acc[0]), "+v"(acc[1])); } while (0)
#pragma unroll
    for (int i = 0; i < 8; ++i) kf[i] = LD(s16x8, kva + i * 544);
    LDW(0);
    SCHED();
    OMM(0); OMM(1); SCHED(); NORM(0); SCHED();
#pragma unroll
    for (int i = 0; i < 8; ++i) {
      if (i + 1 < 8) LDW(i + 1);
      SCHED();
      AMM(i);
      if (i + 2 < 8) OMM(i + 2);
      SCHED();
      if (i + 1 < 8) NORM(i + 1);
      SCHED();
    }
#undef OMM
#undef NORM
#undef LDW
#undef AMM
  }
  STAMP(15);
#pragma unroll
  for (int q = 0; q < 4; ++q) {
    lptr ex = smem + P2_EX + ((2 * q + th) * 4 + kq) * 2048 + lane * 16;
    const f32x16& s = acc[q >> 1];
    ST(f32x4, ex) = (f32x4){s[8 * (q & 1)], s[8 * (q & 1) + 1], s[8 * (q & 1) + 2], s[8 * (q & 1) + 3]};
    ST(f32x4, ex + 1024) = (f32x4){s[8 * (q & 1) + 4], s[8 * (q & 1) + 5], s[8 * (q & 1) + 6], s[8 * (q & 1) + 7]};
  }
  if (!early && w == 1) poll_wait(ws, foff + 1024, lane, tag, fl, ~0ull, ~0ull);
  LBAR();
  if (!early) LOAD_B();
#undef LOAD_B
  if (blockIdx.x == 0 && tid == 0) __hip_atomic_store((gu64*)(ws + WS_SYNC), tag, RLX_AGENT);
  STAMP(16);
  lptr ei = smem + P2_EX + w * 8192 + lane * 16;
  ST(u32x4, smem + P2_BT + w * 1024 + lane * 16) = btr;
  const float g1 = rbf(1.f - gt);
  lptr ba = smem + P2_BT + w * 1024 + (4 * hh + q4) * 64 + blk * 32 + p4 * 8;
  u32x2 res[2];
#pragma unroll
  for (int i = 0; i < 2; ++i) {
    const f32x4 o = (LD(f32x4, ei + 1024 * i) + LD(f32x4, ei + 2048 + 1024 * i)) + (LD(f32x4, ei + 4096 + 1024 * i) + LD(f32x4, ei + 6144 + 1024 * i));
    const u32x2 bb = __builtin_bit_cast(u32x2, tr_read(ba + i * 512));
    res[i] = (u32x2){pk_bf16(gt * o[0] + g1 * bf_lo(bb.x), gt * o[1] + g1 * bf_hi(bb.x)), pk_bf16(gt * o[2] + g1 * bf_lo(bb.y), gt * o[3] + g1 * bf_hi(bb.y))};
  }
  const auto sx = __builtin_amdgcn_permlane32_swap(res[0].x, res[1].x, false, false);
  const auto sy = __builtin_amdgcn_permlane32_swap(res[0].y, res[1].y, false, false);
  *(u32x4*)(out + (size_t)(64 * rt + 32 * th + l31) * DM + 64 * ct + 16 * kq + 8 * hh) = (u32x4){sx[0], sy[0], sx[1], sy[1]};
  STAMP(17);
}

__global__ __launch_bounds__(512, 2) void fused(const u16* __restrict__ x, const u16* __restrict__ wqkv, const u16* __restrict__ wo,
                                                const u16* __restrict__ lar, const u16* __restrict__ aim, const u16* __restrict__ cre,
                                                const u16* __restrict__ cim, const u16* __restrict__ ldt, const u16* __restrict__ dsk,
                                                const u16* __restrict__ gw, char* __restrict__ ws, u16* __restrict__ out) {
  extern __shared__ __attribute__((aligned(16))) char smem_[];
  lptr smem = (lptr)smem_;
  STAMP_DECL; STAMP(0);
  asm volatile("" :: "s"(x), "s"(wqkv), "s"(wo), "s"(lar), "s"(aim), "s"(cre), "s"(cim), "s"(ldt), "s"(dsk), "s"(gw), "s"(ws), "s"(out));
  STAMP(18);
  const int b = blockIdx.x, xcd = b & 7, slot = b >> 3, idx = slot >> 1;
  const int rt = 8 * (xcd >> 1) + (slot & 7), ct = 4 * (xcd & 1) + (slot >> 3);
  const int tt = 4 * (xcd >> 1) + (idx & 3), hg = 4 * (xcd & 1) + (idx >> 2);
  const int cg = xcd * 16 + idx;
  u64 epoch; u32 tmp0, tmp1, tmp2; u64 pc;
  u32 pr[5]; u32x2 xr[8];
  asm volatile(
      "s_bitcmp1_b32 %[b], 3\n\t"
      "s_cbranch_scc0 .Lpre_end%=\n\t"
      "s_cmp_gt_u32 %[wv], 3\n\t"
      "s_cbranch_scc1 .Lpre_b%=\n\t"
      "v_lshlrev_b32 %[p4], 2, %[tid]\n\t"
      "v_lshl_add_u32 %[p4], %[cg], 10, %[p4]\n\t"
      "global_load_dword %[p0], %[p4], %[lar]\n\t"
      "global_load_dword %[p1], %[p4], %[aim]\n\t"
      "global_load_dword %[p2], %[p4], %[cre]\n\t"
      "global_load_dword %[p3], %[p4], %[cim]\n\t"
      "v_lshrrev_b32 %[t0], 7, %[p4]\n\t"
      "v_and_b32 %[t0], 0x3fe, %[t0]\n\t"
      "global_load_ushort %[p4], %[t0], %[ldt]\n\t"
      "s_barrier\n\t"
      "s_branch .Lpre_end%=\n"
      ".Lpre_b%=:\n\t"
      "s_getpc_b64 %[pc]\n\t"
      "v_and_b32 %[t0], 0xff, %[tid]\n\t"
      "v_lshlrev_b32 %[t0], 7, %[t0]\n\t"
      "v_mov_b32 %[t1], .Lfused_code_tail-.Lpre_b%=-4\n\t"
      "v_med3_i32 %[t0], 0, %[t0], %[t1]\n\t"
      "global_load_dword %[t2], %[t0], %[pc]\n\t"
      "v_lshl_add_u32 %[t0], %[cg], 4, %[tid]\n\t"
      "v_and_b32 %[t0], 0xff, %[t0]\n\t"
      "v_lshlrev_b32 %[t0], 12, %[t0]\n\t"
      "v_lshl_add_u32 %[t0], %[cg], 3, %[t0]\n\t"
      "v_add_u32 %[t1], 0x100000, %[t0]\n\t"
      "s_barrier\n\t"
      "global_load_dwordx2 %[x0], %[t0], %[x]\n\t"
      "global_load_dwordx2 %[x1], %[t0], %[x] offset:1024\n\t"
      "global_load_dwordx2 %[x2], %[t0], %[x] offset:2048\n\t"
      "global_load_dwordx2 %[x3], %[t0], %[x] offset:3072\n\t"
      "global_load_dwordx2 %[x4], %[t1], %[x]\n\t"
      "global_load_dwordx2 %[x5], %[t1], %[x] offset:1024\n\t"
      "global_load_dwordx2 %[x6], %[t1], %[x] offset:2048\n\t"
      "global_load_dwordx2 %[x7], %[t1], %[x] offset:3072\n"
      ".Lpre_end%=:"
      : [p0] "=&v"(pr[0]), [p1] "=&v"(pr[1]), [p2] "=&v"(pr[2]), [p3] "=&v"(pr[3]), [p4] "=&v"(pr[4]),
        [x0] "=&v"(xr[0]), [x1] "=&v"(xr[1]), [x2] "=&v"(xr[2]), [x3] "=&v"(xr[3]), [x4] "=&v"(xr[4]), [x5] "=&v"(xr[5]), [x6] "=&v"(xr[6]), [x7] "=&v"(xr[7]),
        [t0] "=&v"(tmp0), [t1] "=&v"(tmp1), [t2] "=&v"(tmp2), [pc] "=&s"(pc)
      : [b] "s"(b), [wv] "s"(__builtin_amdgcn_readfirstlane(threadIdx.x >> 6)), [tid] "v"(threadIdx.x), [cg] "s"(cg),
        [lar] "s"(lar), [aim] "s"(aim), [cre] "s"(cre), [cim] "s"(cim), [ldt] "s"(ldt), [x] "s"(x)
      : "scc", "memory");
  if (__builtin_expect(slot & 1, 0)) conv_role(x, pr, xr, tmp2, dsk, gw, wo, ws, out, rt, cg, ct, epoch, smem STAMP_PASS);
  else gemm_role(x, wqkv, wo, ct, ws, out, rt, tt, hg, epoch, smem STAMP_PASS);
  const int lane = threadIdx.x & 63, w = __builtin_amdgcn_readfirstlane(threadIdx.x >> 6);
  const u64 tag = next_tag(epoch);
  Tags fl = {0, 0};
  if (w == 0) fl = poll_load(ws, (b & 31) * 2048, lane);
  SCHED();
  WAIT_VM(0);
  __builtin_amdgcn_s_setprio(0);
  STAMP(10);
  LBAR();
  if (w == 7 && lane < 32) __hip_atomic_store((gu64*)(ws + WS_FLAG + lane * 2048) + ((slot & 1) ? 128 + cg : tt * 8 + hg), tag, RLX_AGENT);
  out_role(ws, out, rt, ct, tag, fl, smem STAMP_PASS);
  STAMP_FLUSH(ws);
}

extern "C" void kernel_launch(void* const* d_in, const int* in_sizes, int n_in, void* d_out, int out_size, void* d_ws,
                              size_t ws_size, hipStream_t stream) {
  (void)in_sizes; (void)n_in; (void)out_size; (void)ws_size;
  (void)hipFuncSetAttribute((const void*)fused, hipFuncAttributeMaxDynamicSharedMemorySize, 163840);
  fused<<<256, 512, 163840, stream>>>((const u16*)d_in[0], (const u16*)d_in[1], (const u16*)d_in[2], (const u16*)d_in[4],
                                      (const u16*)d_in[5], (const u16*)d_in[6], (const u16*)d_in[7], (const u16*)d_in[8],
                                      (const u16*)d_in[9], (const u16*)d_in[10], (char*)d_ws, (u16*)d_out);
}
```

```cpp
#include <hip/hip_runtime.h>
#include <hip/hip_bf16.h>
#include <stdint.h>

typedef unsigned int u32;
typedef unsigned short u16;
typedef __attribute__((ext_vector_type(8))) short s16x8;
typedef __attribute__((ext_vector_type(4))) short s16x4;
typedef __attribute__((ext_vector_type(8))) _Float16 h16x8;
typedef __attribute__((ext_vector_type(2))) _Float16 h16x2;
typedef __attribute__((ext_vector_type(2))) __bf16 b16x2;
typedef __attribute__((ext_vector_type(2))) float f32x2;
typedef __attribute__((ext_vector_type(4))) float f32x4;
typedef __attribute__((ext_vector_type(16))) float f32x16;
typedef __attribute__((ext_vector_type(2))) u32 u32x2;
typedef __attribute__((ext_vector_type(4))) u32 u32x4;
#define LDS_AS __attribute__((address_space(3)))
#define DI __device__ __forceinline__

static constexpr int SEQ = 2048, DM = 512;
#define KVC 1
static constexpr size_t WS_KV = (6u << 20) + 65536;
static constexpr int KV_CP = 36864;
static constexpr size_t WS_G = 65536;
static constexpr size_t WS_PQ = 1u << 20;
static constexpr size_t WS_BT = (4u << 20) + 4096;
static constexpr int WS_USED = 8 << 20;
#define SYNCO 49152
static constexpr size_t WS_SYNC = SYNCO;
static constexpr size_t WS_FLAG = 131072;
typedef unsigned long long u64;
typedef __attribute__((address_space(1))) u64 gu64;
typedef __attribute__((address_space(1))) u32 gu32;
#define RLX_AGENT __ATOMIC_RELAXED, __HIP_MEMORY_SCOPE_AGENT
__device__ __forceinline__ u64 next_tag(u64 e) { return e * 6364136223846793005ull + 1442695040888963407ull; }

#define STAMP_ARG
#define STAMP_PASS
#define STAMP_DECL do {} while (0)
#define STAMP(i) do {} while (0)
#define CYC(i) do {} while (0)
#define CYCD(i, j) do {} while (0)
#define STAMP_FLUSH(wsp) do {} while (0)
#define MIXM 2
#define TURN_AT 5
#define POLL_SLEEP 2
#define ST_AUX 16
#define WAIT_VM(n) asm volatile("s_waitcnt vmcnt(" #n ")" ::: "memory")
#define WAIT_L0() __builtin_amdgcn_s_waitcnt(0xC07F)
#define RAW_BAR() __builtin_amdgcn_s_barrier()
#define SCHED() __builtin_amdgcn_sched_barrier(0)
#define LBAR() do { WAIT_L0(); asm volatile("" ::: "memory"); RAW_BAR(); asm volatile("" ::: "memory"); } while (0)
typedef LDS_AS char* lptr;
#define LD(T, p) (*(const LDS_AS T*)(p))
#define ST(T, p) (*(LDS_AS T*)(p))

DI u32 pk_bf16(float a, float b) { return __builtin_bit_cast(u32, __builtin_convertvector((f32x2){a, b}, b16x2)); }
DI u32 pk_f16(f32x2 v) { return __builtin_bit_cast(u32, __builtin_convertvector(v, h16x2)); }
DI float bf_lo(u32 v) { return __builtin_bit_cast(float, v << 16); }
DI float bf_hi(u32 v) { return __builtin_bit_cast(float, v & 0xffff0000u); }
DI float u2f(u32 v) { return __builtin_bit_cast(float, v); }
DI b16x2 u2b(u32 v) { return __builtin_bit_cast(b16x2, v); }
DI float rbf(float v) { return bf_lo(pk_bf16(v, v)); }
static constexpr float LOG2E = 1.44269504088896341f, EPS = 1e-6f;
DI float phi_f(float t) {
  const float hi = __builtin_bit_cast(float, max(__builtin_bit_cast(int, t), __builtin_bit_cast(int, EPS)));
  return hi + __builtin_amdgcn_fmed3f(__builtin_amdgcn_exp2f(__builtin_fmaf(t, LOG2E, -EPS * LOG2E)), 0.f, 1.f);
}
DI float exp_f(float v) {
  const float p = v * LOG2E;
  const float r = __builtin_fmaf(v, 1.925963033500011e-8f, __builtin_fmaf(v, LOG2E, -p));
  return __builtin_amdgcn_exp2f(p) * __builtin_fmaf(r, 0.6931471805599453f, 1.f);
}
#define DPP_F(v, ctrl, rmask, bc) __builtin_bit_cast(float, __builtin_amdgcn_update_dpp(0, __builtin_bit_cast(int, v), ctrl, rmask, 0xf, bc))
DI float wave_sum(float r) {
  r += DPP_F(r, 0x111, 0xf, true); r += DPP_F(r, 0x112, 0xf, true); r += DPP_F(r, 0x114, 0xf, true); r += DPP_F(r, 0x118, 0xf, true);
  r += DPP_F(r, 0x142, 0xa, false); r += DPP_F(r, 0x143, 0xc, false);
  return r;
}
DI u32 wave_umax(u32 r) {
#define DPP_U(v, ctrl, rmask, bc) (u32)__builtin_amdgcn_update_dpp(0, (int)(v), ctrl, rmask, 0xf, bc)
  r = max(r, DPP_U(r, 0x111, 0xf, true)); r = max(r, DPP_U(r, 0x112, 0xf, true)); r = max(r, DPP_U(r, 0x114, 0xf, true));
  r = max(r, DPP_U(r, 0x118, 0xf, true)); r = max(r, DPP_U(r, 0x142, 0xa, false)); r = max(r, DPP_U(r, 0x143, 0xc, false));
  return (u32)__builtin_amdgcn_readlane((int)r, 63);
}
DI s16x4 tr_read(lptr p) { return __builtin_amdgcn_ds_read_tr16_b64_v4i16((LDS_AS s16x4*)p); }
DI s16x8 cat8(s16x4 a, s16x4 b) { return __builtin_shufflevector(a, b, 0, 1, 2, 3, 4, 5, 6, 7); }
typedef __amdgpu_buffer_rsrc_t rsrc_t;
DI rsrc_t mk_rsrc(const void* p, int bytes) { return __builtin_amdgcn_make_buffer_rsrc((void*)p, 0, bytes, 0x00020000); }
DI void store16(u32x4 v, rsrc_t r, int off) { __builtin_amdgcn_raw_buffer_store_b128(v, r, off, 0, ST_AUX); }
DI void blds16(rsrc_t r, lptr l, int voff, int soff) { __builtin_amdgcn_raw_ptr_buffer_load_lds(r, (LDS_AS void*)l, 16, voff, soff, 0, 0); }
#define CM_MUL(t, a, w) "v_pk_mul_f32 " t ", " a ", " w " op_sel:[1,1] op_sel_hi:[1,0]\n\t"
#define CM_FMA(r, a, w, t) "v_pk_fma_f32 " r ", " a ", " w ", " t " op_sel_hi:[0,1,1] neg_lo:[0,0,1]\n\t"
DI void cmul2(f32x2 (&r)[2], const f32x2 (&a)[2], const f32x2 (&w)[2]) {
  f32x2 r0, r1;
  asm(CM_MUL("%0", "%2", "%4") CM_MUL("%1", "%3", "%5") CM_FMA("%0", "%2", "%4", "%0") CM_FMA("%1", "%3", "%5", "%1")
      : "=&v"(r0), "=&v"(r1) : "v"(a[0]), "v"(a[1]), "v"(w[0]), "v"(w[1]));
  r[0] = r0; r[1] = r1;
}
DI void cmul4(f32x2 (&r)[2], const f32x2 (&a)[2], const f32x2 (&w)[2], f32x2 (&q)[2], const f32x2 (&b)[2], const f32x2 (&v)[2]) {
  f32x2 r0, r1, q0, q1;
  asm(CM_MUL("%0", "%4", "%6") CM_MUL("%1", "%5", "%7") CM_MUL("%2", "%8", "%10") CM_MUL("%3", "%9", "%11")
      CM_FMA("%0", "%4", "%6", "%0") CM_FMA("%1", "%5", "%7", "%1") CM_FMA("%2", "%8", "%10", "%2") CM_FMA("%3", "%9", "%11", "%3")
      : "=&v"(r0), "=&v"(r1), "=&v"(q0), "=&v"(q1)
      : "v"(a[0]), "v"(a[1]), "v"(w[0]), "v"(w[1]), "v"(b[0]), "v"(b[1]), "v"(v[0]), "v"(v[1]));
  r[0] = r0; r[1] = r1; q[0] = q0; q[1] = q1;
}
DI f32x2 cexp_p(float alpha, float theta, float p) {
  float m = __builtin_amdgcn_exp2f(alpha * p);
  float f = __builtin_amdgcn_fractf(theta * p);
  return (f32x2){m * __builtin_amdgcn_cosf(f), m * __builtin_amdgcn_sinf(f)};
}
#define MFMA16(a, b, c) __builtin_amdgcn_mfma_f32_16x16x32_bf16((a), (b), (c), 0, 0, 0)
#define MFMA16H(a, b, c) __builtin_amdgcn_mfma_f32_16x16x32_f16((a), (b), (c), 0, 0, 0)
#define MFMA32(a, b, c) __builtin_amdgcn_mfma_f32_32x32x16_bf16((a), (b), (c), 0, 0, 0)

static constexpr int STAGE = 40960;
static constexpr int WOQ = 17408;
static constexpr int P2_WO = 0, P2_KV = 4 * WOQ, P2_OK = P2_KV + 17408, P2_BT = P2_KV + 18432, P2_EX = P2_BT + 8192;

DI u64 load_epoch(char* ws) {
  int vzero;
  asm volatile("v_mov_b32 %0, 0" : "=v"(vzero));
  return __hip_atomic_load((gu64*)(ws + WS_SYNC + vzero), RLX_AGENT);
}
DI u32 touch_pages(const char* ws, const u16* out, int rt, int lane) {
  const char* p = lane < 4 ? ws + (size_t)((0xf871u >> (4 * lane)) & 15) * (512u << 10) : (const char*)out + (size_t)rt * 65536;
  return lane < 5 ? *(const u32*)p : 0u;
}
DI void fetch_wo(const u16* __restrict__ wo, int ct, int q, int part, int lane, lptr smem) {
  const rsrc_t rwo = mk_rsrc(wo, DM * DM * 2);
  const int sr = lane >> 3, sp = lane & 7, sc = (sp ^ (((sr >> 1) & 1) << 2)) << 4;
#pragma unroll
  for (int i = 4 * part; i < 4 * part + 4; ++i) blds16(rwo, smem + P2_WO + q * WOQ + 1024 * i, sr * 1024 + sc, (128 * q + 8 * i) * 1024 + 128 * ct);
}

DI void gemm_role(const u16* __restrict__ x, const u16* __restrict__ wqkv, const u16* __restrict__ wo, int ct, char* __restrict__ ws, const u16* out, int rt, int tt, int hg,
                  u64& epoch, lptr smem STAMP_ARG) {
  const int tid = threadIdx.x, lane = tid & 63;
  const int w = __builtin_amdgcn_readfirstlane(tid >> 6);
  const int wm = w >> 2, wh = w & 3, g = lane >> 4, l15 = lane & 15;
  const int sr = lane >> 3, sp = lane & 7;
  const rsrc_t rx = mk_rsrc(x, SEQ * DM * 2), rw = mk_rsrc(wqkv, DM * 1536 * 2);
  const int xv = sr * 1024 + ((sp ^ sr) << 4), xs0 = (tt * 128 + 16 * w) * 1024;
  const int fw = ((sr >> 1) & 1) | ((w & 1) << 1);
  const int wv = sr * 3072 + ((sp ^ (fw << 1)) << 4), ws0 = 8 * w * 3072 + hg * 128;
  const int ROT = 2 * (tt & 3) + ((tt >> 2) & 1);
  const rsrc_t rwo = mk_rsrc(wo, DM * DM * 2);
  const int ov = sr * 1024 + ((sp ^ (((sr >> 1) & 1) << 2)) << 4);
#define ISS(s, i) do { lptr sb = smem + ((s) & 3) * STAGE; \
    if ((s) >= 8) { if ((i) < 4) blds16(rwo, smem + P2_WO + (w >> 1) * WOQ + (64 * (w & 1) + 32 * ((s) - 8) + 8 * (i)) * 128, ov, (64 * w + 32 * ((s) - 8) + 8 * (i)) * 1024 + 128 * ct); } \
    else if ((i) < 2) blds16(rx, sb + w * 2048 + (i) * 1024, xv, xs0 + (((s) + ROT) & 7) * 128 + (i) * 8192); \
    else blds16(rw, sb + 16384 + ((i) - 2) * 8192 + w * 1024, wv, ws0 + (((s) + ROT) & 7) * 64 * 3072 + ((i) - 2) * 1024); } while (0)
#define ISSUE(s) do { ISS(s, 0); ISS(s, 1); ISS(s, 2); ISS(s, 3); ISS(s, 4); } while (0)
  gu64* sync_z = (gu64*)(ws + WS_SYNC + 128 + hg * 128);
  ISSUE(0);
  STAMP(19);
  SCHED(); RAW_BAR(); SCHED();
  u32 pv2 = 0;
  SCHED();
  ISSUE(1);
  SCHED();
  epoch = load_epoch(ws);
  const rsrc_t rws = mk_rsrc(ws, WS_USED);
  const bool clear = tt == 0 && w == 7;
  if (clear) {
    const u32x4 z4 = {0u, 0u, 0u, 0u};
#pragma unroll
    for (int i = 0; i < 4; ++i) store16(z4, rws, lane * 16 + (int)WS_KV + hg * 4352 + 1024 * i);
    store16(z4, rws, (lane & 15) * 16 + (int)WS_KV + hg * 4352 + 4096);
  }
  SCHED();
  const int xo = (64 * wm + l15) * 128 + ((g ^ (lane & 7)) << 4);
  const int q4 = l15 >> 2, p4 = lane & 3;
  const int wo_ = 16384 + (8 * g + q4) * 128 + ((wh ^ (((q4 >> 1) & 1) | ((g & 1) << 1))) << 5) + 8 * p4;
  f32x4 aq[4], ak[4], av[4];
#pragma unroll
  for (int i = 0; i < 4; ++i) { aq[i] = (f32x4){EPS, EPS, EPS, EPS}; ak[i] = aq[i]; av[i] = (f32x4){0, 0, 0, 0}; }
  s16x8 xa[4], wa[3], xb[4], wb[3];
#define LDWF(wf, s, ks, st) wf[st] = cat8(tr_read(smem + ((s) & 3) * STAGE + wo_ + (st) * 8192 + (ks) * 4096), tr_read(smem + ((s) & 3) * STAGE + wo_ + (st) * 8192 + (ks) * 4096 + 512))
#define LDXF(xf, s, ks, mt) xf[mt] = LD(s16x8, smem + ((s) & 3) * STAGE + ((xo ^ ((ks) * 64)) + (mt) * 2048))
#define LOADF(xf, wf, s, ks) do { LDWF(wf, s, ks, 0); LDWF(wf, s, ks, 1); LDWF(wf, s, ks, 2); \
    LDXF(xf, s, ks, 0); LDXF(xf, s, ks, 1); LDXF(xf, s, ks, 2); LDXF(xf, s, ks, 3); } while (0)
#define MM3(xf, wf, mt) do { aq[mt] = MFMA16(wf[0], xf[mt], aq[mt]); ak[mt] = MFMA16(xf[mt], wf[1], ak[mt]); av[mt] = MFMA16(xf[mt], wf[2], av[mt]); \
    asm volatile("" : "+v"(aq[mt]), "+v"(ak[mt]), "+v"(av[mt])); } while (0)
#define HALF(xf, wf, xn, wn, ls, lk, is) do { SCHED(); MM3(xf, wf, 0); SCHED(); \
    if ((ls) >= 0) { LDWF(wn, ls, lk, 0); LDXF(xn, ls, lk, 0); LDWF(wn, ls, lk, 1); } if ((is) >= 0) ISS(is, 0); SCHED(); MM3(xf, wf, 1); SCHED(); \
    if ((ls) >= 0) { LDWF(wn, ls, lk, 2); LDXF(xn, ls, lk, 1); LDXF(xn, ls, lk, 2); } if ((is) >= 0) ISS(is, 1); SCHED(); MM3(xf, wf, 2); SCHED(); \
    if ((ls) >= 0) LDXF(xn, ls, lk, 3); if ((is) >= 0) { ISS(is, 2); ISS(is, 3); } SCHED(); MM3(xf, wf, 3); SCHED(); \
    if ((is) >= 0) ISS(is, 4); SCHED(); } while (0)
#define STEP(s, vm, i1, i2) do { WAIT_L0(); HALF(xa, wa, xb, wb, s, 1, i1); \
    WAIT_L0(); WAIT_VM(vm); RAW_BAR(); HALF(xb, wb, xa, wa, (s) + 1, 0, i2); } while (0)
  STAMP(1);
  if (clear) WAIT_VM(11); else WAIT_VM(6);
  RAW_BAR();
  STAMP(2);
  LOADF(xa, wa, 0, 0);
  STEP(0, 5, 2, -1);
  if (clear && lane == 0) __hip_atomic_store(sync_z, next_tag(epoch), RLX_AGENT);
  STAMP(3);
  STEP(1, 5, 3, -1);
  STEP(2, 5, 4, 5);
  STEP(3, 5, -1, 6);
  STAMP(4); CYC(20);
  if (w == 0) pv2 = touch_pages(ws, out, rt, lane);
  STEP(4, 5, -1, 7);
  u64 ready = __hip_atomic_load(sync_z, RLX_AGENT);
  STEP(5, 5, -1, 8);
  if (w < 4) __builtin_amdgcn_s_setprio(2);
  STEP(6, 8, 9, -1);
  STAMP(5); CYC(21); CYCD(20, 21); CYC(21);
  {
    const u64 tag = next_tag(epoch);
#pragma unroll 1
    for (int spin = 0; ready != tag && spin < (1 << 22); ++spin) {
      __builtin_amdgcn_s_sleep(2); ready = __hip_atomic_load(sync_z, RLX_AGENT); __builtin_amdgcn_s_waitcnt(0x0F70);
    }
  }
  WAIT_L0(); HALF(xa, wa, xb, wb, 7, 1, -1); WAIT_L0(); SCHED();
#pragma unroll
  for (int mt = 0; mt < 4; ++mt) aq[mt] = MFMA16(wb[0], xb[mt], aq[mt]);
  asm volatile("" : "+v"(aq[0]), "+v"(aq[1]), "+v"(aq[2]), "+v"(aq[3]));
  SCHED();
#pragma unroll
  for (int mt = 0; mt < 4; ++mt) { ak[mt] = MFMA16(xb[mt], wb[1], ak[mt]); av[mt] = MFMA16(xb[mt], wb[2], av[mt]); }
  asm volatile("" : "+v"(ak[0]), "+v"(ak[1]), "+v"(ak[2]), "+v"(ak[3]), "+v"(av[0]), "+v"(av[1]), "+v"(av[2]), "+v"(av[3]));
  SCHED();
  asm volatile("" :: "v"(pv2));
  STAMP(6);
#undef ISS
#undef ISSUE
#undef LOADF
#undef MM3
#undef HALF
#undef LDWF
#undef LDXF
#undef STEP
  const int h = 4 * hg + wh;
  const int pqv = (g >> 1) * 512 + l15 * 16 + (g & 1) * 256, pqs = (int)WS_PQ + ((tt * 4 + 2 * wm) * 32 + h) * 1024;
#pragma unroll
  for (int pr = 0; pr < 2; ++pr) {
    const f32x4 q0 = aq[2 * pr], q1 = aq[2 * pr + 1];
    const auto s0 = __builtin_amdgcn_permlane16_swap(pk_bf16(phi_f(q0[0]), phi_f(q0[1])), pk_bf16(phi_f(q1[0]), phi_f(q1[1])), false, false);
    const auto s1 = __builtin_amdgcn_permlane16_swap(pk_bf16(phi_f(q0[2]), phi_f(q0[3])), pk_bf16(phi_f(q1[2]), phi_f(q1[3])), false, false);
    store16((u32x4){s0[0], s1[0], s0[1], s1[1]}, rws, pqv + pqs + pr * 32768);
  }
  SCHED();
  STAMP(7);
  f32x4 kvt = {0, 0, 0, 0}, zz = {0, 0, 0, 0};
  const u32x4 ones_u = {0x3f803f80u, 0x3f803f80u, 0x3f803f80u, 0x3f803f80u};
#pragma unroll
  for (int pr = 0; pr < 2; ++pr) {
    u32x4 pk, vv;
#pragma unroll
    for (int m = 0; m < 2; ++m) {
      const f32x4 kk = ak[2 * pr + m], v4 = av[2 * pr + m];
      pk[2 * m] = pk_bf16(phi_f(kk[0]), phi_f(kk[1])); pk[2 * m + 1] = pk_bf16(phi_f(kk[2]), phi_f(kk[3]));
      vv[2 * m] = pk_bf16(v4[0], v4[1]); vv[2 * m + 1] = pk_bf16(v4[2], v4[3]);
    }
    kvt = MFMA16(__builtin_bit_cast(s16x8, vv), __builtin_bit_cast(s16x8, pk), kvt);
    zz = MFMA16(__builtin_bit_cast(s16x8, ones_u), __builtin_bit_cast(s16x8, pk), zz);
  }
  lptr red = smem + 4 * WOQ;
#pragma unroll
  for (int r = 0; r < 4; ++r) ST(float, red + (wm * 4 + wh) * 1088 + l15 * 4 + (4 * g + r) * 64) = kvt[r];
  if (g == 0) ST(float, red + (wm * 4 + wh) * 1088 + l15 * 4 + 1024) = zz[0];
  LBAR();
  STAMP(8);
  float* kvacc = (float*)(ws + WS_KV + (tt % KVC) * KV_CP) + hg * 1088 + tid;
  atomicAdd(kvacc, LD(float, red + tid * 4) + LD(float, red + 4352 + tid * 4));
  atomicAdd(kvacc + 512, LD(float, red + 2048 + tid * 4) + LD(float, red + 4352 + 2048 + tid * 4));
  if (tid < 64) atomicAdd(kvacc + 1024, LD(float, red + 4096 + tid * 4) + LD(float, red + 4352 + 4096 + tid * 4));
  STAMP(9); CYC(22); CYCD(21, 22);
}

static constexpr int TB_ROW = 544, TB_BUF = 16 * TB_ROW, TB_CH = 2 * TB_BUF;
static constexpr int KR_OFF = 4 * TB_CH, KR_CP = 4160, KR_CH = 2 * KR_CP;
static constexpr int U_OFF = KR_OFF + 4 * KR_CH, U_CH = (2048 + 240) * 2;
static constexpr int EX_OFF = U_OFF + 4 * U_CH;
static_assert(EX_OFF + 32768 <= 163840 && TB_CH == WOQ && P2_EX + 65536 <= 163840, "LDS map");

DI void conv_role(const u16* __restrict__ x, u32 (&pr)[5], u32x2 (&xr)[8], u32 pf, const u16* __restrict__ dsk, const u16* __restrict__ gw,
                  const u16* __restrict__ wo, char* __restrict__ ws, const u16* out, int rt, int cg, int ct, u64& epoch, lptr smem STAMP_ARG) {
  const int tid = threadIdx.x, lane = tid & 63;
  const int w = __builtin_amdgcn_readfirstlane(tid >> 6);
  const int ch = w & 3, half = w >> 2, g = lane >> 4, l15 = lane & 15;
  const int c = 4 * cg + ch;
  const rsrc_t rx = mk_rsrc(x, SEQ * DM * 2);
  u32 dsk2, pv = 0;
  u32x4 gx[4], gwv;
  const rsrc_t rws = mk_rsrc(ws, WS_USED);
  f32x4 acc[8];
#pragma unroll
  for (int i = 0; i < 8; ++i) acc[i] = (f32x4){0, 0, 0, 0};
#define PINC() do { asm volatile("" : "+v"(acc[0]), "+v"(acc[1]), "+v"(acc[2]), "+v"(acc[3]), "+v"(acc[4]), "+v"(acc[5]), "+v"(acc[6]), "+v"(acc[7])); SCHED(); } while (0)
  if (w < 4) {
    const int cw = 4 * cg + w;
    if (lane < 8) ST(u32x4, smem + KR_OFF + w * KR_CH + (lane >> 2) * KR_CP + 4096 + 16 * (lane & 3)) = (u32x4){0u, 0u, 0u, 0u};
    __builtin_amdgcn_s_setprio(2);
    asm volatile("s_waitcnt vmcnt(0)" : "+v"(pr[0]), "+v"(pr[1]), "+v"(pr[2]), "+v"(pr[3]), "+v"(pr[4]));
    const u32 lar2 = pr[0], aim2 = pr[1], cre2 = pr[2], cim2 = pr[3], ldt_u = pr[4];
    const float dt = rbf(exp_f(bf_lo(ldt_u)));
    float al[2], th[2];
    al[0] = -rbf(exp_f(bf_lo(lar2))) * dt * LOG2E; al[1] = -rbf(exp_f(bf_hi(lar2))) * dt * LOG2E;
    th[0] = bf_lo(aim2) * dt * 0.15915494309189535f; th[1] = bf_hi(aim2) * dt * 0.15915494309189535f;
    STAMP(1);
    u32 ce = wave_umax(max(max(cre2 & 0x7f80u, (cre2 >> 16) & 0x7f80u), max(cim2 & 0x7f80u, (cim2 >> 16) & 0x7f80u))) >> 7;
    ce = ce < 1 ? 1 : (ce > 253 ? 253 : ce);
    const float cinv = __builtin_bit_cast(float, (254u - ce) << 23);
    const float kscale = 2.f * dt * __builtin_bit_cast(float, ce << 23);
    lptr tb = smem + w * TB_CH + lane * 8, tr = smem + w * TB_CH + 16 * g, kr = smem + KR_OFF + w * KR_CH;
#define PUT(buf, e, v) ST(u32x2, tb + (buf) * TB_BUF + (e) * TB_ROW) = (u32x2){pk_f16(v[0]), pk_f16(v[1])}
#define CONJ(v) ((f32x2){(v).x, -(v).y})
#define GETF(f, buf, row) do { lptr p_ = tr + (buf) * TB_BUF + (row) * TB_ROW; \
      _Pragma("unroll") for (int ks = 0; ks < 8; ++ks) f[ks] = LD(h16x8, p_ + 64 * ks); } while (0)
#define TILE(d, f) do { d = (f32x4){0, 0, 0, 0}; _Pragma("unroll") for (int ks = 0; ks < 8; ++ks) d = MFMA16H(f[ks], fx[ks], d); } while (0)
    f32x2 y[17][2], wB[2], w16[2], w32[2], xa[2], xb[2], aS[6][2], t[2], t2[2];
#pragma unroll
    for (int m = 0; m < 2; ++m) {
      y[1][m] = CONJ(cexp_p(al[m], th[m], 1.f)); w16[m] = cexp_p(al[m], th[m], 16.f); wB[m] = CONJ(cexp_p(al[m], th[m], 256.f));
      y[16][m] = CONJ(w16[m]);
    }
    xa[0] = (f32x2){bf_lo(cre2) * cinv, bf_lo(cim2) * cinv}; xa[1] = (f32x2){bf_hi(cre2) * cinv, bf_hi(cim2) * cinv};
    ST(u32x2, tb + TB_BUF) = (u32x2){0x3c00u, 0x3c00u};
    PUT(0, 0, xa); PUT(1, 1, y[1]);
    cmul4(xb, xa, w16, w32, w16, w16); cmul4(y[2], y[1], y[1], aS[0], wB, wB);
    PUT(0, 1, xb); PUT(1, 2, y[2]);
#pragma unroll
    for (int i = 1; i < 8; ++i) {
      cmul4(xa, xa, w32, xb, xb, w32);
      if (i < 7) cmul4(y[2 * i + 1], y[2 * i - 1], y[2], y[2 * i + 2], y[2 * i], y[2]); else cmul2(y[15], y[13], y[2]);
      PUT(0, 2 * i, xa); PUT(0, 2 * i + 1, xb); PUT(1, 2 * i + 1, y[2 * i + 1]);
      if (i < 7) PUT(1, 2 * i + 2, y[2 * i + 2]);
    }
    STAMP(2);
    h16x8 fx[8], fa[8], fb[8];
    f32x4 kd;
    const int gq = l15 >> 2, r3 = min(l15 & 3, 2), yk = 2044 - 16 * l15 - 4 * g;
    SCHED();
    GETF(fx, 0, l15); GETF(fa, 1, l15);
    SCHED();
    PUT(0, 0, wB);
#pragma unroll
    for (int m = 1; m < 9; m += 2) {
      cmul4(t, wB, y[2 * m], t2, wB, y[2 * m + 2]);
      if (m == 1) cmul4(aS[1], aS[0], wB, aS[2], aS[0], aS[0]);
      if (m == 3) cmul4(aS[3], aS[2], wB, aS[4], aS[2], aS[0]);
      if (m == 5) cmul2(aS[5], aS[4], wB);
      PUT(0, m, t); PUT(0, m + 1, t2);
    }
    SCHED();
    GETF(fb, 0, (int)((0x8766654443222100ull >> (4 * l15)) & 15));
    TILE(kd, fa);
    SCHED();
#pragma unroll
    for (int s = 0; s < 2; ++s) {
      PUT(1, 5 * s, aS[s]);
#pragma unroll
      for (int m = 1; m < 5; m += 2) { cmul4(t, aS[s], y[4 * m], t2, aS[s], y[4 * m + 4]); PUT(1, 5 * s + m, t); PUT(1, 5 * s + m + 1, t2); }
    }
    {
      const f32x4 k0 = kd * kscale;
      const u32 p32 = pk_bf16(k0[3], k0[2]), p10 = pk_bf16(k0[1], k0[0]);
      ST(u32x2, kr + 2 * yk) = (u32x2){p32, p10};
      ST(u16, kr + KR_CP + 2 * yk + 2) = (u16)p32;
      ST(u32, kr + KR_CP + 2 * yk + 4) = pk_bf16(k0[2], k0[1]);
      ST(u16, kr + KR_CP + 2 * yk + 8) = (u16)(p10 >> 16);
    }
    SCHED();
    GETF(fa, 1, 5 * (gq >> 1) + 2 * (gq & 1) + r3);
    TILE(kd, fb);
    SCHED();
#pragma unroll
    for (int s = 2; s < 6; ++s) {
      cmul4(t, aS[s], y[8], t2, aS[s], y[16]);
      PUT(0, 3 * (s - 2), aS[s]); PUT(0, 3 * (s - 2) + 1, t); PUT(0, 3 * (s - 2) + 2, t2);
    }
    {
      const f32x4 k1 = kd * kscale;
      const u32 q10 = pk_bf16(k1[1], k1[0]);
      ST(u32x2, kr + 2 * (yk - 256)) = (u32x2){pk_bf16(k1[3], k1[2]), q10};
      ST(u32x2, kr + KR_CP + 2 * (yk - 256)) = (u32x2){pk_bf16(k1[3], k1[3]), pk_bf16(k1[2], k1[1])};
      if (lane == 0) ST(u16, kr + KR_CP + 2 * 1792) = (u16)q10;
    }
    SCHED();
    GETF(fb, 0, 3 * gq + r3);
    TILE(kd, fa);
    STAMP(3);
    {
      const f32x4 k0 = kd * kscale;
      const int y2 = 2040 - (512 + 256 * (g >> 1) + 16 * l15 + 8 * (g & 1));
      const u32 b2 = pk_bf16(k0[2], k0[2]), b1 = pk_bf16(k0[1], k0[1]), b0 = pk_bf16(k0[0], k0[0]);
      ST(u32x4, kr + 2 * y2) = (u32x4){b2, b1, pk_bf16(k0[1], k0[0]), b0};
      ST(u32x4, kr + KR_CP + 2 * y2) = (u32x4){b2, pk_bf16(k0[2], k0[1]), b1, b0};
    }
    TILE(kd, fb);
    {
      const f32x4 k1 = kd * kscale;
      const int y3 = 2032 - (1024 + 256 * g + 16 * l15);
      const u32 a2 = pk_bf16(k1[2], k1[2]), a1 = pk_bf16(k1[1], k1[1]), a0 = pk_bf16(k1[0], k1[0]);
      ST(u32x4, kr + 2 * y3) = (u32x4){a2, a2, a1, a1};
      ST(u32x4, kr + 2 * y3 + 16) = (u32x4){a1, pk_bf16(k1[1], k1[0]), a0, a0};
      ST(u32x4, kr + KR_CP + 2 * y3) = (u32x4){a2, a2, pk_bf16(k1[2], k1[1]), a1};
      ST(u32x4, kr + KR_CP + 2 * y3 + 16) = (u32x4){a1, a1, a0, a0};
    }
#undef PUT
#undef GETF
#undef TILE
    SCHED();
    __builtin_amdgcn_s_setprio(0);
    STAMP(4);
  } else {
    const int t2 = (tid + 16 * cg) & 255;
    if (lane < 30) ST(u32x4, smem + U_OFF + ch * U_CH + lane * 16) = (u32x4){0u, 0u, 0u, 0u};
    STAMP(1);
#pragma unroll
    for (int hf = 0; hf < 2; ++hf) {
      lptr ub = smem + U_OFF + 480 + t2 * 8 + hf * 2048;
      if (hf == 0) asm volatile("s_waitcnt vmcnt(4)" : "+v"(xr[0]), "+v"(xr[1]), "+v"(xr[2]), "+v"(xr[3]) : "v"(pf));
      else asm volatile("s_waitcnt vmcnt(0)" : "+v"(xr[4]), "+v"(xr[5]), "+v"(xr[6]), "+v"(xr[7]));
      const u32x2 x0 = xr[4 * hf], x1 = xr[4 * hf + 1], x2 = xr[4 * hf + 2], x3 = xr[4 * hf + 3];
      ST(u32x2, ub + 0 * U_CH) = (u32x2){__builtin_amdgcn_perm(x1.x, x0.x, 0x05040100u), __builtin_amdgcn_perm(x3.x, x2.x, 0x05040100u)};
      ST(u32x2, ub + 1 * U_CH) = (u32x2){__builtin_amdgcn_perm(x1.x, x0.x, 0x07060302u), __builtin_amdgcn_perm(x3.x, x2.x, 0x07060302u)};
      ST(u32x2, ub + 2 * U_CH) = (u32x2){__builtin_amdgcn_perm(x1.y, x0.y, 0x05040100u), __builtin_amdgcn_perm(x3.y, x2.y, 0x05040100u)};
      ST(u32x2, ub + 3 * U_CH) = (u32x2){__builtin_amdgcn_perm(x1.y, x0.y, 0x07060302u), __builtin_amdgcn_perm(x3.y, x2.y, 0x07060302u)};
      SCHED();
    }
    STAMP(4);
  }
  lptr ka[8];
  {
    const int yb = 2031 - l15 + 8 * g - 128 * half - 96 - 1792;
    lptr k7 = smem + KR_OFF + ch * KR_CH + (yb & 1) * KR_CP + ((yb + 1) >> 1) * 4;
#pragma unroll
    for (int n = 0; n < 8; ++n) { ka[n] = k7 + 512 * (7 - n); asm volatile("" : "+v"(ka[n])); }
  }
  lptr ua = smem + U_OFF + ch * U_CH + (8 * g + 16 * l15 + 224 - 128 * half - 96) * 2;
  asm volatile("" : "+v"(ua));
  STAMP(6);
  LBAR();
  STAMP(7); CYC(20);
  {
    u32x4 A0[8], A1[8]; s16x8 B0[8], B1[8];
#define LOADC(A, B, rr) do { _Pragma("unroll") for (int n = 0; n < 8; ++n) { \
      lptr kp = ka[n] + 64 * (3 - (rr)); \
      A[n] = (u32x4){LD(u32, kp), LD(u32, kp + 4), LD(u32, kp + 8), LD(u32, kp + 12)}; \
      B[n] = LD(s16x8, ua + 64 * (3 - (rr)) + 512 * n); } } while (0)
#define MMC(A, B) do { _Pragma("unroll") for (int nu = 0; nu < 8; ++nu) _Pragma("unroll") for (int mu = 0; mu < 8 - nu; ++mu) \
      acc[nu + mu] = MFMA16(__builtin_bit_cast(s16x8, A[nu]), B[mu], acc[nu + mu]); } while (0)
#define MIXC() do { _Pragma("unroll") for (int q = 0; q < 12; ++q) { __builtin_amdgcn_sched_group_barrier(0x8, MIXM, 0); __builtin_amdgcn_sched_group_barrier(0x100, 2, 0); } \
      __builtin_amdgcn_sched_group_barrier(0x8, 36 - 12 * MIXM, 0); } while (0)
    LOADC(A0, B0, 0);
    SCHED();
    dsk2 = *(const u32*)(dsk + (c & ~1));
    epoch = load_epoch(ws);
    if (!half) {
#pragma unroll
      for (int r = 0; r < 4; ++r) gx[r] = __builtin_amdgcn_raw_buffer_load_b128(rx, lane * 16 + 1024 * r, (16 * cg + 4 * ch) * 1024, 0);
      gwv = *(const u32x4*)(gw + 8 * lane);
      pv = touch_pages(ws, out, rt, lane);
    }
    SCHED();
#define WOP(j) do { if (!half) fetch_wo(wo, ct, ch, j, lane, smem); SCHED(); } while (0)
    WOP(0); WAIT_L0(); SCHED(); LOADC(A1, B1, 1); MMC(A0, B0); MIXC(); PINC();
    WOP(1); WAIT_L0(); SCHED(); LOADC(A0, B0, 2); MMC(A1, B1); MIXC(); PINC();
    WOP(2); WAIT_L0(); SCHED(); LOADC(A1, B1, 3); MMC(A0, B0); MIXC(); PINC();
    WOP(3); WAIT_L0(); SCHED(); MMC(A1, B1); PINC();
#undef WOP
#undef LOADC
#undef MMC
#undef MIXC
#undef PINC
  }
  STAMP(8); CYC(21); CYCD(20, 21);
  {
    lptr ex = smem + EX_OFF + w * 4096 + lane * 16, ei = smem + EX_OFF + (w ^ 4) * 4096 + lane * 16;
    f32x4 keep[4];
    if (half) {
#pragma unroll
      for (int i = 0; i < 4; ++i) { ST(f32x4, ex + 1024 * i) = acc[4 + i]; keep[i] = acc[i]; }
    } else {
#pragma unroll
      for (int i = 0; i < 4; ++i) { ST(f32x4, ex + 1024 * i) = acc[i]; keep[i] = acc[4 + i]; }
    }
    lptr up = smem + U_OFF + ch * U_CH + 480 + 2 * (16 * l15 + 4 * g) + 2048 * (1 - half);
    const float dsk_f = (c & 1) ? bf_hi(dsk2) : bf_lo(dsk2);
#pragma unroll
    for (int i = 0; i < 4; ++i) {
      const u32x2 uu = LD(u32x2, up + 512 * i);
      keep[i] += (f32x4){bf_lo(uu.x), bf_hi(uu.x), bf_lo(uu.y), bf_hi(uu.y)} * dsk_f;
    }
    SCHED();
    if (!half) {
      asm volatile("" :: "v"(pv));
      float sg[4];
#pragma unroll
      for (int r = 0; r < 4; ++r) {
        float s0 = 0.f;
#pragma unroll
        for (int i = 0; i < 4; ++i) s0 = __builtin_amdgcn_fdot2_f32_bf16(u2b(gx[r][i]), u2b(gwv[i]), s0, false);
        sg[r] = rbf(__builtin_amdgcn_rcpf(1.f + __builtin_amdgcn_exp2f(-LOG2E * rbf(wave_sum(s0)))));
      }
      if (lane == 63) store16(__builtin_bit_cast(u32x4, (f32x4){sg[0], sg[1], sg[2], sg[3]}), rws, (int)WS_G + (16 * cg + 4 * ch) * 4 + (lane - 63));
    }
    const int bo = (int)WS_BT + c * 4096 + 2048 * (1 - half) + 32 * l15 + 16 * (g >> 1) + 512 * (g & 1);
    SCHED();
    LBAR();
    f32x4 o4[4];
#pragma unroll
    for (int i = 0; i < 4; ++i) o4[i] = LD(f32x4, ei + 1024 * i);
    SCHED();
#pragma unroll
    for (int i = 0; i < 2; ++i) {
      const f32x4 y0 = keep[2 * i] + o4[2 * i], y1 = keep[2 * i + 1] + o4[2 * i + 1];
      const auto sx = __builtin_amdgcn_permlane16_swap(pk_bf16(y0[0], y0[1]), pk_bf16(y1[0], y1[1]), false, false);
      const auto sy = __builtin_amdgcn_permlane16_swap(pk_bf16(y0[2], y0[3]), pk_bf16(y1[2], y1[3]), false, false);
      store16((u32x4){sx[0], sy[0], sx[1], sy[1]}, rws, bo + 1024 * i);
    }
    asm volatile(".Lfused_code_tail:");
  }
  STAMP(9);
}

struct Tags { u64 a, b; };
DI Tags poll_load(char* ws, int off, int lane) {
  gu64* p = (gu64*)(ws + WS_FLAG + off) + lane;
  return Tags{__hip_atomic_load(p, RLX_AGENT), __hip_atomic_load(p + 64, RLX_AGENT)};
}
DI void poll_wait(char* ws, int off, int lane, u64 tag, Tags& f, u64 ma, u64 mb) {
#pragma unroll 1
  for (int spin = 0; spin < (1 << 20); ++spin) {
    if ((__ballot(f.a != tag) & ma) == 0 && (__ballot(f.b != tag) & mb) == 0) break;
    __builtin_amdgcn_s_sleep(POLL_SLEEP);
    f = poll_load(ws, off, lane);
    __builtin_amdgcn_s_waitcnt(0x0F70);
  }
}

DI void out_role(char* __restrict__ ws, u16* __restrict__ out, int rt, int ct, u64 tag, Tags fl, lptr smem STAMP_ARG) {
  const int tid = threadIdx.x, lane = tid & 63;
  const int w = __builtin_amdgcn_readfirstlane(tid >> 6);
  const int th = w & 1, kq = w >> 1, hh = lane >> 5, l31 = lane & 31;
  const rsrc_t rws = mk_rsrc(ws, WS_USED);
  const int foff = (blockIdx.x & 31) * 2048;
  if (w == 0) poll_wait(ws, foff, lane, tag, fl, ~0ull, ~0ull);
  RAW_BAR();
  STAMP(11);
  if (w == 1) fl = poll_load(ws, foff + 1024, lane);
  f32x4 kvr[KVC][5];
#pragma unroll
  for (int cp = 0; cp < KVC; ++cp) {
#pragma unroll
    for (int i = 0; i < 4; ++i) kvr[cp][i] = __builtin_bit_cast(f32x4, __builtin_amdgcn_raw_buffer_load_b128(rws, tid * 16, (int)WS_KV + cp * KV_CP + 8192 * i, 16));
    kvr[cp][4] = __builtin_bit_cast(f32x4, __builtin_amdgcn_raw_buffer_load_b128(rws, (tid & 127) * 16, (int)WS_KV + cp * KV_CP + 32768, 16));
  }
  SCHED(); RAW_BAR(); SCHED();
  u32x4 pf[8];
#define LOAD_PQ(i0, i1) do { _Pragma("unroll") for (int i = i0; i < i1; ++i) \
    pf[i] = __builtin_amdgcn_raw_buffer_load_b128(rws, lane * 16, (int)WS_PQ + ((2 * rt + th) * 32 + 8 * kq + i) * 1024, 16); } while (0)
  LOAD_PQ(0, 3);
  STAMP(12);
#pragma unroll
  for (int i = 0; i < 5; ++i) {
    f32x4 v = kvr[0][i];
#pragma unroll
    for (int cp = 1; cp < KVC; ++cp) v += kvr[cp][i];
    ST(u32x2, smem + P2_KV + (i < 4 ? tid + 512 * i : 2048 + (tid & 127)) * 8) = (u32x2){pk_bf16(v[0], v[1]), pk_bf16(v[2], v[3])};
  }
  f32x16 acc[2];
#pragma unroll
  for (int i = 0; i < 16; ++i) { acc[0][i] = 0.f; acc[1][i] = 0.f; }
  f32x16 cinit = acc[0];
  cinit[8] = EPS;
  lptr kva = smem + P2_KV + 8 * kq * 544 + min(l31, 16) * 32 + hh * 16;
  const int blk = (lane >> 4) & 1, q4 = (lane & 15) >> 2, p4 = lane & 3;
  const int sw = ((q4 >> 1) & 1) << 6;
  lptr wa0 = smem + P2_WO + kq * WOQ + (4 * hh + q4) * 128 + blk * 32 + p4 * 8 + sw;
  lptr wa1 = smem + P2_WO + kq * WOQ + (4 * hh + q4) * 128 + blk * 32 + p4 * 8 + (64 ^ sw);
  STAMP(13);
  const u64 mct = 0xffffull << (16 * ct & 63), mrt = 0xfull << (4 * rt & 63);
  const u64 ma = (ct < 4 ? mct : 0ull) | (rt < 16 ? mrt : 0ull), mb = (ct < 4 ? 0ull : mct) | (rt < 16 ? 0ull : mrt);
  if (w == 1) { const u32 ok = (__ballot(fl.a != tag) & ma) == 0 && (__ballot(fl.b != tag) & mb) == 0; if (lane == 0) ST(u32, smem + P2_OK) = ok; }
  LBAR();
  LOAD_PQ(3, 8);
#undef LOAD_PQ
  const bool early = __builtin_amdgcn_readfirstlane(LD(u32, smem + P2_OK)) != 0;
  u32x4 btr; float gt;
#define LOAD_B() do { \
    btr = __builtin_amdgcn_raw_buffer_load_b128(rws, (lane >> 2) * 4096 + (lane & 3) * 16, (int)WS_BT + (64 * ct + 16 * kq) * 4096 + 128 * rt + 64 * th, 16); \
    gt = u2f(__builtin_amdgcn_raw_buffer_load_b32(rws, l31 * 4, (int)WS_G + (64 * rt + 32 * th) * 4, 16)); } while (0)
  if (early) LOAD_B();
  u32x2 bb[2];
  lptr ba = smem + P2_BT + w * 1024 + (4 * hh + q4) * 64 + blk * 32 + p4 * 8;
#define TURN_B() do { ST(u32x4, smem + P2_BT + w * 1024 + lane * 16) = btr;        \
    bb[0] = __builtin_bit_cast(u32x2, tr_read(ba)); bb[1] = __builtin_bit_cast(u32x2, tr_read(ba + 512)); } while (0)
  STAMP(14);
  {
    f32x16 d[2]; s16x8 kf[8], wf[2][2]; u32x4 of[2];
#define OMM(i) do { d[(i) & 1] = MFMA32(kf[i], __builtin_bit_cast(s16x8, pf[i]), cinit); asm volatile("" : "+v"(d[(i) & 1])); } while (0)
#define NORM(i) do { const f32x16& dd = d[(i) & 1]; const float inv = __builtin_amdgcn_rcpf(dd[8]); \
      of[(i) & 1] = (u32x4){pk_bf16(dd[0] * inv, dd[1] * inv), pk_bf16(dd[2] * inv, dd[3] * inv), pk_bf16(dd[4] * inv, dd[5] * inv), pk_bf16(dd[6] * inv, dd[7] * inv)}; \
      asm volatile("" : "+v"(of[(i) & 1]) : "v"(dd)); } while (0)
#define LDW(i) do { wf[(i) & 1][0] = cat8(tr_read(wa0 + (i) * 2048), tr_read(wa0 + (i) * 2048 + 1024)); \
      wf[(i) & 1][1] = cat8(tr_read(wa1 + (i) * 2048), tr_read(wa1 + (i) * 2048 + 1024)); } while (0)
#define AMM(i) do { acc[0] = MFMA32(wf[(i) & 1][0], __builtin_bit_cast(s16x8, of[(i) & 1]), acc[0]); \
      acc[1] = MFMA32(wf[(i) & 1][1], __builtin_bit_cast(s16x8, of[(i) & 1]), acc[1]); asm volatile("" : "+v"(acc[0]), "+v"(acc[1])); } while (0)
#pragma unroll
    for (int i = 0; i < 8; ++i) kf[i] = LD(s16x8, kva + i * 544);
    LDW(0);
    SCHED();
    OMM(0); OMM(1); SCHED(); NORM(0); SCHED();
#pragma unroll
    for (int i = 0; i < 8; ++i) {
      if (i + 1 < 8) LDW(i + 1);
      SCHED();
      AMM(i);
      if (i + 2 < 8) OMM(i + 2);
      SCHED();
      if (i + 1 < 8) NORM(i + 1);
      SCHED();
      if (i == TURN_AT && early) { TURN_B(); SCHED(); }
    }
#undef OMM
#undef NORM
#undef LDW
#undef AMM
  }
  STAMP(15);
  f32x4 own[2];
#pragma unroll
  for (int q = 0; q < 4; ++q) {
    lptr ex = smem + P2_EX + ((2 * q + th) * 4 + kq) * 2048 + lane * 16;
    const f32x16& s = acc[q >> 1];
    const f32x4 s0 = {s[8 * (q & 1)], s[8 * (q & 1) + 1], s[8 * (q & 1) + 2], s[8 * (q & 1) + 3]};
    const f32x4 s1 = {s[8 * (q & 1) + 4], s[8 * (q & 1) + 5], s[8 * (q & 1) + 6], s[8 * (q & 1) + 7]};
    if (q == kq) { own[0] = s0; own[1] = s1; } else { ST(f32x4, ex) = s0; ST(f32x4, ex + 1024) = s1; }
  }
  if (!early && w == 1) poll_wait(ws, foff + 1024, lane, tag, fl, ma, mb);
  LBAR();
  if (!early) { LOAD_B(); TURN_B(); }
#undef TURN_B
#undef LOAD_B
  STAMP(16);
  lptr ei = smem + P2_EX + w * 8192 + lane * 16;
  const float g1 = rbf(1.f - gt);
  u32x2 res[2];
#pragma unroll
  for (int i = 0; i < 2; ++i) {
    const f32x4 o = (own[i] + LD(f32x4, ei + ((kq ^ 1) * 2048 + 1024 * i))) + (LD(f32x4, ei + ((kq ^ 2) * 2048 + 1024 * i)) + LD(f32x4, ei + ((kq ^ 3) * 2048 + 1024 * i)));
    res[i] = (u32x2){pk_bf16(gt * o[0] + g1 * bf_lo(bb[i].x), gt * o[1] + g1 * bf_hi(bb[i].x)), pk_bf16(gt * o[2] + g1 * bf_lo(bb[i].y), gt * o[3] + g1 * bf_hi(bb[i].y))};
  }
  const auto sx = __builtin_amdgcn_permlane32_swap(res[0].x, res[1].x, false, false);
  const auto sy = __builtin_amdgcn_permlane32_swap(res[0].y, res[1].y, false, false);
  *(u32x4*)(out + (size_t)(64 * rt + 32 * th + l31) * DM + 64 * ct + 16 * kq + 8 * hh) = (u32x4){sx[0], sy[0], sx[1], sy[1]};
  STAMP(17);
  if (blockIdx.x == 0 && w == 1) {
    poll_wait(ws, foff + 1024, lane, tag, fl, ~0ull, ~0ull);
    if (lane == 0) __hip_atomic_store((gu64*)(ws + WS_SYNC), tag, RLX_AGENT);
  }
}

__global__ __launch_bounds__(512, 2) void fused(const u16* __restrict__ x, const u16* __restrict__ wqkv, const u16* __restrict__ wo,
                                                const u16* __restrict__ lar, const u16* __restrict__ aim, const u16* __restrict__ cre,
                                                const u16* __restrict__ cim, const u16* __restrict__ ldt, const u16* __restrict__ dsk,
                                                const u16* __restrict__ gw, char* __restrict__ ws, u16* __restrict__ out) {
  extern __shared__ __attribute__((aligned(16))) char smem_[];
  lptr smem = (lptr)smem_;
  STAMP_DECL; STAMP(0);
  asm volatile("" :: "s"(x), "s"(wqkv), "s"(wo), "s"(lar), "s"(aim), "s"(cre), "s"(cim), "s"(ldt), "s"(dsk), "s"(gw), "s"(ws), "s"(out));
  STAMP(18);
  const int b = blockIdx.x, xcd = b & 7, slot = b >> 3, idx = slot >> 1;
  const int rt = 8 * (xcd >> 1) + (slot & 7), ct = 4 * (xcd & 1) + (slot >> 3);
  const int tt = 4 * (xcd >> 1) + (idx & 3), hg = 4 * (xcd & 1) + (idx >> 2);
  const int cg = xcd * 16 + idx;
  int pofs = (threadIdx.x & 63) * 8;
  asm volatile("" : "+v"(pofs));
  u64 epoch; u32 tmp0, tmp1, tmp2; u64 pc;
  u32 pr[5]; u32x2 xr[8];
  asm volatile(
      "s_bitcmp1_b32 %[b], 3\n\t"
      "s_cbranch_scc0 .Lpre_end%=\n\t"
      "s_cmp_gt_u32 %[wv], 3\n\t"
      "s_cbranch_scc1 .Lpre_b%=\n\t"
      "v_lshlrev_b32 %[p4], 2, %[tid]\n\t"
      "v_lshl_add_u32 %[p4], %[cg], 10, %[p4]\n\t"
      "global_load_dword %[p0], %[p4], %[lar]\n\t"
      "global_load_dword %[p1], %[p4], %[aim]\n\t"
      "global_load_dword %[p2], %[p4], %[cre]\n\t"
      "global_load_dword %[p3], %[p4], %[cim]\n\t"
      "v_lshrrev_b32 %[t0], 7, %[p4]\n\t"
      "v_and_b32 %[t0], 0x3fe, %[t0]\n\t"
      "global_load_ushort %[p4], %[t0], %[ldt]\n\t"
      "s_barrier\n\t"
      "s_branch .Lpre_end%=\n"
      ".Lpre_b%=:\n\t"
      "s_getpc_b64 %[pc]\n\t"
      "v_and_b32 %[t0], 0xff, %[tid]\n\t"
      "v_lshlrev_b32 %[t0], 7, %[t0]\n\t"
      "v_mov_b32 %[t1], .Lfused_code_tail-.Lpre_b%=-4\n\t"
      "v_med3_i32 %[t0], 0, %[t0], %[t1]\n\t"
      "global_load_dword %[t2], %[t0], %[pc]\n\t"
      "v_lshl_add_u32 %[t0], %[cg], 4, %[tid]\n\t"
      "v_and_b32 %[t0], 0xff, %[t0]\n\t"
      "v_lshlrev_b32 %[t0], 12, %[t0]\n\t"
      "v_lshl_add_u32 %[t0], %[cg], 3, %[t0]\n\t"
      "v_add_u32 %[t1], 0x100000, %[t0]\n\t"
      "s_barrier\n\t"
      "global_load_dwordx2 %[x0], %[t0], %[x]\n\t"
      "global_load_dwordx2 %[x1], %[t0], %[x] offset:1024\n\t"
      "global_load_dwordx2 %[x2], %[t0], %[x] offset:2048\n\t"
      "global_load_dwordx2 %[x3], %[t0], %[x] offset:3072\n\t"
      "global_load_dwordx2 %[x4], %[t1], %[x]\n\t"
      "global_load_dwordx2 %[x5], %[t1], %[x] offset:1024\n\t"
      "global_load_dwordx2 %[x6], %[t1], %[x] offset:2048\n\t"
      "global_load_dwordx2 %[x7], %[t1], %[x] offset:3072\n"
      ".Lpre_end%=:"
      : [p0] "=&v"(pr[0]), [p1] "=&v"(pr[1]), [p2] "=&v"(pr[2]), [p3] "=&v"(pr[3]), [p4] "=&v"(pr[4]),
        [x0] "=&v"(xr[0]), [x1] "=&v"(xr[1]), [x2] "=&v"(xr[2]), [x3] "=&v"(xr[3]), [x4] "=&v"(xr[4]), [x5] "=&v"(xr[5]), [x6] "=&v"(xr[6]), [x7] "=&v"(xr[7]),
        [t0] "=&v"(tmp0), [t1] "=&v"(tmp1), [t2] "=&v"(tmp2), [pc] "=&s"(pc)
      : [b] "s"(b), [wv] "s"(__builtin_amdgcn_readfirstlane(threadIdx.x >> 6)), [tid] "v"(threadIdx.x), [cg] "s"(cg),
        [lar] "s"(lar), [aim] "s"(aim), [cre] "s"(cre), [cim] "s"(cim), [ldt] "s"(ldt), [x] "s"(x)
      : "scc", "memory");
  if (__builtin_expect(slot & 1, 0)) conv_role(x, pr, xr, tmp2, dsk, gw, wo, ws, out, rt, cg, ct, epoch, smem STAMP_PASS);
  else gemm_role(x, wqkv, wo, ct, ws, out, rt, tt, hg, epoch, smem STAMP_PASS);
  const int lane = threadIdx.x & 63, w = __builtin_amdgcn_readfirstlane(threadIdx.x >> 6);
  const u64 tag = next_tag(epoch);
  Tags fl = {0, 0};
  if (w == 0) {
    const rsrc_t rf = mk_rsrc(ws, WS_USED);
    fl.a = __builtin_bit_cast(u64, __builtin_amdgcn_raw_buffer_load_b64(rf, pofs, (int)WS_FLAG + (b & 31) * 2048, 16));
    fl.b = __builtin_bit_cast(u64, __builtin_amdgcn_raw_buffer_load_b64(rf, pofs, (int)WS_FLAG + (b & 31) * 2048 + 512, 16));
  }
  SCHED();
  WAIT_VM(0);
  __builtin_amdgcn_s_setprio(0);
  STAMP(10);
  LBAR();
  if (w == 7 && lane < 32) __hip_atomic_store((gu64*)(ws + WS_FLAG + lane * 2048) + ((slot & 1) ? 128 + cg : tt * 8 + hg), tag, RLX_AGENT);
  out_role(ws, out, rt, ct, tag, fl, smem STAMP_PASS);
  STAMP_FLUSH(ws);
}

extern "C" void kernel_launch(void* const* d_in, const int* in_sizes, int n_in, void* d_out, int out_size, void* d_ws,
                              size_t ws_size, hipStream_t stream) {
  (void)in_sizes; (void)n_in; (void)out_size; (void)ws_size;
  (void)hipFuncSetAttribute((const void*)fused, hipFuncAttributeMaxDynamicSharedMemorySize, 163840);
  fused<<<256, 512, 163840, stream>>>((const u16*)d_in[0], (const u16*)d_in[1], (const u16*)d_in[2], (const u16*)d_in[4],
                                      (const u16*)d_in[5], (const u16*)d_in[6], (const u16*)d_in[7], (const u16*)d_in[8],
                                      (const u16*)d_in[9], (const u16*)d_in[10], (char*)d_ws, (u16*)d_out);
}
```

```cpp
#include <hip/hip_runtime.h>
#include <hip/hip_bf16.h>
#include <stdint.h>

typedef unsigned int u32;
typedef unsigned short u16;
typedef __attribute__((ext_vector_type(8))) short s16x8;
typedef __attribute__((ext_vector_type(4))) short s16x4;
typedef __attribute__((ext_vector_type(8))) _Float16 h16x8;
typedef __attribute__((ext_vector_type(2))) _Float16 h16x2;
typedef __attribute__((ext_vector_type(2))) __bf16 b16x2;
typedef __attribute__((ext_vector_type(2))) float f32x2;
typedef __attribute__((ext_vector_type(4))) float f32x4;
typedef __attribute__((ext_vector_type(16))) float f32x16;
typedef __attribute__((ext_vector_type(2))) u32 u32x2;
typedef __attribute__((ext_vector_type(4))) u32 u32x4;
#define LDS_AS __attribute__((address_space(3)))
#define DI __device__ __forceinline__

static constexpr int SEQ = 2048, DM = 512;
#define KVC 1
static constexpr size_t WS_KV = (6u << 20) + 65536;
static constexpr int KV_CP = 36864;
static constexpr size_t WS_G = 65536;
static constexpr size_t WS_PQ = 1u << 20;
static constexpr size_t WS_BT = (4u << 20) + 4096;
static constexpr int WS_USED = 8 << 20;
#define SYNCO 49152
static constexpr size_t WS_SYNC = SYNCO;
static constexpr size_t WS_FLAG = 131072;
typedef unsigned long long u64;
typedef __attribute__((address_space(1))) u64 gu64;
typedef __attribute__((address_space(1))) u32 gu32;
#define RLX_AGENT __ATOMIC_RELAXED, __HIP_MEMORY_SCOPE_AGENT
__device__ __forceinline__ u64 next_tag(u64 e) { return e * 6364136223846793005ull + 1442695040888963407ull; }

#define STAMP_ARG
#define STAMP_PASS
#define STAMP_DECL do {} while (0)
#define STAMP(i) do {} while (0)
#define CYC(i) do {} while (0)
#define CYCD(i, j) do {} while (0)
#define STAMP_FLUSH(wsp) do {} while (0)
#define MIXM 2
#define TURN_AT 5
#define POLL_SLEEP 2
#define ST_AUX 16
#define WAIT_VM(n) asm volatile("s_waitcnt vmcnt(" #n ")" ::: "memory")
#define WAIT_L0() __builtin_amdgcn_s_waitcnt(0xC07F)
#define RAW_BAR() __builtin_amdgcn_s_barrier()
#define SCHED() __builtin_amdgcn_sched_barrier(0)
#define LBAR() do { WAIT_L0(); asm volatile("" ::: "memory"); RAW_BAR(); asm volatile("" ::: "memory"); } while (0)
typedef LDS_AS char* lptr;
#define LD(T, p) (*(const LDS_AS T*)(p))
#define ST(T, p) (*(LDS_AS T*)(p))

DI u32 pk_bf16(float a, float b) { return __builtin_bit_cast(u32, __builtin_convertvector((f32x2){a, b}, b16x2)); }
DI u32 pk_f16(f32x2 v) { return __builtin_bit_cast(u32, __builtin_convertvector(v, h16x2)); }
DI float bf_lo(u32 v) { return __builtin_bit_cast(float, v << 16); }
DI float bf_hi(u32 v) { return __builtin_bit_cast(float, v & 0xffff0000u); }
DI float u2f(u32 v) { return __builtin_bit_cast(float, v); }
DI b16x2 u2b(u32 v) { return __builtin_bit_cast(b16x2, v); }
DI float rbf(float v) { return bf_lo(pk_bf16(v, v)); }
static constexpr float LOG2E = 1.44269504088896341f, EPS = 1e-6f;
DI float phi_f(float t) {
  const float hi = __builtin_bit_cast(float, max(__builtin_bit_cast(int, t), __builtin_bit_cast(int, EPS)));
  return hi + __builtin_amdgcn_fmed3f(__builtin_amdgcn_exp2f(__builtin_fmaf(t, LOG2E, -EPS * LOG2E)), 0.f, 1.f);
}
DI float exp_f(float v) {
  const float p = v * LOG2E;
  const float r = __builtin_fmaf(v, 1.925963033500011e-8f, __builtin_fmaf(v, LOG2E, -p));
  return __builtin_amdgcn_exp2f(p) * __builtin_fmaf(r, 0.6931471805599453f, 1.f);
}
#define DPP_F(v, ctrl, rmask, bc) __builtin_bit_cast(float, __builtin_amdgcn_update_dpp(0, __builtin_bit_cast(int, v), ctrl, rmask, 0xf, bc))
DI float wave_sum(float r) {
  r += DPP_F(r, 0x111, 0xf, true); r += DPP_F(r, 0x112, 0xf, true); r += DPP_F(r, 0x114, 0xf, true); r += DPP_F(r, 0x118, 0xf, true);
  r += DPP_F(r, 0x142, 0xa, false); r += DPP_F(r, 0x143, 0xc, false);
  return r;
}
DI u32 wave_umax(u32 r) {
#define DPP_U(v, ctrl, rmask, bc) (u32)__builtin_amdgcn_update_dpp(0, (int)(v), ctrl, rmask, 0xf, bc)
  r = max(r, DPP_U(r, 0x111, 0xf, true)); r = max(r, DPP_U(r, 0x112, 0xf, true)); r = max(r, DPP_U(r, 0x114, 0xf, true));
  r = max(r, DPP_U(r, 0x118, 0xf, true)); r = max(r, DPP_U(r, 0x142, 0xa, false)); r = max(r, DPP_U(r, 0x143, 0xc, false));
  return (u32)__builtin_amdgcn_readlane((int)r, 63);
}
DI s16x4 tr_read(lptr p) { return __builtin_amdgcn_ds_read_tr16_b64_v4i16((LDS_AS s16x4*)p); }
DI s16x8 cat8(s16x4 a, s16x4 b) { return __builtin_shufflevector(a, b, 0, 1, 2, 3, 4, 5, 6, 7); }
typedef __amdgpu_buffer_rsrc_t rsrc_t;
DI rsrc_t mk_rsrc(const void* p, int bytes) { return __builtin_amdgcn_make_buffer_rsrc((void*)p, 0, bytes, 0x00020000); }
DI void store16(u32x4 v, rsrc_t r, int off) { __builtin_amdgcn_raw_buffer_store_b128(v, r, off, 0, ST_AUX); }
DI void blds16(rsrc_t r, lptr l, int voff, int soff) { __builtin_amdgcn_raw_ptr_buffer_load_lds(r, (LDS_AS void*)l, 16, voff, soff, 0, 0); }
#define CM_MUL(t, a, w) "v_pk_mul_f32 " t ", " a ", " w " op_sel:[1,1] op_sel_hi:[1,0]\n\t"
#define CM_FMA(r, a, w, t) "v_pk_fma_f32 " r ", " a ", " w ", " t " op_sel_hi:[0,1,1] neg_lo:[0,0,1]\n\t"
DI void cmul2(f32x2 (&r)[2], const f32x2 (&a)[2], const f32x2 (&w)[2]) {
  f32x2 r0, r1;
  asm(CM_MUL("%0", "%2", "%4") CM_MUL("%1", "%3", "%5") CM_FMA("%0", "%2", "%4", "%0") CM_FMA("%1", "%3", "%5", "%1")
      : "=&v"(r0), "=&v"(r1) : "v"(a[0]), "v"(a[1]), "v"(w[0]), "v"(w[1]));
  r[0] = r0; r[1] = r1;
}
DI void cmul4(f32x2 (&r)[2], const f32x2 (&a)[2], const f32x2 (&w)[2], f32x2 (&q)[2], const f32x2 (&b)[2], const f32x2 (&v)[2]) {
  f32x2 r0, r1, q0, q1;
  asm(CM_MUL("%0", "%4", "%6") CM_MUL("%1", "%5", "%7") CM_MUL("%2", "%8", "%10") CM_MUL("%3", "%9", "%11")
      CM_FMA("%0", "%4", "%6", "%0") CM_FMA("%1", "%5", "%7", "%1") CM_FMA("%2", "%8", "%10", "%2") CM_FMA("%3", "%9", "%11", "%3")
      : "=&v"(r0), "=&v"(r1), "=&v"(q0), "=&v"(q1)
      : "v"(a[0]), "v"(a[1]), "v"(w[0]), "v"(w[1]), "v"(b[0]), "v"(b[1]), "v"(v[0]), "v"(v[1]));
  r[0] = r0; r[1] = r1; q[0] = q0; q[1] = q1;
}
DI f32x2 cexp_p(float alpha, float theta, float p) {
  float m = __builtin_amdgcn_exp2f(alpha * p);
  float f = __builtin_amdgcn_fractf(theta * p);
  return (f32x2){m * __builtin_amdgcn_cosf(f), m * __builtin_amdgcn_sinf(f)};
}
#define MFMA16(a, b, c) __builtin_amdgcn_mfma_f32_16x16x32_bf16((a), (b), (c), 0, 0, 0)
#define MFMA16H(a, b, c) __builtin_amdgcn_mfma_f32_16x16x32_f16((a), (b), (c), 0, 0, 0)
#define MFMA32(a, b, c) __builtin_amdgcn_mfma_f32_32x32x16_bf16((a), (b), (c), 0, 0, 0)

static constexpr int STAGE = 40960;
static constexpr int WOQ = 17408;
static constexpr int P2_WO = 0, P2_KV = 4 * WOQ, P2_OK = P2_KV + 17408, P2_BT = P2_KV + 18432, P2_EX = P2_BT + 8192;

DI u64 load_epoch(char* ws) {
  int vzero;
  asm volatile("v_mov_b32 %0, 0" : "=v"(vzero));
  return __hip_atomic_load((gu64*)(ws + WS_SYNC + vzero), RLX_AGENT);
}
DI u32 touch_pages(const char* ws, const u16* out, int rt, int lane) {
  const char* p = lane < 4 ? ws + (size_t)((0xf871u >> (4 * lane)) & 15) * (512u << 10) : (const char*)out + (size_t)rt * 65536;
  return lane < 5 ? *(const u32*)p : 0u;
}
DI void fetch_wo(const u16* __restrict__ wo, int ct, int q, int lane, lptr smem) {
  const rsrc_t rwo = mk_rsrc(wo, DM * DM * 2);
  const int sr = lane >> 3, sp = lane & 7, sc = (sp ^ (((sr >> 1) & 1) << 2)) << 4;
#pragma unroll
  for (int i = 0; i < 16; ++i) blds16(rwo, smem + P2_WO + q * WOQ + 1024 * i, sr * 1024 + sc, (128 * q + 8 * i) * 1024 + 128 * ct);
}

DI void gemm_role(const u16* __restrict__ x, const u16* __restrict__ wqkv, const u16* __restrict__ wo, int ct, char* __restrict__ ws, const u16* out, int rt, int tt, int hg,
                  u64& epoch, lptr smem STAMP_ARG) {
  const int tid = threadIdx.x, lane = tid & 63;
  const int w = __builtin_amdgcn_readfirstlane(tid >> 6);
  const int wm = w >> 2, wh = w & 3, g = lane >> 4, l15 = lane & 15;
  const int sr = lane >> 3, sp = lane & 7;
  const rsrc_t rx = mk_rsrc(x, SEQ * DM * 2), rw = mk_rsrc(wqkv, DM * 1536 * 2);
  const int xv = sr * 1024 + ((sp ^ sr) << 4), xs0 = (tt * 128 + 16 * w) * 1024;
  const int fw = ((sr >> 1) & 1) | ((w & 1) << 1);
  const int wv = sr * 3072 + ((sp ^ (fw << 1)) << 4), ws0 = 8 * w * 3072 + hg * 128;
  const int ROT = 2 * (tt & 3) + ((tt >> 2) & 1);
  const rsrc_t rwo = mk_rsrc(wo, DM * DM * 2);
  const int ov = sr * 1024 + ((sp ^ (((sr >> 1) & 1) << 2)) << 4);
#define ISS(s, i) do { lptr sb = smem + ((s) & 3) * STAGE; \
    if ((s) >= 8) { if ((i) < 4) blds16(rwo, smem + P2_WO + (w >> 1) * WOQ + (64 * (w & 1) + 32 * ((s) - 8) + 8 * (i)) * 128, ov, (64 * w + 32 * ((s) - 8) + 8 * (i)) * 1024 + 128 * ct); } \
    else if ((i) < 2) blds16(rx, sb + w * 2048 + (i) * 1024, xv, xs0 + (((s) + ROT) & 7) * 128 + (i) * 8192); \
    else blds16(rw, sb + 16384 + ((i) - 2) * 8192 + w * 1024, wv, ws0 + (((s) + ROT) & 7) * 64 * 3072 + ((i) - 2) * 1024); } while (0)
#define ISSUE(s) do { ISS(s, 0); ISS(s, 1); ISS(s, 2); ISS(s, 3); ISS(s, 4); } while (0)
  gu64* sync_z = (gu64*)(ws + WS_SYNC + 128 + hg * 128);
  ISSUE(0);
  STAMP(19);
  SCHED(); RAW_BAR(); SCHED();
  u32 pv2 = 0;
  SCHED();
  ISSUE(1);
  SCHED();
  epoch = load_epoch(ws);
  const rsrc_t rws = mk_rsrc(ws, WS_USED);
  const bool clear = tt == 0 && w == 7;
  if (clear) {
    const u32x4 z4 = {0u, 0u, 0u, 0u};
#pragma unroll
    for (int i = 0; i < 4; ++i) store16(z4, rws, lane * 16 + (int)WS_KV + hg * 4352 + 1024 * i);
    store16(z4, rws, (lane & 15) * 16 + (int)WS_KV + hg * 4352 + 4096);
  }
  SCHED();
  const int xo = (64 * wm + l15) * 128 + ((g ^ (lane & 7)) << 4);
  const int q4 = l15 >> 2, p4 = lane & 3;
  const int wo_ = 16384 + (8 * g + q4) * 128 + ((wh ^ (((q4 >> 1) & 1) | ((g & 1) << 1))) << 5) + 8 * p4;
  f32x4 aq[4], ak[4], av[4];
#pragma unroll
  for (int i = 0; i < 4; ++i) { aq[i] = (f32x4){EPS, EPS, EPS, EPS}; ak[i] = aq[i]; av[i] = (f32x4){0, 0, 0, 0}; }
  s16x8 xa[4], wa[3], xb[4], wb[3];
#define LDWF(wf, s, ks, st) wf[st] = cat8(tr_read(smem + ((s) & 3) * STAGE + wo_ + (st) * 8192 + (ks) * 4096), tr_read(smem + ((s) & 3) * STAGE + wo_ + (st) * 8192 + (ks) * 4096 + 512))
#define LDXF(xf, s, ks, mt) xf[mt] = LD(s16x8, smem + ((s) & 3) * STAGE + ((xo ^ ((ks) * 64)) + (mt) * 2048))
#define LOADF(xf, wf, s, ks) do { LDWF(wf, s, ks, 0); LDWF(wf, s, ks, 1); LDWF(wf, s, ks, 2); \
    LDXF(xf, s, ks, 0); LDXF(xf, s, ks, 1); LDXF(xf, s, ks, 2); LDXF(xf, s, ks, 3); } while (0)
#define MM3(xf, wf, mt) do { aq[mt] = MFMA16(wf[0], xf[mt], aq[mt]); ak[mt] = MFMA16(xf[mt], wf[1], ak[mt]); av[mt] = MFMA16(xf[mt], wf[2], av[mt]); \
    asm volatile("" : "+v"(aq[mt]), "+v"(ak[mt]), "+v"(av[mt])); } while (0)
#define HALF(xf, wf, xn, wn, ls, lk, is) do { SCHED(); MM3(xf, wf, 0); SCHED(); \
    if ((ls) >= 0) { LDWF(wn, ls, lk, 0); LDXF(xn, ls, lk, 0); LDWF(wn, ls, lk, 1); } if ((is) >= 0) ISS(is, 0); SCHED(); MM3(xf, wf, 1); SCHED(); \
    if ((ls) >= 0) { LDWF(wn, ls, lk, 2); LDXF(xn, ls, lk, 1); LDXF(xn, ls, lk, 2); } if ((is) >= 0) ISS(is, 1); SCHED(); MM3(xf, wf, 2); SCHED(); \
    if ((ls) >= 0) LDXF(xn, ls, lk, 3); if ((is) >= 0) { ISS(is, 2); ISS(is, 3); } SCHED(); MM3(xf, wf, 3); SCHED(); \
    if ((is) >= 0) ISS(is, 4); SCHED(); } while (0)
#define STEP(s, vm, i1, i2) do { WAIT_L0(); HALF(xa, wa, xb, wb, s, 1, i1); \
    WAIT_L0(); WAIT_VM(vm); RAW_BAR(); HALF(xb, wb, xa, wa, (s) + 1, 0, i2); } while (0)
  STAMP(1);
  if (clear) WAIT_VM(11); else WAIT_VM(6);
  RAW_BAR();
  STAMP(2);
  LOADF(xa, wa, 0, 0);
  STEP(0, 5, 2, -1);
  if (clear && lane == 0) __hip_atomic_store(sync_z, next_tag(epoch), RLX_AGENT);
  STAMP(3);
  STEP(1, 5, 3, -1);
  STEP(2, 5, 4, 5);
  STEP(3, 5, -1, 6);
  STAMP(4); CYC(20);
  if (w == 0) pv2 = touch_pages(ws, out, rt, lane);
  STEP(4, 5, -1, 7);
  u64 ready = __hip_atomic_load(sync_z, RLX_AGENT);
  STEP(5, 5, -1, 8);
  if (w < 4) __builtin_amdgcn_s_setprio(2);
  STEP(6, 8, 9, -1);
  STAMP(5); CYC(21); CYCD(20, 21); CYC(21);
  {
    const u64 tag = next_tag(epoch);
#pragma unroll 1
    for (int spin = 0; ready != tag && spin < (1 << 22); ++spin) {
      __builtin_amdgcn_s_sleep(2); ready = __hip_atomic_load(sync_z, RLX_AGENT); __builtin_amdgcn_s_waitcnt(0x0F70);
    }
  }
  WAIT_L0(); HALF(xa, wa, xb, wb, 7, 1, -1); WAIT_L0(); SCHED();
#pragma unroll
  for (int mt = 0; mt < 4; ++mt) aq[mt] = MFMA16(wb[0], xb[mt], aq[mt]);
  asm volatile("" : "+v"(aq[0]), "+v"(aq[1]), "+v"(aq[2]), "+v"(aq[3]));
  SCHED();
#pragma unroll
  for (int mt = 0; mt < 4; ++mt) { ak[mt] = MFMA16(xb[mt], wb[1], ak[mt]); av[mt] = MFMA16(xb[mt], wb[2], av[mt]); }
  asm volatile("" : "+v"(ak[0]), "+v"(ak[1]), "+v"(ak[2]), "+v"(ak[3]), "+v"(av[0]), "+v"(av[1]), "+v"(av[2]), "+v"(av[3]));
  SCHED();
  asm volatile("" :: "v"(pv2));
  STAMP(6);
#undef ISS
#undef ISSUE
#undef LOADF
#undef MM3
#undef HALF
#undef LDWF
#undef LDXF
#undef STEP
  const int h = 4 * hg + wh;
  const int pqv = (g >> 1) * 512 + l15 * 16 + (g & 1) * 256, pqs = (int)WS_PQ + ((tt * 4 + 2 * wm) * 32 + h) * 1024;
#pragma unroll
  for (int pr = 0; pr < 2; ++pr) {
    const f32x4 q0 = aq[2 * pr], q1 = aq[2 * pr + 1];
    const auto s0 = __builtin_amdgcn_permlane16_swap(pk_bf16(phi_f(q0[0]), phi_f(q0[1])), pk_bf16(phi_f(q1[0]), phi_f(q1[1])), false, false);
    const auto s1 = __builtin_amdgcn_permlane16_swap(pk_bf16(phi_f(q0[2]), phi_f(q0[3])), pk_bf16(phi_f(q1[2]), phi_f(q1[3])), false, false);
    store16((u32x4){s0[0], s1[0], s0[1], s1[1]}, rws, pqv + pqs + pr * 32768);
  }
  SCHED();
  STAMP(7);
  f32x4 kvt = {0, 0, 0, 0}, zz = {0, 0, 0, 0};
  const u32x4 ones_u = {0x3f803f80u, 0x3f803f80u, 0x3f803f80u, 0x3f803f80u};
#pragma unroll
  for (int pr = 0; pr < 2; ++pr) {
    u32x4 pk, vv;
#pragma unroll
    for (int m = 0; m < 2; ++m) {
      const f32x4 kk = ak[2 * pr + m], v4 = av[2 * pr + m];
      pk[2 * m] = pk_bf16(phi_f(kk[0]), phi_f(kk[1])); pk[2 * m + 1] = pk_bf16(phi_f(kk[2]), phi_f(kk[3]));
      vv[2 * m] = pk_bf16(v4[0], v4[1]); vv[2 * m + 1] = pk_bf16(v4[2], v4[3]);
    }
    kvt = MFMA16(__builtin_bit_cast(s16x8, vv), __builtin_bit_cast(s16x8, pk), kvt);
    zz = MFMA16(__builtin_bit_cast(s16x8, ones_u), __builtin_bit_cast(s16x8, pk), zz);
  }
  lptr red = smem + 4 * WOQ;
#pragma unroll
  for (int r = 0; r < 4; ++r) ST(float, red + (wm * 4 + wh) * 1088 + l15 * 4 + (4 * g + r) * 64) = kvt[r];
  if (g == 0) ST(float, red + (wm * 4 + wh) * 1088 + l15 * 4 + 1024) = zz[0];
  LBAR();
  STAMP(8);
  float* kvacc = (float*)(ws + WS_KV + (tt % KVC) * KV_CP) + hg * 1088 + tid;
  atomicAdd(kvacc, LD(float, red + tid * 4) + LD(float, red + 4352 + tid * 4));
  atomicAdd(kvacc + 512, LD(float, red + 2048 + tid * 4) + LD(float, red + 4352 + 2048 + tid * 4));
  if (tid < 64) atomicAdd(kvacc + 1024, LD(float, red + 4096 + tid * 4) + LD(float, red + 4352 + 4096 + tid * 4));
  STAMP(9); CYC(22); CYCD(21, 22);
}

static constexpr int TB_ROW = 544, TB_BUF = 16 * TB_ROW, TB_CH = 2 * TB_BUF;
static constexpr int KR_OFF = 4 * TB_CH, KR_CP = 4160, KR_CH = 2 * KR_CP;
static constexpr int U_OFF = KR_OFF + 4 * KR_CH, U_CH = (2048 + 240) * 2;
static constexpr int EX_OFF = U_OFF + 4 * U_CH;
static_assert(EX_OFF + 32768 <= 163840 && TB_CH == WOQ && P2_EX + 65536 <= 163840, "LDS map");

DI void conv_role(const u16* __restrict__ x, u32 (&pr)[5], u32x2 (&xr)[8], u32 pf, const u16* __restrict__ dsk, const u16* __restrict__ gw,
                  const u16* __restrict__ wo, char* __restrict__ ws, const u16* out, int rt, int cg, int ct, u64& epoch, lptr smem STAMP_ARG) {
  const int tid = threadIdx.x, lane = tid & 63;
  const int w = __builtin_amdgcn_readfirstlane(tid >> 6);
  const int ch = w & 3, half = w >> 2, g = lane >> 4, l15 = lane & 15;
  const int c = 4 * cg + ch;
  const rsrc_t rx = mk_rsrc(x, SEQ * DM * 2);
  u32 dsk2, pv = 0;
  u32x4 gx[4], gwv;
  const rsrc_t rws = mk_rsrc(ws, WS_USED);
  f32x4 acc[8];
#pragma unroll
  for (int i = 0; i < 8; ++i) acc[i] = (f32x4){0, 0, 0, 0};
#define PINC() do { asm volatile("" : "+v"(acc[0]), "+v"(acc[1]), "+v"(acc[2]), "+v"(acc[3]), "+v"(acc[4]), "+v"(acc[5]), "+v"(acc[6]), "+v"(acc[7])); SCHED(); } while (0)
  const int t0 = 16 * l15 + 4 * g;
  auto put_b = [&](int T, f32x4 y0, f32x4 y1) {
    lptr up = smem + U_OFF + ch * U_CH + 480 + 2 * t0 + 512 * T;
    u32 dd = dsk2;
    asm volatile("" : "+v"(dd));
    const float dsk_f = (c & 1) ? bf_hi(dd) : bf_lo(dd);
    const u32x2 u0 = LD(u32x2, up), u1 = LD(u32x2, up + 512);
    y0 += (f32x4){bf_lo(u0.x), bf_hi(u0.x), bf_lo(u0.y), bf_hi(u0.y)} * dsk_f;
    y1 += (f32x4){bf_lo(u1.x), bf_hi(u1.x), bf_lo(u1.y), bf_hi(u1.y)} * dsk_f;
    const auto sx = __builtin_amdgcn_permlane16_swap(pk_bf16(y0[0], y0[1]), pk_bf16(y1[0], y1[1]), false, false);
    const auto sy = __builtin_amdgcn_permlane16_swap(pk_bf16(y0[2], y0[3]), pk_bf16(y1[2], y1[3]), false, false);
    store16((u32x4){sx[0], sy[0], sx[1], sy[1]}, rws, (int)WS_BT + c * 4096 + 32 * l15 + 16 * (g >> 1) + 512 * (g & 1) + 512 * T);
  };
  if (w < 4) {
    const int cw = 4 * cg + w;
    if (lane < 8) ST(u32x4, smem + KR_OFF + w * KR_CH + (lane >> 2) * KR_CP + 4096 + 16 * (lane & 3)) = (u32x4){0u, 0u, 0u, 0u};
    __builtin_amdgcn_s_setprio(2);
    asm volatile("s_waitcnt vmcnt(0)" : "+v"(pr[0]), "+v"(pr[1]), "+v"(pr[2]), "+v"(pr[3]), "+v"(pr[4]));
    const u32 lar2 = pr[0], aim2 = pr[1], cre2 = pr[2], cim2 = pr[3], ldt_u = pr[4];
    const float dt = rbf(exp_f(bf_lo(ldt_u)));
    float al[2], th[2];
    al[0] = -rbf(exp_f(bf_lo(lar2))) * dt * LOG2E; al[1] = -rbf(exp_f(bf_hi(lar2))) * dt * LOG2E;
    th[0] = bf_lo(aim2) * dt * 0.15915494309189535f; th[1] = bf_hi(aim2) * dt * 0.15915494309189535f;
    STAMP(1);
    u32 ce = wave_umax(max(max(cre2 & 0x7f80u, (cre2 >> 16) & 0x7f80u), max(cim2 & 0x7f80u, (cim2 >> 16) & 0x7f80u))) >> 7;
    ce = ce < 1 ? 1 : (ce > 253 ? 253 : ce);
    const float cinv = __builtin_bit_cast(float, (254u - ce) << 23);
    const float kscale = 2.f * dt * __builtin_bit_cast(float, ce << 23);
    lptr tb = smem + w * TB_CH + lane * 8, tr = smem + w * TB_CH + 16 * g, kr = smem + KR_OFF + w * KR_CH;
#define PUT(buf, e, v) ST(u32x2, tb + (buf) * TB_BUF + (e) * TB_ROW) = (u32x2){pk_f16(v[0]), pk_f16(v[1])}
#define CONJ(v) ((f32x2){(v).x, -(v).y})
#define GETF(f, buf, row) do { lptr p_ = tr + (buf) * TB_BUF + (row) * TB_ROW; \
      _Pragma("unroll") for (int ks = 0; ks < 8; ++ks) f[ks] = LD(h16x8, p_ + 64 * ks); } while (0)
#define TILE(d, f) do { d = (f32x4){0, 0, 0, 0}; _Pragma("unroll") for (int ks = 0; ks < 8; ++ks) d = MFMA16H(f[ks], fx[ks], d); } while (0)
    f32x2 y[17][2], wB[2], w16[2], w32[2], xa[2], xb[2], aS[6][2], t[2], t2[2];
#pragma unroll
    for (int m = 0; m < 2; ++m) {
      y[1][m] = CONJ(cexp_p(al[m], th[m], 1.f)); w16[m] = cexp_p(al[m], th[m], 16.f); wB[m] = CONJ(cexp_p(al[m], th[m], 256.f));
      y[16][m] = CONJ(w16[m]);
    }
    xa[0] = (f32x2){bf_lo(cre2) * cinv, bf_lo(cim2) * cinv}; xa[1] = (f32x2){bf_hi(cre2) * cinv, bf_hi(cim2) * cinv};
    ST(u32x2, tb + TB_BUF) = (u32x2){0x3c00u, 0x3c00u};
    PUT(0, 0, xa); PUT(1, 1, y[1]);
    cmul4(xb, xa, w16, w32, w16, w16); cmul4(y[2], y[1], y[1], aS[0], wB, wB);
    PUT(0, 1, xb); PUT(1, 2, y[2]);
#pragma unroll
    for (int i = 1; i < 8; ++i) {
      cmul4(xa, xa, w32, xb, xb, w32);
      if (i < 7) cmul4(y[2 * i + 1], y[2 * i - 1], y[2], y[2 * i + 2], y[2 * i], y[2]); else cmul2(y[15], y[13], y[2]);
      PUT(0, 2 * i, xa); PUT(0, 2 * i + 1, xb); PUT(1, 2 * i + 1, y[2 * i + 1]);
      if (i < 7) PUT(1, 2 * i + 2, y[2 * i + 2]);
    }
    STAMP(2);
    h16x8 fx[8], fa[8], fb[8];
    f32x4 kd;
    const int gq = l15 >> 2, r3 = min(l15 & 3, 2), yk = 2044 - 16 * l15 - 4 * g;
    SCHED();
    GETF(fx, 0, l15); GETF(fa, 1, l15);
    SCHED();
    PUT(0, 0, wB);
#pragma unroll
    for (int m = 1; m < 9; m += 2) {
      cmul4(t, wB, y[2 * m], t2, wB, y[2 * m + 2]);
      if (m == 1) cmul4(aS[1], aS[0], wB, aS[2], aS[0], aS[0]);
      if (m == 3) cmul4(aS[3], aS[2], wB, aS[4], aS[2], aS[0]);
      if (m == 5) cmul2(aS[5], aS[4], wB);
      PUT(0, m, t); PUT(0, m + 1, t2);
    }
    SCHED();
    GETF(fb, 0, (int)((0x8766654443222100ull >> (4 * l15)) & 15));
    TILE(kd, fa);
    SCHED();
#pragma unroll
    for (int s = 0; s < 2; ++s) {
      PUT(1, 5 * s, aS[s]);
#pragma unroll
      for (int m = 1; m < 5; m += 2) { cmul4(t, aS[s], y[4 * m], t2, aS[s], y[4 * m + 4]); PUT(1, 5 * s + m, t); PUT(1, 5 * s + m + 1, t2); }
    }
    {
      const f32x4 k0 = kd * kscale;
      const u32 p32 = pk_bf16(k0[3], k0[2]), p10 = pk_bf16(k0[1], k0[0]);
      ST(u32x2, kr + 2 * yk) = (u32x2){p32, p10};
      ST(u16, kr + KR_CP + 2 * yk + 2) = (u16)p32;
      ST(u32, kr + KR_CP + 2 * yk + 4) = pk_bf16(k0[2], k0[1]);
      ST(u16, kr + KR_CP + 2 * yk + 8) = (u16)(p10 >> 16);
    }
    SCHED();
    GETF(fa, 1, 5 * (gq >> 1) + 2 * (gq & 1) + r3);
    TILE(kd, fb);
    SCHED();
#pragma unroll
    for (int s = 2; s < 6; ++s) {
      cmul4(t, aS[s], y[8], t2, aS[s], y[16]);
      PUT(0, 3 * (s - 2), aS[s]); PUT(0, 3 * (s - 2) + 1, t); PUT(0, 3 * (s - 2) + 2, t2);
    }
    {
      const f32x4 k1 = kd * kscale;
      const u32 q10 = pk_bf16(k1[1], k1[0]);
      ST(u32x2, kr + 2 * (yk - 256)) = (u32x2){pk_bf16(k1[3], k1[2]), q10};
      ST(u32x2, kr + KR_CP + 2 * (yk - 256)) = (u32x2){pk_bf16(k1[3], k1[3]), pk_bf16(k1[2], k1[1])};
      if (lane == 0) ST(u16, kr + KR_CP + 2 * 1792) = (u16)q10;
    }
    SCHED();
    GETF(fb, 0, 3 * gq + r3);
    TILE(kd, fa);
    STAMP(3);
    {
      const f32x4 k0 = kd * kscale;
      const int y2 = 2040 - (512 + 256 * (g >> 1) + 16 * l15 + 8 * (g & 1));
      const u32 b2 = pk_bf16(k0[2], k0[2]), b1 = pk_bf16(k0[1], k0[1]), b0 = pk_bf16(k0[0], k0[0]);
      ST(u32x4, kr + 2 * y2) = (u32x4){b2, b1, pk_bf16(k0[1], k0[0]), b0};
      ST(u32x4, kr + KR_CP + 2 * y2) = (u32x4){b2, pk_bf16(k0[2], k0[1]), b1, b0};
    }
    TILE(kd, fb);
    {
      const f32x4 k1 = kd * kscale;
      const int y3 = 2032 - (1024 + 256 * g + 16 * l15);
      const u32 a2 = pk_bf16(k1[2], k1[2]), a1 = pk_bf16(k1[1], k1[1]), a0 = pk_bf16(k1[0], k1[0]);
      ST(u32x4, kr + 2 * y3) = (u32x4){a2, a2, a1, a1};
      ST(u32x4, kr + 2 * y3 + 16) = (u32x4){a1, pk_bf16(k1[1], k1[0]), a0, a0};
      ST(u32x4, kr + KR_CP + 2 * y3) = (u32x4){a2, a2, pk_bf16(k1[2], k1[1]), a1};
      ST(u32x4, kr + KR_CP + 2 * y3 + 16) = (u32x4){a1, a1, a0, a0};
    }
#undef PUT
#undef GETF
#undef TILE
    SCHED();
    __builtin_amdgcn_s_setprio(0);
    WAIT_L0(); SCHED();
    fetch_wo(wo, ct, ch, lane, smem);
    STAMP(4);
  } else {
    const int t2 = (tid + 16 * cg) & 255;
    if (lane < 30) ST(u32x4, smem + U_OFF + ch * U_CH + lane * 16) = (u32x4){0u, 0u, 0u, 0u};
    STAMP(1);
#pragma unroll
    for (int hf = 0; hf < 2; ++hf) {
      lptr ub = smem + U_OFF + 480 + t2 * 8 + hf * 2048;
      if (hf == 0) asm volatile("s_waitcnt vmcnt(4)" : "+v"(xr[0]), "+v"(xr[1]), "+v"(xr[2]), "+v"(xr[3]) : "v"(pf));
      else asm volatile("s_waitcnt vmcnt(0)" : "+v"(xr[4]), "+v"(xr[5]), "+v"(xr[6]), "+v"(xr[7]));
      const u32x2 x0 = xr[4 * hf], x1 = xr[4 * hf + 1], x2 = xr[4 * hf + 2], x3 = xr[4 * hf + 3];
      ST(u32x2, ub + 0 * U_CH) = (u32x2){__builtin_amdgcn_perm(x1.x, x0.x, 0x05040100u), __builtin_amdgcn_perm(x3.x, x2.x, 0x05040100u)};
      ST(u32x2, ub + 1 * U_CH) = (u32x2){__builtin_amdgcn_perm(x1.x, x0.x, 0x07060302u), __builtin_amdgcn_perm(x3.x, x2.x, 0x07060302u)};
      ST(u32x2, ub + 2 * U_CH) = (u32x2){__builtin_amdgcn_perm(x1.y, x0.y, 0x05040100u), __builtin_amdgcn_perm(x3.y, x2.y, 0x05040100u)};
      ST(u32x2, ub + 3 * U_CH) = (u32x2){__builtin_amdgcn_perm(x1.y, x0.y, 0x07060302u), __builtin_amdgcn_perm(x3.y, x2.y, 0x07060302u)};
      SCHED();
    }
    STAMP(4);
  }
  lptr ka[8];
  {
    const int yb = 2031 - l15 + 8 * g - 128 * half - 96 - 1792;
    lptr k7 = smem + KR_OFF + ch * KR_CH + (yb & 1) * KR_CP + ((yb + 1) >> 1) * 4;
#pragma unroll
    for (int n = 0; n < 8; ++n) { ka[n] = k7 + 512 * (7 - n); asm volatile("" : "+v"(ka[n])); }
  }
  lptr ua = smem + U_OFF + ch * U_CH + (8 * g + 16 * l15 + 224 - 128 * half - 96) * 2;
  asm volatile("" : "+v"(ua));
  STAMP(6);
  LBAR();
  STAMP(7); CYC(20);
  {
    u32x4 A0[8], A1[8]; s16x8 B0[8], B1[8];
#define LOADC(A, B, rr) do { _Pragma("unroll") for (int n = 0; n < 8; ++n) { \
      lptr kp = ka[n] + 64 * (3 - (rr)); \
      A[n] = (u32x4){LD(u32, kp), LD(u32, kp + 4), LD(u32, kp + 8), LD(u32, kp + 12)}; \
      B[n] = LD(s16x8, ua + 64 * (3 - (rr)) + 512 * n); } } while (0)
#define MMC(A, B) do { _Pragma("unroll") for (int nu = 0; nu < 8; ++nu) _Pragma("unroll") for (int mu = 0; mu < 8 - nu; ++mu) \
      acc[nu + mu] = MFMA16(__builtin_bit_cast(s16x8, A[nu]), B[mu], acc[nu + mu]); } while (0)
#define MIXC() do { _Pragma("unroll") for (int q = 0; q < 12; ++q) { __builtin_amdgcn_sched_group_barrier(0x8, MIXM, 0); __builtin_amdgcn_sched_group_barrier(0x100, 2, 0); } \
      __builtin_amdgcn_sched_group_barrier(0x8, 36 - 12 * MIXM, 0); } while (0)
    LOADC(A0, B0, 0);
    SCHED();
    dsk2 = *(const u32*)(dsk + (c & ~1));
    epoch = load_epoch(ws);
    if (!half) {
#pragma unroll
      for (int r = 0; r < 4; ++r) gx[r] = __builtin_amdgcn_raw_buffer_load_b128(rx, lane * 16 + 1024 * r, (16 * cg + 4 * ch) * 1024, 0);
      gwv = *(const u32x4*)(gw + 8 * lane);
      pv = touch_pages(ws, out, rt, lane);
    }
    SCHED();
    WAIT_L0(); SCHED(); LOADC(A1, B1, 1); MMC(A0, B0); MIXC(); PINC();
    WAIT_L0(); SCHED(); LOADC(A0, B0, 2); MMC(A1, B1); MIXC(); PINC();
    WAIT_L0(); SCHED(); LOADC(A1, B1, 3); MMC(A0, B0); MIXC(); PINC();
    WAIT_L0(); SCHED(); MMC(A1, B1); PINC();
#undef LOADC
#undef MMC
#undef MIXC
#undef PINC
  }
  STAMP(8); CYC(21); CYCD(20, 21);
  {
    lptr ex = smem + EX_OFF + w * 4096 + lane * 16, ei = smem + EX_OFF + (w ^ 4) * 4096 + lane * 16;
    if (half) {
#pragma unroll
      for (int i = 0; i < 4; ++i) ST(f32x4, ex + 1024 * i) = acc[4 + i];
      LBAR();
      put_b(0, acc[0] + LD(f32x4, ei), acc[1] + LD(f32x4, ei + 1024));
      put_b(2, acc[2] + LD(f32x4, ei + 2048), acc[3] + LD(f32x4, ei + 3072));
    } else {
#pragma unroll
      for (int i = 0; i < 4; ++i) ST(f32x4, ex + 1024 * i) = acc[i];
      asm volatile("" :: "v"(pv));
      float sg[4];
#pragma unroll
      for (int r = 0; r < 4; ++r) {
        float s0 = 0.f;
#pragma unroll
        for (int i = 0; i < 4; ++i) s0 = __builtin_amdgcn_fdot2_f32_bf16(u2b(gx[r][i]), u2b(gwv[i]), s0, false);
        sg[r] = rbf(__builtin_amdgcn_rcpf(1.f + __builtin_amdgcn_exp2f(-LOG2E * rbf(wave_sum(s0)))));
      }
      if (lane == 63) store16(__builtin_bit_cast(u32x4, (f32x4){sg[0], sg[1], sg[2], sg[3]}), rws, (int)WS_G + (16 * cg + 4 * ch) * 4 + (lane - 63));
      LBAR();
      put_b(4, acc[4] + LD(f32x4, ei), acc[5] + LD(f32x4, ei + 1024));
      put_b(6, acc[6] + LD(f32x4, ei + 2048), acc[7] + LD(f32x4, ei + 3072));
      asm volatile(".Lfused_code_tail:");
    }
  }
  STAMP(9);
}

struct Tags { u64 a, b; };
DI Tags poll_load(char* ws, int off, int lane) {
  gu64* p = (gu64*)(ws + WS_FLAG + off) + lane;
  return Tags{__hip_atomic_load(p, RLX_AGENT), __hip_atomic_load(p + 64, RLX_AGENT)};
}
DI void poll_wait(char* ws, int off, int lane, u64 tag, Tags& f, u64 ma, u64 mb) {
#pragma unroll 1
  for (int spin = 0; spin < (1 << 20); ++spin) {
    if ((__ballot(f.a != tag) & ma) == 0 && (__ballot(f.b != tag) & mb) == 0) break;
    __builtin_amdgcn_s_sleep(POLL_SLEEP);
    f = poll_load(ws, off, lane);
    __builtin_amdgcn_s_waitcnt(0x0F70);
  }
}

DI void out_role(char* __restrict__ ws, u16* __restrict__ out, int rt, int ct, u64 tag, Tags fl, lptr smem STAMP_ARG) {
  const int tid = threadIdx.x, lane = tid & 63;
  const int w = __builtin_amdgcn_readfirstlane(tid >> 6);
  const int th = w & 1, kq = w >> 1, hh = lane >> 5, l31 = lane & 31;
  const rsrc_t rws = mk_rsrc(ws, WS_USED);
  const int foff = (blockIdx.x & 31) * 2048;
  if (w == 0) poll_wait(ws, foff, lane, tag, fl, ~0ull, ~0ull);
  RAW_BAR();
  STAMP(11);
  if (w == 1) fl = poll_load(ws, foff + 1024, lane);
  f32x4 kvr[KVC][5];
#pragma unroll
  for (int cp = 0; cp < KVC; ++cp) {
#pragma unroll
    for (int i = 0; i < 4; ++i) kvr[cp][i] = __builtin_bit_cast(f32x4, __builtin_amdgcn_raw_buffer_load_b128(rws, tid * 16, (int)WS_KV + cp * KV_CP + 8192 * i, 16));
    kvr[cp][4] = __builtin_bit_cast(f32x4, __builtin_amdgcn_raw_buffer_load_b128(rws, (tid & 127) * 16, (int)WS_KV + cp * KV_CP + 32768, 16));
  }
  SCHED(); RAW_BAR(); SCHED();
  u32x4 pf[8];
#define LOAD_PQ(i0, i1) do { _Pragma("unroll") for (int i = i0; i < i1; ++i) \
    pf[i] = __builtin_amdgcn_raw_buffer_load_b128(rws, lane * 16, (int)WS_PQ + ((2 * rt + th) * 32 + 8 * kq + i) * 1024, 16); } while (0)
  LOAD_PQ(0, 3);
  STAMP(12);
#pragma unroll
  for (int i = 0; i < 5; ++i) {
    f32x4 v = kvr[0][i];
#pragma unroll
    for (int cp = 1; cp < KVC; ++cp) v += kvr[cp][i];
    ST(u32x2, smem + P2_KV + (i < 4 ? tid + 512 * i : 2048 + (tid & 127)) * 8) = (u32x2){pk_bf16(v[0], v[1]), pk_bf16(v[2], v[3])};
  }
  f32x16 acc[2];
#pragma unroll
  for (int i = 0; i < 16; ++i) { acc[0][i] = 0.f; acc[1][i] = 0.f; }
  f32x16 cinit = acc[0];
  cinit[8] = EPS;
  lptr kva = smem + P2_KV + 8 * kq * 544 + min(l31, 16) * 32 + hh * 16;
  const int blk = (lane >> 4) & 1, q4 = (lane & 15) >> 2, p4 = lane & 3;
  const int sw = ((q4 >> 1) & 1) << 6;
  lptr wa0 = smem + P2_WO + kq * WOQ + (4 * hh + q4) * 128 + blk * 32 + p4 * 8 + sw;
  lptr wa1 = smem + P2_WO + kq * WOQ + (4 * hh + q4) * 128 + blk * 32 + p4 * 8 + (64 ^ sw);
  STAMP(13);
  const u64 mct = 0xffffull << (16 * ct & 63), mrt = 0xfull << (4 * rt & 63);
  const u64 ma = (ct < 4 ? mct : 0ull) | (rt < 16 ? mrt : 0ull), mb = (ct < 4 ? 0ull : mct) | (rt < 16 ? 0ull : mrt);
  if (w == 1) { const u32 ok = (__ballot(fl.a != tag) & ma) == 0 && (__ballot(fl.b != tag) & mb) == 0; if (lane == 0) ST(u32, smem + P2_OK) = ok; }
  LBAR();
  LOAD_PQ(3, 8);
#undef LOAD_PQ
  const bool early = __builtin_amdgcn_readfirstlane(LD(u32, smem + P2_OK)) != 0;
  u32x4 btr; float gt;
#define LOAD_B() do { \
    btr = __builtin_amdgcn_raw_buffer_load_b128(rws, (lane >> 2) * 4096 + (lane & 3) * 16, (int)WS_BT + (64 * ct + 16 * kq) * 4096 + 128 * rt + 64 * th, 16); \
    gt = u2f(__builtin_amdgcn_raw_buffer_load_b32(rws, l31 * 4, (int)WS_G + (64 * rt + 32 * th) * 4, 16)); } while (0)
  if (early) LOAD_B();
  u32x2 bb[2];
  lptr ba = smem + P2_BT + w * 1024 + (4 * hh + q4) * 64 + blk * 32 + p4 * 8;
#define TURN_B() do { ST(u32x4, smem + P2_BT + w * 1024 + lane * 16) = btr;        \
    bb[0] = __builtin_bit_cast(u32x2, tr_read(ba)); bb[1] = __builtin_bit_cast(u32x2, tr_read(ba + 512)); } while (0)
  STAMP(14);
  {
    f32x16 d[2]; s16x8 kf[8], wf[2][2]; u32x4 of[2];
#define OMM(i) do { d[(i) & 1] = MFMA32(kf[i], __builtin_bit_cast(s16x8, pf[i]), cinit); asm volatile("" : "+v"(d[(i) & 1])); } while (0)
#define NORM(i) do { const f32x16& dd = d[(i) & 1]; const float inv = __builtin_amdgcn_rcpf(dd[8]); \
      of[(i) & 1] = (u32x4){pk_bf16(dd[0] * inv, dd[1] * inv), pk_bf16(dd[2] * inv, dd[3] * inv), pk_bf16(dd[4] * inv, dd[5] * inv), pk_bf16(dd[6] * inv, dd[7] * inv)}; \
      asm volatile("" : "+v"(of[(i) & 1]) : "v"(dd)); } while (0)
#define LDW(i) do { wf[(i) & 1][0] = cat8(tr_read(wa0 + (i) * 2048), tr_read(wa0 + (i) * 2048 + 1024)); \
      wf[(i) & 1][1] = cat8(tr_read(wa1 + (i) * 2048), tr_read(wa1 + (i) * 2048 + 1024)); } while (0)
#define AMM(i) do { acc[0] = MFMA32(wf[(i) & 1][0], __builtin_bit_cast(s16x8, of[(i) & 1]), acc[0]); \
      acc[1] = MFMA32(wf[(i) & 1][1], __builtin_bit_cast(s16x8, of[(i) & 1]), acc[1]); asm volatile("" : "+v"(acc[0]), "+v"(acc[1])); } while (0)
#pragma unroll
    for (int i = 0; i < 8; ++i) kf[i] = LD(s16x8, kva + i * 544);
    LDW(0);
    SCHED();
    OMM(0); OMM(1); SCHED(); NORM(0); SCHED();
#pragma unroll
    for (int i = 0; i < 8; ++i) {
      if (i + 1 < 8) LDW(i + 1);
      SCHED();
      AMM(i);
      if (i + 2 < 8) OMM(i + 2);
      SCHED();
      if (i + 1 < 8) NORM(i + 1);
      SCHED();
      if (i == TURN_AT && early) { TURN_B(); SCHED(); }
    }
#undef OMM
#undef NORM
#undef LDW
#undef AMM
  }
  STAMP(15);
  f32x4 own[2];
#pragma unroll
  for (int q = 0; q < 4; ++q) {
    lptr ex = smem + P2_EX + ((2 * q + th) * 4 + kq) * 2048 + lane * 16;
    const f32x16& s = acc[q >> 1];
    const f32x4 s0 = {s[8 * (q & 1)], s[8 * (q & 1) + 1], s[8 * (q & 1) + 2], s[8 * (q & 1) + 3]};
    const f32x4 s1 = {s[8 * (q & 1) + 4], s[8 * (q & 1) + 5], s[8 * (q & 1) + 6], s[8 * (q & 1) + 7]};
    if (q == kq) { own[0] = s0; own[1] = s1; } else { ST(f32x4, ex) = s0; ST(f32x4, ex + 1024) = s1; }
  }
  if (!early && w == 1) poll_wait(ws, foff + 1024, lane, tag, fl, ma, mb);
  LBAR();
  if (!early) { LOAD_B(); TURN_B(); }
#undef TURN_B
#undef LOAD_B
  STAMP(16);
  lptr ei = smem + P2_EX + w * 8192 + lane * 16;
  const float g1 = rbf(1.f - gt);
  u32x2 res[2];
#pragma unroll
  for (int i = 0; i < 2; ++i) {
    const f32x4 o = (own[i] + LD(f32x4, ei + ((kq ^ 1) * 2048 + 1024 * i))) + (LD(f32x4, ei + ((kq ^ 2) * 2048 + 1024 * i)) + LD(f32x4, ei + ((kq ^ 3) * 2048 + 1024 * i)));
    res[i] = (u32x2){pk_bf16(gt * o[0] + g1 * bf_lo(bb[i].x), gt * o[1] + g1 * bf_hi(bb[i].x)), pk_bf16(gt * o[2] + g1 * bf_lo(bb[i].y), gt * o[3] + g1 * bf_hi(bb[i].y))};
  }
  const auto sx = __builtin_amdgcn_permlane32_swap(res[0].x, res[1].x, false, false);
  const auto sy = __builtin_amdgcn_permlane32_swap(res[0].y, res[1].y, false, false);
  *(u32x4*)(out + (size_t)(64 * rt + 32 * th + l31) * DM + 64 * ct + 16 * kq + 8 * hh) = (u32x4){sx[0], sy[0], sx[1], sy[1]};
  STAMP(17);
  if (blockIdx.x == 0 && w == 1) {
    poll_wait(ws, foff + 1024, lane, tag, fl, ~0ull, ~0ull);
    if (lane == 0) __hip_atomic_store((gu64*)(ws + WS_SYNC), tag, RLX_AGENT);
  }
}

__global__ __launch_bounds__(512, 2) void fused(const u16* __restrict__ x, const u16* __restrict__ wqkv, const u16* __restrict__ wo,
                                                const u16* __restrict__ lar, const u16* __restrict__ aim, const u16* __restrict__ cre,
                                                const u16* __restrict__ cim, const u16* __restrict__ ldt, const u16* __restrict__ dsk,
                                                const u16* __restrict__ gw, char* __restrict__ ws, u16* __restrict__ out) {
  extern __shared__ __attribute__((aligned(16))) char smem_[];
  lptr smem = (lptr)smem_;
  STAMP_DECL; STAMP(0);
  asm volatile("" :: "s"(x), "s"(wqkv), "s"(wo), "s"(lar), "s"(aim), "s"(cre), "s"(cim), "s"(ldt), "s"(dsk), "s"(gw), "s"(ws), "s"(out));
  STAMP(18);
  const int b = blockIdx.x, xcd = b & 7, slot = b >> 3, idx = slot >> 1;
  const int rt = 8 * (xcd >> 1) + (slot & 7), ct = 4 * (xcd & 1) + (slot >> 3);
  const int tt = 4 * (xcd >> 1) + (idx & 3), hg = 4 * (xcd & 1) + (idx >> 2);
  const int cg = xcd * 16 + idx;
  int pofs = (threadIdx.x & 63) * 8;
  asm volatile("" : "+v"(pofs));
  u64 epoch; u32 tmp0, tmp1, tmp2; u64 pc;
  u32 pr[5]; u32x2 xr[8];
  asm volatile(
      "s_bitcmp1_b32 %[b], 3\n\t"
      "s_cbranch_scc0 .Lpre_end%=\n\t"
      "s_cmp_gt_u32 %[wv], 3\n\t"
      "s_cbranch_scc1 .Lpre_b%=\n\t"
      "v_lshlrev_b32 %[p4], 2, %[tid]\n\t"
      "v_lshl_add_u32 %[p4], %[cg], 10, %[p4]\n\t"
      "global_load_dword %[p0], %[p4], %[lar]\n\t"
      "global_load_dword %[p1], %[p4], %[aim]\n\t"
      "global_load_dword %[p2], %[p4], %[cre]\n\t"
      "global_load_dword %[p3], %[p4], %[cim]\n\t"
      "v_lshrrev_b32 %[t0], 7, %[p4]\n\t"
      "v_and_b32 %[t0], 0x3fe, %[t0]\n\t"
      "global_load_ushort %[p4], %[t0], %[ldt]\n\t"
      "s_barrier\n\t"
      "s_branch .Lpre_end%=\n"
      ".Lpre_b%=:\n\t"
      "s_getpc_b64 %[pc]\n\t"
      "v_and_b32 %[t0], 0xff, %[tid]\n\t"
      "v_lshlrev_b32 %[t0], 7, %[t0]\n\t"
      "v_mov_b32 %[t1], .Lfused_code_tail-.Lpre_b%=-4\n\t"
      "v_med3_i32 %[t0], 0, %[t0], %[t1]\n\t"
      "global_load_dword %[t2], %[t0], %[pc]\n\t"
      "v_lshl_add_u32 %[t0], %[cg], 4, %[tid]\n\t"
      "v_and_b32 %[t0], 0xff, %[t0]\n\t"
      "v_lshlrev_b32 %[t0], 12, %[t0]\n\t"
      "v_lshl_add_u32 %[t0], %[cg], 3, %[t0]\n\t"
      "v_add_u32 %[t1], 0x100000, %[t0]\n\t"
      "s_barrier\n\t"
      "global_load_dwordx2 %[x0], %[t0], %[x]\n\t"
      "global_load_dwordx2 %[x1], %[t0], %[x] offset:1024\n\t"
      "global_load_dwordx2 %[x2], %[t0], %[x] offset:2048\n\t"
      "global_load_dwordx2 %[x3], %[t0], %[x] offset:3072\n\t"
      "global_load_dwordx2 %[x4], %[t1], %[x]\n\t"
      "global_load_dwordx2 %[x5], %[t1], %[x] offset:1024\n\t"
      "global_load_dwordx2 %[x6], %[t1], %[x] offset:2048\n\t"
      "global_load_dwordx2 %[x7], %[t1], %[x] offset:3072\n"
      ".Lpre_end%=:"
      : [p0] "=&v"(pr[0]), [p1] "=&v"(pr[1]), [p2] "=&v"(pr[2]), [p3] "=&v"(pr[3]), [p4] "=&v"(pr[4]),
        [x0] "=&v"(xr[0]), [x1] "=&v"(xr[1]), [x2] "=&v"(xr[2]), [x3] "=&v"(xr[3]), [x4] "=&v"(xr[4]), [x5] "=&v"(xr[5]), [x6] "=&v"(xr[6]), [x7] "=&v"(xr[7]),
        [t0] "=&v"(tmp0), [t1] "=&v"(tmp1), [t2] "=&v"(tmp2), [pc] "=&s"(pc)
      : [b] "s"(b), [wv] "s"(__builtin_amdgcn_readfirstlane(threadIdx.x >> 6)), [tid] "v"(threadIdx.x), [cg] "s"(cg),
        [lar] "s"(lar), [aim] "s"(aim), [cre] "s"(cre), [cim] "s"(cim), [ldt] "s"(ldt), [x] "s"(x)
      : "scc", "memory");
  if (__builtin_expect(slot & 1, 0)) conv_role(x, pr, xr, tmp2, dsk, gw, wo, ws, out, rt, cg, ct, epoch, smem STAMP_PASS);
  else gemm_role(x, wqkv, wo, ct, ws, out, rt, tt, hg, epoch, smem STAMP_PASS);
  const int lane = threadIdx.x & 63, w = __builtin_amdgcn_readfirstlane(threadIdx.x >> 6);
  const u64 tag = next_tag(epoch);
  Tags fl = {0, 0};
  if (w == 0) {
    const rsrc_t rf = mk_rsrc(ws, WS_USED);
    fl.a = __builtin_bit_cast(u64, __builtin_amdgcn_raw_buffer_load_b64(rf, pofs, (int)WS_FLAG + (b & 31) * 2048, 16));
    fl.b = __builtin_bit_cast(u64, __builtin_amdgcn_raw_buffer_load_b64(rf, pofs, (int)WS_FLAG + (b & 31) * 2048 + 512, 16));
  }
  SCHED();
  WAIT_VM(0);
  __builtin_amdgcn_s_setprio(0);
  STAMP(10);
  LBAR();
  if (w == 7 && lane < 32) __hip_atomic_store((gu64*)(ws + WS_FLAG + lane * 2048) + ((slot & 1) ? 128 + cg : tt * 8 + hg), tag, RLX_AGENT);
  out_role(ws, out, rt, ct, tag, fl, smem STAMP_PASS);
  STAMP_FLUSH(ws);
}

extern "C" void kernel_launch(void* const* d_in, const int* in_sizes, int n_in, void* d_out, int out_size, void* d_ws,
                              size_t ws_size, hipStream_t stream) {
  (void)in_sizes; (void)n_in; (void)out_size; (void)ws_size;
  (void)hipFuncSetAttribute((const void*)fused, hipFuncAttributeMaxDynamicSharedMemorySize, 163840);
  fused<<<256, 512, 163840, stream>>>((const u16*)d_in[0], (const u16*)d_in[1], (const u16*)d_in[2], (const u16*)d_in[4],
                                      (const u16*)d_in[5], (const u16*)d_in[6], (const u16*)d_in[7], (const u16*)d_in[8],
                                      (const u16*)d_in[9], (const u16*)d_in[10], (char*)d_ws, (u16*)d_out);
}
```

```cpp
#include <hip/hip_runtime.h>
#include <hip/hip_bf16.h>
#include <stdint.h>

typedef unsigned int u32;
typedef unsigned short u16;
typedef __attribute__((ext_vector_type(8))) short s16x8;
typedef __attribute__((ext_vector_type(4))) short s16x4;
typedef __attribute__((ext_vector_type(8))) _Float16 h16x8;
typedef __attribute__((ext_vector_type(2))) _Float16 h16x2;
typedef __attribute__((ext_vector_type(2))) __bf16 b16x2;
typedef __attribute__((ext_vector_type(2))) float f32x2;
typedef __attribute__((ext_vector_type(4))) float f32x4;
typedef __attribute__((ext_vector_type(16))) float f32x16;
typedef __attribute__((ext_vector_type(2))) u32 u32x2;
typedef __attribute__((ext_vector_type(4))) u32 u32x4;
#define LDS_AS __attribute__((address_space(3)))
#define DI __device__ __forceinline__

static constexpr int SEQ = 2048, DM = 512;
#define KVC 1
static constexpr size_t WS_KV = (6u << 20) + 65536;
static constexpr int KV_CP = 36864;
static constexpr size_t WS_G = 65536;
static constexpr size_t WS_PQ = 1u << 20;
static constexpr size_t WS_BT = (4u << 20) + 4096;
static constexpr int WS_USED = 8 << 20;
#define SYNCO 49152
static constexpr size_t WS_SYNC = SYNCO;
static constexpr size_t WS_FLAG = 131072;
typedef unsigned long long u64;
typedef __attribute__((address_space(1))) u64 gu64;
typedef __attribute__((address_space(1))) u32 gu32;
#define RLX_AGENT __ATOMIC_RELAXED, __HIP_MEMORY_SCOPE_AGENT
__device__ __forceinline__ u64 next_tag(u64 e) { return e * 6364136223846793005ull + 1442695040888963407ull; }

#define STAMP_ARG
#define STAMP_PASS
#define STAMP_DECL do {} while (0)
#define STAMP(i) do {} while (0)
#define CYC(i) do {} while (0)
#define CYCD(i, j) do {} while (0)
#define STAMP_FLUSH(wsp) do {} while (0)
#define MIXM 2
#define TURN_AT 5
#define WOE 4
#define POLL_SLEEP 2
#define ST_AUX 16
#define WAIT_VM(n) asm volatile("s_waitcnt vmcnt(" #n ")" ::: "memory")
#define WAIT_L0() __builtin_amdgcn_s_waitcnt(0xC07F)
#define RAW_BAR() __builtin_amdgcn_s_barrier()
#define SCHED() __builtin_amdgcn_sched_barrier(0)
#define LBAR() do { WAIT_L0(); asm volatile("" ::: "memory"); RAW_BAR(); asm volatile("" ::: "memory"); } while (0)
typedef LDS_AS char* lptr;
#define LD(T, p) (*(const LDS_AS T*)(p))
#define ST(T, p) (*(LDS_AS T*)(p))

DI u32 pk_bf16(float a, float b) { return __builtin_bit_cast(u32, __builtin_convertvector((f32x2){a, b}, b16x2)); }
DI u32 pk_f16(f32x2 v) { return __builtin_bit_cast(u32, __builtin_convertvector(v, h16x2)); }
DI float bf_lo(u32 v) { return __builtin_bit_cast(float, v << 16); }
DI float bf_hi(u32 v) { return __builtin_bit_cast(float, v & 0xffff0000u); }
DI float u2f(u32 v) { return __builtin_bit_cast(float, v); }
DI b16x2 u2b(u32 v) { return __builtin_bit_cast(b16x2, v); }
DI float rbf(float v) { return bf_lo(pk_bf16(v, v)); }
static constexpr float LOG2E = 1.44269504088896341f, EPS = 1e-6f;
DI float phi_f(float t) {
  const float hi = __builtin_bit_cast(float, max(__builtin_bit_cast(int, t), __builtin_bit_cast(int, EPS)));
  return hi + __builtin_amdgcn_fmed3f(__builtin_amdgcn_exp2f(__builtin_fmaf(t, LOG2E, -EPS * LOG2E)), 0.f, 1.f);
}
DI float exp_f(float v) {
  const float p = v * LOG2E;
  const float r = __builtin_fmaf(v, 1.925963033500011e-8f, __builtin_fmaf(v, LOG2E, -p));
  return __builtin_amdgcn_exp2f(p) * __builtin_fmaf(r, 0.6931471805599453f, 1.f);
}
#define DPP_F(v, ctrl, rmask, bc) __builtin_bit_cast(float, __builtin_amdgcn_update_dpp(0, __builtin_bit_cast(int, v), ctrl, rmask, 0xf, bc))
DI float wave_sum(float r) {
  r += DPP_F(r, 0x111, 0xf, true); r += DPP_F(r, 0x112, 0xf, true); r += DPP_F(r, 0x114, 0xf, true); r += DPP_F(r, 0x118, 0xf, true);
  r += DPP_F(r, 0x142, 0xa, false); r += DPP_F(r, 0x143, 0xc, false);
  return r;
}
DI u32 wave_umax(u32 r) {
#define DPP_U(v, ctrl, rmask, bc) (u32)__builtin_amdgcn_update_dpp(0, (int)(v), ctrl, rmask, 0xf, bc)
  r = max(r, DPP_U(r, 0x111, 0xf, true)); r = max(r, DPP_U(r, 0x112, 0xf, true)); r = max(r, DPP_U(r, 0x114, 0xf, true));
  r = max(r, DPP_U(r, 0x118, 0xf, true)); r = max(r, DPP_U(r, 0x142, 0xa, false)); r = max(r, DPP_U(r, 0x143, 0xc, false));
  return (u32)__builtin_amdgcn_readlane((int)r, 63);
}
DI s16x4 tr_read(lptr p) { return __builtin_amdgcn_ds_read_tr16_b64_v4i16((LDS_AS s16x4*)p); }
DI s16x8 cat8(s16x4 a, s16x4 b) { return __builtin_shufflevector(a, b, 0, 1, 2, 3, 4, 5, 6, 7); }
typedef __amdgpu_buffer_rsrc_t rsrc_t;
DI rsrc_t mk_rsrc(const void* p, int bytes) { return __builtin_amdgcn_make_buffer_rsrc((void*)p, 0, bytes, 0x00020000); }
DI void store16(u32x4 v, rsrc_t r, int off) { __builtin_amdgcn_raw_buffer_store_b128(v, r, off, 0, ST_AUX); }
DI void blds16(rsrc_t r, lptr l, int voff, int soff) { __builtin_amdgcn_raw_ptr_buffer_load_lds(r, (LDS_AS void*)l, 16, voff, soff, 0, 0); }
#define CM_MUL(t, a, w) "v_pk_mul_f32 " t ", " a ", " w " op_sel:[1,1] op_sel_hi:[1,0]\n\t"
#define CM_FMA(r, a, w, t) "v_pk_fma_f32 " r ", " a ", " w ", " t " op_sel_hi:[0,1,1] neg_lo:[0,0,1]\n\t"
DI void cmul2(f32x2 (&r)[2], const f32x2 (&a)[2], const f32x2 (&w)[2]) {
  f32x2 r0, r1;
  asm(CM_MUL("%0", "%2", "%4") CM_MUL("%1", "%3", "%5") CM_FMA("%0", "%2", "%4", "%0") CM_FMA("%1", "%3", "%5", "%1")
      : "=&v"(r0), "=&v"(r1) : "v"(a[0]), "v"(a[1]), "v"(w[0]), "v"(w[1]));
  r[0] = r0; r[1] = r1;
}
DI void cmul4(f32x2 (&r)[2], const f32x2 (&a)[2], const f32x2 (&w)[2], f32x2 (&q)[2], const f32x2 (&b)[2], const f32x2 (&v)[2]) {
  f32x2 r0, r1, q0, q1;
  asm(CM_MUL("%0", "%4", "%6") CM_MUL("%1", "%5", "%7") CM_MUL("%2", "%8", "%10") CM_MUL("%3", "%9", "%11")
      CM_FMA("%0", "%4", "%6", "%0") CM_FMA("%1", "%5", "%7", "%1") CM_FMA("%2", "%8", "%10", "%2") CM_FMA("%3", "%9", "%11", "%3")
      : "=&v"(r0), "=&v"(r1), "=&v"(q0), "=&v"(q1)
      : "v"(a[0]), "v"(a[1]), "v"(w[0]), "v"(w[1]), "v"(b[0]), "v"(b[1]), "v"(v[0]), "v"(v[1]));
  r[0] = r0; r[1] = r1; q[0] = q0; q[1] = q1;
}
DI f32x2 cexp_p(float alpha, float theta, float p) {
  float m = __builtin_amdgcn_exp2f(alpha * p);
  float f = __builtin_amdgcn_fractf(theta * p);
  return (f32x2){m * __builtin_amdgcn_cosf(f), m * __builtin_amdgcn_sinf(f)};
}
#define MFMA16(a, b, c) __builtin_amdgcn_mfma_f32_16x16x32_bf16((a), (b), (c), 0, 0, 0)
#define MFMA16H(a, b, c) __builtin_amdgcn_mfma_f32_16x16x32_f16((a), (b), (c), 0, 0, 0)
#define MFMA32(a, b, c) __builtin_amdgcn_mfma_f32_32x32x16_bf16((a), (b), (c), 0, 0, 0)

static constexpr int STAGE = 40960;
static constexpr int WOQ = 17408;
static constexpr int P2_WO = 0, P2_KV = 4 * WOQ, P2_OK = P2_KV + 17408, P2_BT = P2_KV + 18432, P2_EX = P2_BT + 8192;

DI u64 load_epoch(char* ws) {
  int vzero;
  asm volatile("v_mov_b32 %0, 0" : "=v"(vzero));
  return __hip_atomic_load((gu64*)(ws + WS_SYNC + vzero), RLX_AGENT);
}
DI u32 touch_pages(const char* ws, const u16* out, int rt, int lane) {
  const char* p = lane < 4 ? ws + (size_t)((0xf871u >> (4 * lane)) & 15) * (512u << 10) : (const char*)out + (size_t)rt * 65536;
  return lane < 5 ? *(const u32*)p : 0u;
}
DI void fetch_wo(const u16* __restrict__ wo, int ct, int q, int part, int lane, lptr smem) {
  const rsrc_t rwo = mk_rsrc(wo, DM * DM * 2);
  const int sr = lane >> 3, sp = lane & 7, sc = (sp ^ (((sr >> 1) & 1) << 2)) << 4;
#pragma unroll
  for (int i = 4 * part; i < 4 * part + 4; ++i) blds16(rwo, smem + P2_WO + q * WOQ + 1024 * i, sr * 1024 + sc, (128 * q + 8 * i) * 1024 + 128 * ct);
}

DI void gemm_role(const u16* __restrict__ x, const u16* __restrict__ wqkv, const u16* __restrict__ wo, int ct, char* __restrict__ ws, const u16* out, int rt, int tt, int hg,
                  u64& epoch, lptr smem STAMP_ARG) {
  const int tid = threadIdx.x, lane = tid & 63;
  const int w = __builtin_amdgcn_readfirstlane(tid >> 6);
  const int wm = w >> 2, wh = w & 3, g = lane >> 4, l15 = lane & 15;
  const int sr = lane >> 3, sp = lane & 7;
  const rsrc_t rx = mk_rsrc(x, SEQ * DM * 2), rw = mk_rsrc(wqkv, DM * 1536 * 2);
  const int xv = sr * 1024 + ((sp ^ sr) << 4), xs0 = (tt * 128 + 16 * w) * 1024;
  const int fw = ((sr >> 1) & 1) | ((w & 1) << 1);
  const int wv = sr * 3072 + ((sp ^ (fw << 1)) << 4), ws0 = 8 * w * 3072 + hg * 128;
  const int ROT = 2 * (tt & 3) + ((tt >> 2) & 1);
  const rsrc_t rwo = mk_rsrc(wo, DM * DM * 2);
  const int ov = sr * 1024 + ((sp ^ (((sr >> 1) & 1) << 2)) << 4);
#define ISS(s, i) do { lptr sb = smem + ((s) & 3) * STAGE; \
    if ((s) >= 8) { if ((i) < 4) blds16(rwo, smem + P2_WO + (w >> 1) * WOQ + (64 * (w & 1) + 32 * ((s) - 8) + 8 * (i)) * 128, ov, (64 * w + 32 * ((s) - 8) + 8 * (i)) * 1024 + 128 * ct); } \
    else if ((i) < 2) blds16(rx, sb + w * 2048 + (i) * 1024, xv, xs0 + (((s) + ROT) & 7) * 128 + (i) * 8192); \
    else blds16(rw, sb + 16384 + ((i) - 2) * 8192 + w * 1024, wv, ws0 + (((s) + ROT) & 7) * 64 * 3072 + ((i) - 2) * 1024); } while (0)
#define ISSUE(s) do { ISS(s, 0); ISS(s, 1); ISS(s, 2); ISS(s, 3); ISS(s, 4); } while (0)
  gu64* sync_z = (gu64*)(ws + WS_SYNC + 128 + hg * 128);
  ISSUE(0);
  STAMP(19);
  SCHED(); RAW_BAR(); SCHED();
  u32 pv2 = 0;
  SCHED();
  ISSUE(1);
  SCHED();
  epoch = load_epoch(ws);
  const rsrc_t rws = mk_rsrc(ws, WS_USED);
  const bool clear = tt == 0 && w == 7;
  if (clear) {
    const u32x4 z4 = {0u, 0u, 0u, 0u};
#pragma unroll
    for (int i = 0; i < 4; ++i) store16(z4, rws, lane * 16 + (int)WS_KV + hg * 4352 + 1024 * i);
    store16(z4, rws, (lane & 15) * 16 + (int)WS_KV + hg * 4352 + 4096);
  }
  SCHED();
  const int xo = (64 * wm + l15) * 128 + ((g ^ (lane & 7)) << 4);
  const int q4 = l15 >> 2, p4 = lane & 3;
  const int wo_ = 16384 + (8 * g + q4) * 128 + ((wh ^ (((q4 >> 1) & 1) | ((g & 1) << 1))) << 5) + 8 * p4;
  f32x4 aq[4], ak[4], av[4];
#pragma unroll
  for (int i = 0; i < 4; ++i) { aq[i] = (f32x4){EPS, EPS, EPS, EPS}; ak[i] = aq[i]; av[i] = (f32x4){0, 0, 0, 0}; }
  s16x8 xa[4], wa[3], xb[4], wb[3];
#define LDWF(wf, s, ks, st) wf[st] = cat8(tr_read(smem + ((s) & 3) * STAGE + wo_ + (st) * 8192 + (ks) * 4096), tr_read(smem + ((s) & 3) * STAGE + wo_ + (st) * 8192 + (ks) * 4096 + 512))
#define LDXF(xf, s, ks, mt) xf[mt] = LD(s16x8, smem + ((s) & 3) * STAGE + ((xo ^ ((ks) * 64)) + (mt) * 2048))
#define LOADF(xf, wf, s, ks) do { LDWF(wf, s, ks, 0); LDWF(wf, s, ks, 1); LDWF(wf, s, ks, 2); \
    LDXF(xf, s, ks, 0); LDXF(xf, s, ks, 1); LDXF(xf, s, ks, 2); LDXF(xf, s, ks, 3); } while (0)
#define MM3(xf, wf, mt) do { aq[mt] = MFMA16(wf[0], xf[mt], aq[mt]); ak[mt] = MFMA16(xf[mt], wf[1], ak[mt]); av[mt] = MFMA16(xf[mt], wf[2], av[mt]); \
    asm volatile("" : "+v"(aq[mt]), "+v"(ak[mt]), "+v"(av[mt])); } while (0)
#define HALF(xf, wf, xn, wn, ls, lk, is) do { SCHED(); MM3(xf, wf, 0); SCHED(); \
    if ((ls) >= 0) { LDWF(wn, ls, lk, 0); LDXF(xn, ls, lk, 0); LDWF(wn, ls, lk, 1); } if ((is) >= 0) ISS(is, 0); SCHED(); MM3(xf, wf, 1); SCHED(); \
    if ((ls) >= 0) { LDWF(wn, ls, lk, 2); LDXF(xn, ls, lk, 1); LDXF(xn, ls, lk, 2); } if ((is) >= 0) ISS(is, 1); SCHED(); MM3(xf, wf, 2); SCHED(); \
    if ((ls) >= 0) LDXF(xn, ls, lk, 3); if ((is) >= 0) { ISS(is, 2); ISS(is, 3); } SCHED(); MM3(xf, wf, 3); SCHED(); \
    if ((is) >= 0) ISS(is, 4); SCHED(); } while (0)
#define STEP(s, vm, i1, i2) do { WAIT_L0(); HALF(xa, wa, xb, wb, s, 1, i1); \
    WAIT_L0(); WAIT_VM(vm); RAW_BAR(); HALF(xb, wb, xa, wa, (s) + 1, 0, i2); } while (0)
  STAMP(1);
  if (clear) WAIT_VM(11); else WAIT_VM(6);
  RAW_BAR();
  STAMP(2);
  LOADF(xa, wa, 0, 0);
  STEP(0, 5, 2, -1);
  if (clear && lane == 0) __hip_atomic_store(sync_z, next_tag(epoch), RLX_AGENT);
  STAMP(3);
  STEP(1, 5, 3, -1);
  STEP(2, 5, 4, 5);
  STEP(3, 5, -1, 6);
  STAMP(4); CYC(20);
  if (w == 0) pv2 = touch_pages(ws, out, rt, lane);
  STEP(4, 5, -1, 7);
  u64 ready = __hip_atomic_load(sync_z, RLX_AGENT);
  STEP(5, 5, -1, 8);
  if (w < 4) __builtin_amdgcn_s_setprio(2);
  STEP(6, 8, 9, -1);
  STAMP(5); CYC(21); CYCD(20, 21); CYC(21);
  {
    const u64 tag = next_tag(epoch);
#pragma unroll 1
    for (int spin = 0; ready != tag && spin < (1 << 22); ++spin) {
      __builtin_amdgcn_s_sleep(2); ready = __hip_atomic_load(sync_z, RLX_AGENT); __builtin_amdgcn_s_waitcnt(0x0F70);
    }
  }
  WAIT_L0(); HALF(xa, wa, xb, wb, 7, 1, -1); WAIT_L0(); SCHED();
#pragma unroll
  for (int mt = 0; mt < 4; ++mt) aq[mt] = MFMA16(wb[0], xb[mt], aq[mt]);
  asm volatile("" : "+v"(aq[0]), "+v"(aq[1]), "+v"(aq[2]), "+v"(aq[3]));
  SCHED();
#pragma unroll
  for (int mt = 0; mt < 4; ++mt) { ak[mt] = MFMA16(xb[mt], wb[1], ak[mt]); av[mt] = MFMA16(xb[mt], wb[2], av[mt]); }
  asm volatile("" : "+v"(ak[0]), "+v"(ak[1]), "+v"(ak[2]), "+v"(ak[3]), "+v"(av[0]), "+v"(av[1]), "+v"(av[2]), "+v"(av[3]));
  SCHED();
  asm volatile("" :: "v"(pv2));
  STAMP(6);
#undef ISS
#undef ISSUE
#undef LOADF
#undef MM3
#undef HALF
#undef LDWF
#undef LDXF
#undef STEP
  const int h = 4 * hg + wh;
  const int pqv = (g >> 1) * 512 + l15 * 16 + (g & 1) * 256, pqs = (int)WS_PQ + ((tt * 4 + 2 * wm) * 32 + h) * 1024;
#pragma unroll
  for (int pr = 0; pr < 2; ++pr) {
    const f32x4 q0 = aq[2 * pr], q1 = aq[2 * pr + 1];
    const auto s0 = __builtin_amdgcn_permlane16_swap(pk_bf16(phi_f(q0[0]), phi_f(q0[1])), pk_bf16(phi_f(q1[0]), phi_f(q1[1])), false, false);
    const auto s1 = __builtin_amdgcn_permlane16_swap(pk_bf16(phi_f(q0[2]), phi_f(q0[3])), pk_bf16(phi_f(q1[2]), phi_f(q1[3])), false, false);
    store16((u32x4){s0[0], s1[0], s0[1], s1[1]}, rws, pqv + pqs + pr * 32768);
  }
  SCHED();
  STAMP(7);
  f32x4 kvt = {0, 0, 0, 0}, zz = {0, 0, 0, 0};
  const u32x4 ones_u = {0x3f803f80u, 0x3f803f80u, 0x3f803f80u, 0x3f803f80u};
#pragma unroll
  for (int pr = 0; pr < 2; ++pr) {
    u32x4 pk, vv;
#pragma unroll
    for (int m = 0; m < 2; ++m) {
      const f32x4 kk = ak[2 * pr + m], v4 = av[2 * pr + m];
      pk[2 * m] = pk_bf16(phi_f(kk[0]), phi_f(kk[1])); pk[2 * m + 1] = pk_bf16(phi_f(kk[2]), phi_f(kk[3]));
      vv[2 * m] = pk_bf16(v4[0], v4[1]); vv[2 * m + 1] = pk_bf16(v4[2], v4[3]);
    }
    kvt = MFMA16(__builtin_bit_cast(s16x8, vv), __builtin_bit_cast(s16x8, pk), kvt);
    zz = MFMA16(__builtin_bit_cast(s16x8, ones_u), __builtin_bit_cast(s16x8, pk), zz);
  }
  lptr red = smem + 4 * WOQ;
#pragma unroll
  for (int r = 0; r < 4; ++r) ST(float, red + (wm * 4 + wh) * 1088 + l15 * 4 + (4 * g + r) * 64) = kvt[r];
  if (g == 0) ST(float, red + (wm * 4 + wh) * 1088 + l15 * 4 + 1024) = zz[0];
  LBAR();
  STAMP(8);
  float* kvacc = (float*)(ws + WS_KV + (tt % KVC) * KV_CP) + hg * 1088 + tid;
  atomicAdd(kvacc, LD(float, red + tid * 4) + LD(float, red + 4352 + tid * 4));
  atomicAdd(kvacc + 512, LD(float, red + 2048 + tid * 4) + LD(float, red + 4352 + 2048 + tid * 4));
  if (tid < 64) atomicAdd(kvacc + 1024, LD(float, red + 4096 + tid * 4) + LD(float, red + 4352 + 4096 + tid * 4));
  STAMP(9); CYC(22); CYCD(21, 22);
}

static constexpr int TB_ROW = 544, TB_BUF = 16 * TB_ROW, TB_CH = 2 * TB_BUF;
static constexpr int KR_OFF = 4 * TB_CH, KR_CP = 4160, KR_CH = 2 * KR_CP;
static constexpr int U_OFF = KR_OFF + 4 * KR_CH, U_CH = (2048 + 240) * 2;
static constexpr int EX_OFF = U_OFF + 4 * U_CH;
static_assert(EX_OFF + 32768 <= 163840 && TB_CH == WOQ && P2_EX + 65536 <= 163840, "LDS map");

DI void conv_role(const u16* __restrict__ x, u32 (&pr)[5], u32x2 (&xr)[8], u32 pf, const u16* __restrict__ dsk, const u16* __restrict__ gw,
                  const u16* __restrict__ wo, char* __restrict__ ws, const u16* out, int rt, int cg, int ct, u64& epoch, lptr smem STAMP_ARG) {
  const int tid = threadIdx.x, lane = tid & 63;
  const int w = __builtin_amdgcn_readfirstlane(tid >> 6);
  const int ch = w & 3, half = w >> 2, g = lane >> 4, l15 = lane & 15;
  const int c = 4 * cg + ch;
  const rsrc_t rx = mk_rsrc(x, SEQ * DM * 2);
  u32 dsk2, pv = 0;
  u32x4 gx[4], gwv;
  const rsrc_t rws = mk_rsrc(ws, WS_USED);
  f32x4 acc[8];
#pragma unroll
  for (int i = 0; i < 8; ++i) acc[i] = (f32x4){0, 0, 0, 0};
#define PINC() do { asm volatile("" : "+v"(acc[0]), "+v"(acc[1]), "+v"(acc[2]), "+v"(acc[3]), "+v"(acc[4]), "+v"(acc[5]), "+v"(acc[6]), "+v"(acc[7])); SCHED(); } while (0)
  if (w < 4) {
    const int cw = 4 * cg + w;
    if (lane < 8) ST(u32x4, smem + KR_OFF + w * KR_CH + (lane >> 2) * KR_CP + 4096 + 16 * (lane & 3)) = (u32x4){0u, 0u, 0u, 0u};
    __builtin_amdgcn_s_setprio(2);
    asm volatile("s_waitcnt vmcnt(0)" : "+v"(pr[0]), "+v"(pr[1]), "+v"(pr[2]), "+v"(pr[3]), "+v"(pr[4]));
    const u32 lar2 = pr[0], aim2 = pr[1], cre2 = pr[2], cim2 = pr[3], ldt_u = pr[4];
    const float dt = rbf(exp_f(bf_lo(ldt_u)));
    float al[2], th[2];
    al[0] = -rbf(exp_f(bf_lo(lar2))) * dt * LOG2E; al[1] = -rbf(exp_f(bf_hi(lar2))) * dt * LOG2E;
    th[0] = bf_lo(aim2) * dt * 0.15915494309189535f; th[1] = bf_hi(aim2) * dt * 0.15915494309189535f;
    STAMP(1);
    u32 ce = wave_umax(max(max(cre2 & 0x7f80u, (cre2 >> 16) & 0x7f80u), max(cim2 & 0x7f80u, (cim2 >> 16) & 0x7f80u))) >> 7;
    ce = ce < 1 ? 1 : (ce > 253 ? 253 : ce);
    const float cinv = __builtin_bit_cast(float, (254u - ce) << 23);
    const float kscale = 2.f * dt * __builtin_bit_cast(float, ce << 23);
    lptr tb = smem + w * TB_CH + lane * 8, tr = smem + w * TB_CH + 16 * g, kr = smem + KR_OFF + w * KR_CH;
#define PUT(buf, e, v) ST(u32x2, tb + (buf) * TB_BUF + (e) * TB_ROW) = (u32x2){pk_f16(v[0]), pk_f16(v[1])}
#define CONJ(v) ((f32x2){(v).x, -(v).y})
#define GETF(f, buf, row) do { lptr p_ = tr + (buf) * TB_BUF + (row) * TB_ROW; \
      _Pragma("unroll") for (int ks = 0; ks < 8; ++ks) f[ks] = LD(h16x8, p_ + 64 * ks); } while (0)
#define TILE(d, f) do { d = (f32x4){0, 0, 0, 0}; _Pragma("unroll") for (int ks = 0; ks < 8; ++ks) d = MFMA16H(f[ks], fx[ks], d); } while (0)
    f32x2 y[17][2], wB[2], w16[2], w32[2], xa[2], xb[2], aS[6][2], t[2], t2[2];
#pragma unroll
    for (int m = 0; m < 2; ++m) {
      y[1][m] = CONJ(cexp_p(al[m], th[m], 1.f)); w16[m] = cexp_p(al[m], th[m], 16.f); wB[m] = CONJ(cexp_p(al[m], th[m], 256.f));
      y[16][m] = CONJ(w16[m]);
    }
    xa[0] = (f32x2){bf_lo(cre2) * cinv, bf_lo(cim2) * cinv}; xa[1] = (f32x2){bf_hi(cre2) * cinv, bf_hi(cim2) * cinv};
    ST(u32x2, tb + TB_BUF) = (u32x2){0x3c00u, 0x3c00u};
    PUT(0, 0, xa); PUT(1, 1, y[1]);
    cmul4(xb, xa, w16, w32, w16, w16); cmul4(y[2], y[1], y[1], aS[0], wB, wB);
    PUT(0, 1, xb); PUT(1, 2, y[2]);
#pragma unroll
    for (int i = 1; i < 8; ++i) {
      cmul4(xa, xa, w32, xb, xb, w32);
      if (i < 7) cmul4(y[2 * i + 1], y[2 * i - 1], y[2], y[2 * i + 2], y[2 * i], y[2]); else cmul2(y[15], y[13], y[2]);
      PUT(0, 2 * i, xa); PUT(0, 2 * i + 1, xb); PUT(1, 2 * i + 1, y[2 * i + 1]);
      if (i < 7) PUT(1, 2 * i + 2, y[2 * i + 2]);
    }
    STAMP(2);
    h16x8 fx[8], fa[8], fb[8];
    f32x4 kd;
    const int gq = l15 >> 2, r3 = min(l15 & 3, 2), yk = 2044 - 16 * l15 - 4 * g;
    SCHED();
    GETF(fx, 0, l15); GETF(fa, 1, l15);
    SCHED();
    PUT(0, 0, wB);
#pragma unroll
    for (int m = 1; m < 9; m += 2) {
      cmul4(t, wB, y[2 * m], t2, wB, y[2 * m + 2]);
      if (m == 1) cmul4(aS[1], aS[0], wB, aS[2], aS[0], aS[0]);
      if (m == 3) cmul4(aS[3], aS[2], wB, aS[4], aS[2], aS[0]);
      if (m == 5) cmul2(aS[5], aS[4], wB);
      PUT(0, m, t); PUT(0, m + 1, t2);
    }
    SCHED();
    GETF(fb, 0, (int)((0x8766654443222100ull >> (4 * l15)) & 15));
    TILE(kd, fa);
    SCHED();
#pragma unroll
    for (int s = 0; s < 2; ++s) {
      PUT(1, 5 * s, aS[s]);
#pragma unroll
      for (int m = 1; m < 5; m += 2) { cmul4(t, aS[s], y[4 * m], t2, aS[s], y[4 * m + 4]); PUT(1, 5 * s + m, t); PUT(1, 5 * s + m + 1, t2); }
    }
    {
      const f32x4 k0 = kd * kscale;
      const u32 p32 = pk_bf16(k0[3], k0[2]), p10 = pk_bf16(k0[1], k0[0]);
      ST(u32x2, kr + 2 * yk) = (u32x2){p32, p10};
      ST(u16, kr + KR_CP + 2 * yk + 2) = (u16)p32;
      ST(u32, kr + KR_CP + 2 * yk + 4) = pk_bf16(k0[2], k0[1]);
      ST(u16, kr + KR_CP + 2 * yk + 8) = (u16)(p10 >> 16);
    }
    SCHED();
    GETF(fa, 1, 5 * (gq >> 1) + 2 * (gq & 1) + r3);
    TILE(kd, fb);
    SCHED();
#pragma unroll
    for (int s = 2; s < 6; ++s) {
      cmul4(t, aS[s], y[8], t2, aS[s], y[16]);
      PUT(0, 3 * (s - 2), aS[s]); PUT(0, 3 * (s - 2) + 1, t); PUT(0, 3 * (s - 2) + 2, t2);
    }
    {
      const f32x4 k1 = kd * kscale;
      const u32 q10 = pk_bf16(k1[1], k1[0]);
      ST(u32x2, kr + 2 * (yk - 256)) = (u32x2){pk_bf16(k1[3], k1[2]), q10};
      ST(u32x2, kr + KR_CP + 2 * (yk - 256)) = (u32x2){pk_bf16(k1[3], k1[3]), pk_bf16(k1[2], k1[1])};
      if (lane == 0) ST(u16, kr + KR_CP + 2 * 1792) = (u16)q10;
    }
    SCHED();
    GETF(fb, 0, 3 * gq + r3);
    TILE(kd, fa);
    STAMP(3);
    {
      const f32x4 k0 = kd * kscale;
      const int y2 = 2040 - (512 + 256 * (g >> 1) + 16 * l15 + 8 * (g & 1));
      const u32 b2 = pk_bf16(k0[2], k0[2]), b1 = pk_bf16(k0[1], k0[1]), b0 = pk_bf16(k0[0], k0[0]);
      ST(u32x4, kr + 2 * y2) = (u32x4){b2, b1, pk_bf16(k0[1], k0[0]), b0};
      ST(u32x4, kr + KR_CP + 2 * y2) = (u32x4){b2, pk_bf16(k0[2], k0[1]), b1, b0};
    }
    TILE(kd, fb);
    {
      const f32x4 k1 = kd * kscale;
      const int y3 = 2032 - (1024 + 256 * g + 16 * l15);
      const u32 a2 = pk_bf16(k1[2], k1[2]), a1 = pk_bf16(k1[1], k1[1]), a0 = pk_bf16(k1[0], k1[0]);
      ST(u32x4, kr + 2 * y3) = (u32x4){a2, a2, a1, a1};
      ST(u32x4, kr + 2 * y3 + 16) = (u32x4){a1, pk_bf16(k1[1], k1[0]), a0, a0};
      ST(u32x4, kr + KR_CP + 2 * y3) = (u32x4){a2, a2, pk_bf16(k1[2], k1[1]), a1};
      ST(u32x4, kr + KR_CP + 2 * y3 + 16) = (u32x4){a1, a1, a0, a0};
    }
#undef PUT
#undef GETF
#undef TILE
    SCHED();
    __builtin_amdgcn_s_setprio(0);
    WAIT_L0(); SCHED();
#pragma unroll
    for (int j = 0; j < WOE; ++j) fetch_wo(wo, ct, ch, j, lane, smem);
    STAMP(4);
  } else {
    const int t2 = (tid + 16 * cg) & 255;
    if (lane < 30) ST(u32x4, smem + U_OFF + ch * U_CH + lane * 16) = (u32x4){0u, 0u, 0u, 0u};
    STAMP(1);
#pragma unroll
    for (int hf = 0; hf < 2; ++hf) {
      lptr ub = smem + U_OFF + 480 + t2 * 8 + hf * 2048;
      if (hf == 0) asm volatile("s_waitcnt vmcnt(4)" : "+v"(xr[0]), "+v"(xr[1]), "+v"(xr[2]), "+v"(xr[3]) : "v"(pf));
      else asm volatile("s_waitcnt vmcnt(0)" : "+v"(xr[4]), "+v"(xr[5]), "+v"(xr[6]), "+v"(xr[7]));
      const u32x2 x0 = xr[4 * hf], x1 = xr[4 * hf + 1], x2 = xr[4 * hf + 2], x3 = xr[4 * hf + 3];
      ST(u32x2, ub + 0 * U_CH) = (u32x2){__builtin_amdgcn_perm(x1.x, x0.x, 0x05040100u), __builtin_amdgcn_perm(x3.x, x2.x, 0x05040100u)};
      ST(u32x2, ub + 1 * U_CH) = (u32x2){__builtin_amdgcn_perm(x1.x, x0.x, 0x07060302u), __builtin_amdgcn_perm(x3.x, x2.x, 0x07060302u)};
      ST(u32x2, ub + 2 * U_CH) = (u32x2){__builtin_amdgcn_perm(x1.y, x0.y, 0x05040100u), __builtin_amdgcn_perm(x3.y, x2.y, 0x05040100u)};
      ST(u32x2, ub + 3 * U_CH) = (u32x2){__builtin_amdgcn_perm(x1.y, x0.y, 0x07060302u), __builtin_amdgcn_perm(x3.y, x2.y, 0x07060302u)};
      SCHED();
    }
    STAMP(4);
  }
  lptr ka[8];
  {
    const int yb = 2031 - l15 + 8 * g - 128 * half - 96 - 1792;
    lptr k7 = smem + KR_OFF + ch * KR_CH + (yb & 1) * KR_CP + ((yb + 1) >> 1) * 4;
#pragma unroll
    for (int n = 0; n < 8; ++n) { ka[n] = k7 + 512 * (7 - n); asm volatile("" : "+v"(ka[n])); }
  }
  lptr ua = smem + U_OFF + ch * U_CH + (8 * g + 16 * l15 + 224 - 128 * half - 96) * 2;
  asm volatile("" : "+v"(ua));
  STAMP(6);
  LBAR();
  STAMP(7); CYC(20);
  {
    u32x4 A0[8], A1[8]; s16x8 B0[8], B1[8];
#define LOADC(A, B, rr) do { _Pragma("unroll") for (int n = 0; n < 8; ++n) { \
      lptr kp = ka[n] + 64 * (3 - (rr)); \
      A[n] = (u32x4){LD(u32, kp), LD(u32, kp + 4), LD(u32, kp + 8), LD(u32, kp + 12)}; \
      B[n] = LD(s16x8, ua + 64 * (3 - (rr)) + 512 * n); } } while (0)
#define MMC(A, B) do { _Pragma("unroll") for (int nu = 0; nu < 8; ++nu) _Pragma("unroll") for (int mu = 0; mu < 8 - nu; ++mu) \
      acc[nu + mu] = MFMA16(__builtin_bit_cast(s16x8, A[nu]), B[mu], acc[nu + mu]); } while (0)
#define MIXC() do { _Pragma("unroll") for (int q = 0; q < 12; ++q) { __builtin_amdgcn_sched_group_barrier(0x8, MIXM, 0); __builtin_amdgcn_sched_group_barrier(0x100, 2, 0); } \
      __builtin_amdgcn_sched_group_barrier(0x8, 36 - 12 * MIXM, 0); } while (0)
    LOADC(A0, B0, 0);
    SCHED();
    dsk2 = *(const u32*)(dsk + (c & ~1));
    epoch = load_epoch(ws);
    if (!half) {
#pragma unroll
      for (int r = 0; r < 4; ++r) gx[r] = __builtin_amdgcn_raw_buffer_load_b128(rx, lane * 16 + 1024 * r, (16 * cg + 4 * ch) * 1024, 0);
      gwv = *(const u32x4*)(gw + 8 * lane);
      pv = touch_pages(ws, out, rt, lane);
    }
    SCHED();
#define WOP(j) do { if (!half && (j) >= WOE) fetch_wo(wo, ct, ch, j, lane, smem); SCHED(); } while (0)
    WOP(0); WAIT_L0(); SCHED(); LOADC(A1, B1, 1); MMC(A0, B0); MIXC(); PINC();
    WOP(1); WAIT_L0(); SCHED(); LOADC(A0, B0, 2); MMC(A1, B1); MIXC(); PINC();
    WOP(2); WAIT_L0(); SCHED(); LOADC(A1, B1, 3); MMC(A0, B0); MIXC(); PINC();
    WOP(3); WAIT_L0(); SCHED(); MMC(A1, B1); PINC();
#undef WOP
#undef LOADC
#undef MMC
#undef MIXC
#undef PINC
  }
  STAMP(8); CYC(21); CYCD(20, 21);
  {
    lptr ex = smem + EX_OFF + w * 4096 + lane * 16, ei = smem + EX_OFF + (w ^ 4) * 4096 + lane * 16;
    f32x4 keep[4];
    if (half) {
#pragma unroll
      for (int i = 0; i < 4; ++i) { ST(f32x4, ex + 1024 * i) = acc[4 + i]; keep[i] = acc[i]; }
    } else {
#pragma unroll
      for (int i = 0; i < 4; ++i) { ST(f32x4, ex + 1024 * i) = acc[i]; keep[i] = acc[4 + i]; }
    }
    lptr up = smem + U_OFF + ch * U_CH + 480 + 2 * (16 * l15 + 4 * g) + 2048 * (1 - half);
    const float dsk_f = (c & 1) ? bf_hi(dsk2) : bf_lo(dsk2);
#pragma unroll
    for (int i = 0; i < 4; ++i) {
      const u32x2 uu = LD(u32x2, up + 512 * i);
      keep[i] += (f32x4){bf_lo(uu.x), bf_hi(uu.x), bf_lo(uu.y), bf_hi(uu.y)} * dsk_f;
    }
    SCHED();
    if (!half) {
      asm volatile("" :: "v"(pv));
      float sg[4];
#pragma unroll
      for (int r = 0; r < 4; ++r) {
        float s0 = 0.f;
#pragma unroll
        for (int i = 0; i < 4; ++i) s0 = __builtin_amdgcn_fdot2_f32_bf16(u2b(gx[r][i]), u2b(gwv[i]), s0, false);
        sg[r] = rbf(__builtin_amdgcn_rcpf(1.f + __builtin_amdgcn_exp2f(-LOG2E * rbf(wave_sum(s0)))));
      }
      if (lane == 63) store16(__builtin_bit_cast(u32x4, (f32x4){sg[0], sg[1], sg[2], sg[3]}), rws, (int)WS_G + (16 * cg + 4 * ch) * 4 + (lane - 63));
    }
    const int bo = (int)WS_BT + c * 4096 + 2048 * (1 - half) + 32 * l15 + 16 * (g >> 1) + 512 * (g & 1);
    SCHED();
    LBAR();
    f32x4 o4[4];
#pragma unroll
    for (int i = 0; i < 4; ++i) o4[i] = LD(f32x4, ei + 1024 * i);
    SCHED();
#pragma unroll
    for (int i = 0; i < 2; ++i) {
      const f32x4 y0 = keep[2 * i] + o4[2 * i], y1 = keep[2 * i + 1] + o4[2 * i + 1];
      const auto sx = __builtin_amdgcn_permlane16_swap(pk_bf16(y0[0], y0[1]), pk_bf16(y1[0], y1[1]), false, false);
      const auto sy = __builtin_amdgcn_permlane16_swap(pk_bf16(y0[2], y0[3]), pk_bf16(y1[2], y1[3]), false, false);
      store16((u32x4){sx[0], sy[0], sx[1], sy[1]}, rws, bo + 1024 * i);
    }
    asm volatile(".Lfused_code_tail:");
  }
  STAMP(9);
}

struct Tags { u64 a, b; };
DI Tags poll_load(char* ws, int off, int lane) {
  gu64* p = (gu64*)(ws + WS_FLAG + off) + lane;
  return Tags{__hip_atomic_load(p, RLX_AGENT), __hip_atomic_load(p + 64, RLX_AGENT)};
}
DI void poll_wait(char* ws, int off, int lane, u64 tag, Tags& f, u64 ma, u64 mb) {
#pragma unroll 1
  for (int spin = 0; spin < (1 << 20); ++spin) {
    if ((__ballot(f.a != tag) & ma) == 0 && (__ballot(f.b != tag) & mb) == 0) break;
    __builtin_amdgcn_s_sleep(POLL_SLEEP);
    f = poll_load(ws, off, lane);
    __builtin_amdgcn_s_waitcnt(0x0F70);
  }
}

DI void out_role(char* __restrict__ ws, u16* __restrict__ out, int rt, int ct, u64 tag, Tags fl, lptr smem STAMP_ARG) {
  const int tid = threadIdx.x, lane = tid & 63;
  const int w = __builtin_amdgcn_readfirstlane(tid >> 6);
  const int th = w & 1, kq = w >> 1, hh = lane >> 5, l31 = lane & 31;
  const rsrc_t rws = mk_rsrc(ws, WS_USED);
  const int foff = (blockIdx.x & 31) * 2048;
  if (w == 0) poll_wait(ws, foff, lane, tag, fl, ~0ull, ~0ull);
  RAW_BAR();
  STAMP(11);
  if (w == 1) fl = poll_load(ws, foff + 1024, lane);
  f32x4 kvr[KVC][5];
#pragma unroll
  for (int cp = 0; cp < KVC; ++cp) {
#pragma unroll
    for (int i = 0; i < 4; ++i) kvr[cp][i] = __builtin_bit_cast(f32x4, __builtin_amdgcn_raw_buffer_load_b128(rws, tid * 16, (int)WS_KV + cp * KV_CP + 8192 * i, 16));
    kvr[cp][4] = __builtin_bit_cast(f32x4, __builtin_amdgcn_raw_buffer_load_b128(rws, (tid & 127) * 16, (int)WS_KV + cp * KV_CP + 32768, 16));
  }
  SCHED(); RAW_BAR(); SCHED();
  u32x4 pf[8];
#define LOAD_PQ(i0, i1) do { _Pragma("unroll") for (int i = i0; i < i1; ++i) \
    pf[i] = __builtin_amdgcn_raw_buffer_load_b128(rws, lane * 16, (int)WS_PQ + ((2 * rt + th) * 32 + 8 * kq + i) * 1024, 16); } while (0)
  LOAD_PQ(0, 3);
  STAMP(12);
#pragma unroll
  for (int i = 0; i < 5; ++i) {
    f32x4 v = kvr[0][i];
#pragma unroll
    for (int cp = 1; cp < KVC; ++cp) v += kvr[cp][i];
    ST(u32x2, smem + P2_KV + (i < 4 ? tid + 512 * i : 2048 + (tid & 127)) * 8) = (u32x2){pk_bf16(v[0], v[1]), pk_bf16(v[2], v[3])};
  }
  f32x16 acc[2];
#pragma unroll
  for (int i = 0; i < 16; ++i) { acc[0][i] = 0.f; acc[1][i] = 0.f; }
  f32x16 cinit = acc[0];
  cinit[8] = EPS;
  lptr kva = smem + P2_KV + 8 * kq * 544 + min(l31, 16) * 32 + hh * 16;
  const int blk = (lane >> 4) & 1, q4 = (lane & 15) >> 2, p4 = lane & 3;
  const int sw = ((q4 >> 1) & 1) << 6;
  lptr wa0 = smem + P2_WO + kq * WOQ + (4 * hh + q4) * 128 + blk * 32 + p4 * 8 + sw;
  lptr wa1 = smem + P2_WO + kq * WOQ + (4 * hh + q4) * 128 + blk * 32 + p4 * 8 + (64 ^ sw);
  STAMP(13);
  const u64 mct = 0xffffull << (16 * ct & 63), mrt = 0xfull << (4 * rt & 63);
  const u64 ma = (ct < 4 ? mct : 0ull) | (rt < 16 ? mrt : 0ull), mb = (ct < 4 ? 0ull : mct) | (rt < 16 ? 0ull : mrt);
  if (w == 1) { const u32 ok = (__ballot(fl.a != tag) & ma) == 0 && (__ballot(fl.b != tag) & mb) == 0; if (lane == 0) ST(u32, smem + P2_OK) = ok; }
  LBAR();
  LOAD_PQ(3, 8);
#undef LOAD_PQ
  const bool early = __builtin_amdgcn_readfirstlane(LD(u32, smem + P2_OK)) != 0;
  u32x4 btr; float gt;
#define LOAD_B() do { \
    btr = __builtin_amdgcn_raw_buffer_load_b128(rws, (lane >> 2) * 4096 + (lane & 3) * 16, (int)WS_BT + (64 * ct + 16 * kq) * 4096 + 128 * rt + 64 * th, 16); \
    gt = u2f(__builtin_amdgcn_raw_buffer_load_b32(rws, l31 * 4, (int)WS_G + (64 * rt + 32 * th) * 4, 16)); } while (0)
  if (early) LOAD_B();
  u32x2 bb[2];
  lptr ba = smem + P2_BT + w * 1024 + (4 * hh + q4) * 64 + blk * 32 + p4 * 8;
#define TURN_B() do { ST(u32x4, smem + P2_BT + w * 1024 + lane * 16) = btr;        \
    bb[0] = __builtin_bit_cast(u32x2, tr_read(ba)); bb[1] = __builtin_bit_cast(u32x2, tr_read(ba + 512)); } while (0)
  STAMP(14);
  {
    f32x16 d[2]; s16x8 kf[8], wf[2][2]; u32x4 of[2];
#define OMM(i) do { d[(i) & 1] = MFMA32(kf[i], __builtin_bit_cast(s16x8, pf[i]), cinit); asm volatile("" : "+v"(d[(i) & 1])); } while (0)
#define NORM(i) do { const f32x16& dd = d[(i) & 1]; const float inv = __builtin_amdgcn_rcpf(dd[8]); \
      of[(i) & 1] = (u32x4){pk_bf16(dd[0] * inv, dd[1] * inv), pk_bf16(dd[2] * inv, dd[3] * inv), pk_bf16(dd[4] * inv, dd[5] * inv), pk_bf16(dd[6] * inv, dd[7] * inv)}; \
      asm volatile("" : "+v"(of[(i) & 1]) : "v"(dd)); } while (0)
#define LDW(i) do { wf[(i) & 1][0] = cat8(tr_read(wa0 + (i) * 2048), tr_read(wa0 + (i) * 2048 + 1024)); \
      wf[(i) & 1][1] = cat8(tr_read(wa1 + (i) * 2048), tr_read(wa1 + (i) * 2048 + 1024)); } while (0)
#define AMM(i) do { acc[0] = MFMA32(wf[(i) & 1][0], __builtin_bit_cast(s16x8, of[(i) & 1]), acc[0]); \
      acc[1] = MFMA32(wf[(i) & 1][1], __builtin_bit_cast(s16x8, of[(i) & 1]), acc[1]); asm volatile("" : "+v"(acc[0]), "+v"(acc[1])); } while (0)
#pragma unroll
    for (int i = 0; i < 8; ++i) kf[i] = LD(s16x8, kva + i * 544);
    LDW(0);
    SCHED();
    OMM(0); OMM(1); SCHED(); NORM(0); SCHED();
#pragma unroll
    for (int i = 0; i < 8; ++i) {
      if (i + 1 < 8) LDW(i + 1);
      SCHED();
      AMM(i);
      if (i + 2 < 8) OMM(i + 2);
      SCHED();
      if (i + 1 < 8) NORM(i + 1);
      SCHED();
      if (i == TURN_AT && early) { TURN_B(); SCHED(); }
    }
#undef OMM
#undef NORM
#undef LDW
#undef AMM
  }
  STAMP(15);
  f32x4 own[2];
#pragma unroll
  for (int q = 0; q < 4; ++q) {
    lptr ex = smem + P2_EX + ((2 * q + th) * 4 + kq) * 2048 + lane * 16;
    const f32x16& s = acc[q >> 1];
    const f32x4 s0 = {s[8 * (q & 1)], s[8 * (q & 1) + 1], s[8 * (q & 1) + 2], s[8 * (q & 1) + 3]};
    const f32x4 s1 = {s[8 * (q & 1) + 4], s[8 * (q & 1) + 5], s[8 * (q & 1) + 6], s[8 * (q & 1) + 7]};
    if (q == kq) { own[0] = s0; own[1] = s1; } else { ST(f32x4, ex) = s0; ST(f32x4, ex + 1024) = s1; }
  }
  if (!early && w == 1) poll_wait(ws, foff + 1024, lane, tag, fl, ma, mb);
  LBAR();
  if (!early) { LOAD_B(); TURN_B(); }
#undef TURN_B
#undef LOAD_B
  STAMP(16);
  lptr ei = smem + P2_EX + w * 8192 + lane * 16;
  const float g1 = rbf(1.f - gt);
  u32x2 res[2];
#pragma unroll
  for (int i = 0; i < 2; ++i) {
    const f32x4 o = (own[i] + LD(f32x4, ei + ((kq ^ 1) * 2048 + 1024 * i))) + (LD(f32x4, ei + ((kq ^ 2) * 2048 + 1024 * i)) + LD(f32x4, ei + ((kq ^ 3) * 2048 + 1024 * i)));
    res[i] = (u32x2){pk_bf16(gt * o[0] + g1 * bf_lo(bb[i].x), gt * o[1] + g1 * bf_hi(bb[i].x)), pk_bf16(gt * o[2] + g1 * bf_lo(bb[i].y), gt * o[3] + g1 * bf_hi(bb[i].y))};
  }
  const auto sx = __builtin_amdgcn_permlane32_swap(res[0].x, res[1].x, false, false);
  const auto sy = __builtin_amdgcn_permlane32_swap(res[0].y, res[1].y, false, false);
  *(u32x4*)(out + (size_t)(64 * rt + 32 * th + l31) * DM + 64 * ct + 16 * kq + 8 * hh) = (u32x4){sx[0], sy[0], sx[1], sy[1]};
  STAMP(17);
  if (blockIdx.x == 0 && w == 1) {
    poll_wait(ws, foff + 1024, lane, tag, fl, ~0ull, ~0ull);
    if (lane == 0) __hip_atomic_store((gu64*)(ws + WS_SYNC), tag, RLX_AGENT);
  }
}

__global__ __launch_bounds__(512, 2) void fused(const u16* __restrict__ x, const u16* __restrict__ wqkv, const u16* __restrict__ wo,
                                                const u16* __restrict__ lar, const u16* __restrict__ aim, const u16* __restrict__ cre,
                                                const u16* __restrict__ cim, const u16* __restrict__ ldt, const u16* __restrict__ dsk,
                                                const u16* __restrict__ gw, char* __restrict__ ws, u16* __restrict__ out) {
  extern __shared__ __attribute__((aligned(16))) char smem_[];
  lptr smem = (lptr)smem_;
  STAMP_DECL; STAMP(0);
  asm volatile("" :: "s"(x), "s"(wqkv), "s"(wo), "s"(lar), "s"(aim), "s"(cre), "s"(cim), "s"(ldt), "s"(dsk), "s"(gw), "s"(ws), "s"(out));
  STAMP(18);
  const int b = blockIdx.x, xcd = b & 7, slot = b >> 3, idx = slot >> 1;
  const int rt = 8 * (xcd >> 1) + (slot & 7), ct = 4 * (xcd & 1) + (slot >> 3);
  const int tt = 4 * (xcd >> 1) + (idx & 3), hg = 4 * (xcd & 1) + (idx >> 2);
  const int cg = xcd * 16 + idx;
  int pofs = (threadIdx.x & 63) * 8;
  asm volatile("" : "+v"(pofs));
  u64 epoch; u32 tmp0, tmp1, tmp2; u64 pc;
  u32 pr[5]; u32x2 xr[8];
  asm volatile(
      "s_bitcmp1_b32 %[b], 3\n\t"
      "s_cbranch_scc0 .Lpre_end%=\n\t"
      "s_cmp_gt_u32 %[wv], 3\n\t"
      "s_cbranch_scc1 .Lpre_b%=\n\t"
      "v_lshlrev_b32 %[p4], 2, %[tid]\n\t"
      "v_lshl_add_u32 %[p4], %[cg], 10, %[p4]\n\t"
      "global_load_dword %[p0], %[p4], %[lar]\n\t"
      "global_load_dword %[p1], %[p4], %[aim]\n\t"
      "global_load_dword %[p2], %[p4], %[cre]\n\t"
      "global_load_dword %[p3], %[p4], %[cim]\n\t"
      "v_lshrrev_b32 %[t0], 7, %[p4]\n\t"
      "v_and_b32 %[t0], 0x3fe, %[t0]\n\t"
      "global_load_ushort %[p4], %[t0], %[ldt]\n\t"
      "s_barrier\n\t"
      "s_branch .Lpre_end%=\n"
      ".Lpre_b%=:\n\t"
      "s_getpc_b64 %[pc]\n\t"
      "v_and_b32 %[t0], 0xff, %[tid]\n\t"
      "v_lshlrev_b32 %[t0], 7, %[t0]\n\t"
      "v_mov_b32 %[t1], .Lfused_code_tail-.Lpre_b%=-4\n\t"
      "v_med3_i32 %[t0], 0, %[t0], %[t1]\n\t"
      "global_load_dword %[t2], %[t0], %[pc]\n\t"
      "v_lshl_add_u32 %[t0], %[cg], 4, %[tid]\n\t"
      "v_and_b32 %[t0], 0xff, %[t0]\n\t"
      "v_lshlrev_b32 %[t0], 12, %[t0]\n\t"
      "v_lshl_add_u32 %[t0], %[cg], 3, %[t0]\n\t"
      "v_add_u32 %[t1], 0x100000, %[t0]\n\t"
      "s_barrier\n\t"
      "global_load_dwordx2 %[x0], %[t0], %[x]\n\t"
      "global_load_dwordx2 %[x1], %[t0], %[x] offset:1024\n\t"
      "global_load_dwordx2 %[x2], %[t0], %[x] offset:2048\n\t"
      "global_load_dwordx2 %[x3], %[t0], %[x] offset:3072\n\t"
      "global_load_dwordx2 %[x4], %[t1], %[x]\n\t"
      "global_load_dwordx2 %[x5], %[t1], %[x] offset:1024\n\t"
      "global_load_dwordx2 %[x6], %[t1], %[x] offset:2048\n\t"
      "global_load_dwordx2 %[x7], %[t1], %[x] offset:3072\n"
      ".Lpre_end%=:"
      : [p0] "=&v"(pr[0]), [p1] "=&v"(pr[1]), [p2] "=&v"(pr[2]), [p3] "=&v"(pr[3]), [p4] "=&v"(pr[4]),
        [x0] "=&v"(xr[0]), [x1] "=&v"(xr[1]), [x2] "=&v"(xr[2]), [x3] "=&v"(xr[3]), [x4] "=&v"(xr[4]), [x5] "=&v"(xr[5]), [x6] "=&v"(xr[6]), [x7] "=&v"(xr[7]),
        [t0] "=&v"(tmp0), [t1] "=&v"(tmp1), [t2] "=&v"(tmp2), [pc] "=&s"(pc)
      : [b] "s"(b), [wv] "s"(__builtin_amdgcn_readfirstlane(threadIdx.x >> 6)), [tid] "v"(threadIdx.x), [cg] "s"(cg),
        [lar] "s"(lar), [aim] "s"(aim), [cre] "s"(cre), [cim] "s"(cim), [ldt] "s"(ldt), [x] "s"(x)
      : "scc", "memory");
  if (__builtin_expect(slot & 1, 0)) conv_role(x, pr, xr, tmp2, dsk, gw, wo, ws, out, rt, cg, ct, epoch, smem STAMP_PASS);
  else gemm_role(x, wqkv, wo, ct, ws, out, rt, tt, hg, epoch, smem STAMP_PASS);
  const int lane = threadIdx.x & 63, w = __builtin_amdgcn_readfirstlane(threadIdx.x >> 6);
  const u64 tag = next_tag(epoch);
  Tags fl = {0, 0};
  if (w == 0) {
    const rsrc_t rf = mk_rsrc(ws, WS_USED);
    fl.a = __builtin_bit_cast(u64, __builtin_amdgcn_raw_buffer_load_b64(rf, pofs, (int)WS_FLAG + (b & 31) * 2048, 16));
    fl.b = __builtin_bit_cast(u64, __builtin_amdgcn_raw_buffer_load_b64(rf, pofs, (int)WS_FLAG + (b & 31) * 2048 + 512, 16));
  }
  SCHED();
  WAIT_VM(0);
  __builtin_amdgcn_s_setprio(0);
  STAMP(10);
  LBAR();
  if (w == 7 && lane < 32) __hip_atomic_store((gu64*)(ws + WS_FLAG + lane * 2048) + ((slot & 1) ? 128 + cg : tt * 8 + hg), tag, RLX_AGENT);
  out_role(ws, out, rt, ct, tag, fl, smem STAMP_PASS);
  STAMP_FLUSH(ws);
}

extern "C" void kernel_launch(void* const* d_in, const int* in_sizes, int n_in, void* d_out, int out_size, void* d_ws,
                              size_t ws_size, hipStream_t stream) {
  (void)in_sizes; (void)n_in; (void)out_size; (void)ws_size;
  (void)hipFuncSetAttribute((const void*)fused, hipFuncAttributeMaxDynamicSharedMemorySize, 163840);
  fused<<<256, 512, 163840, stream>>>((const u16*)d_in[0], (const u16*)d_in[1], (const u16*)d_in[2], (const u16*)d_in[4],
                                      (const u16*)d_in[5], (const u16*)d_in[6], (const u16*)d_in[7], (const u16*)d_in[8],
                                      (const u16*)d_in[9], (const u16*)d_in[10], (char*)d_ws, (u16*)d_out);
}
```

```cpp
#include <hip/hip_runtime.h>
#include <hip/hip_bf16.h>
#include <stdint.h>

typedef unsigned int u32;
typedef unsigned short u16;
typedef __attribute__((ext_vector_type(8))) short s16x8;
typedef __attribute__((ext_vector_type(4))) short s16x4;
typedef __attribute__((ext_vector_type(8))) _Float16 h16x8;
typedef __attribute__((ext_vector_type(2))) _Float16 h16x2;
typedef __attribute__((ext_vector_type(2))) __bf16 b16x2;
typedef __attribute__((ext_vector_type(2))) float f32x2;
typedef __attribute__((ext_vector_type(4))) float f32x4;
typedef __attribute__((ext_vector_type(16))) float f32x16;
typedef __attribute__((ext_vector_type(2))) u32 u32x2;
typedef __attribute__((ext_vector_type(4))) u32 u32x4;
#define LDS_AS __attribute__((address_space(3)))
#define DI __device__ __forceinline__

static constexpr int SEQ = 2048, DM = 512;
#define KVC 1
#define KVO ((6u << 20) + 131072)
static constexpr size_t WS_KV = KVO;
static constexpr int KV_CP = 36864;
static constexpr size_t WS_G = 65536;
static constexpr size_t WS_PQ = 1u << 20;
static constexpr size_t WS_BT = (4u << 20) + 4096;
static constexpr int WS_USED = 8 << 20;
#define SYNCO 49152
static constexpr size_t WS_SYNC = SYNCO;
static constexpr size_t WS_FLAG = 131072;
typedef unsigned long long u64;
typedef __attribute__((address_space(1))) u64 gu64;
typedef __attribute__((address_space(1))) u32 gu32;
#define RLX_AGENT __ATOMIC_RELAXED, __HIP_MEMORY_SCOPE_AGENT
__device__ __forceinline__ u64 next_tag(u64 e) { return e * 6364136223846793005ull + 1442695040888963407ull; }

#define STAMP_ARG
#define STAMP_PASS
#define STAMP_DECL do {} while (0)
#define STAMP(i) do {} while (0)
#define CYC(i) do {} while (0)
#define CYCD(i, j) do {} while (0)
#define STAMP_FLUSH(wsp) do {} while (0)
#define MIXM 2
#define TURN_AT 5
#define WOE 4
#define FPITCH 2048
#define NREP 64
#define BTP 4224
#define PQP 32768
#define WS_BASE 0
#define ROLEBIT 3
#define POLL_SLEEP 2
#define ST_AUX 16
#define WAIT_VM(n) asm volatile("s_waitcnt vmcnt(" #n ")" ::: "memory")
#define WAIT_L0() __builtin_amdgcn_s_waitcnt(0xC07F)
#define RAW_BAR() __builtin_amdgcn_s_barrier()
#define SCHED() __builtin_amdgcn_sched_barrier(0)
#define LBAR() do { WAIT_L0(); asm volatile("" ::: "memory"); RAW_BAR(); asm volatile("" ::: "memory"); } while (0)
typedef LDS_AS char* lptr;
#define LD(T, p) (*(const LDS_AS T*)(p))
#define ST(T, p) (*(LDS_AS T*)(p))

DI u32 pk_bf16(float a, float b) { return __builtin_bit_cast(u32, __builtin_convertvector((f32x2){a, b}, b16x2)); }
DI u32 pk_f16(f32x2 v) { return __builtin_bit_cast(u32, __builtin_convertvector(v, h16x2)); }
DI float bf_lo(u32 v) { return __builtin_bit_cast(float, v << 16); }
DI float bf_hi(u32 v) { return __builtin_bit_cast(float, v & 0xffff0000u); }
DI float u2f(u32 v) { return __builtin_bit_cast(float, v); }
DI b16x2 u2b(u32 v) { return __builtin_bit_cast(b16x2, v); }
DI float rbf(float v) { return bf_lo(pk_bf16(v, v)); }
static constexpr float LOG2E = 1.44269504088896341f, EPS = 1e-6f;
DI float phi_f(float t) {
  const float hi = __builtin_bit_cast(float, max(__builtin_bit_cast(int, t), __builtin_bit_cast(int, EPS)));
  return hi + __builtin_amdgcn_fmed3f(__builtin_amdgcn_exp2f(__builtin_fmaf(t, LOG2E, -EPS * LOG2E)), 0.f, 1.f);
}
DI float exp_f(float v) {
  const float p = v * LOG2E;
  const float r = __builtin_fmaf(v, 1.925963033500011e-8f, __builtin_fmaf(v, LOG2E, -p));
  return __builtin_amdgcn_exp2f(p) * __builtin_fmaf(r, 0.6931471805599453f, 1.f);
}
#define DPP_F(v, ctrl, rmask, bc) __builtin_bit_cast(float, __builtin_amdgcn_update_dpp(0, __builtin_bit_cast(int, v), ctrl, rmask, 0xf, bc))
DI float wave_sum(float r) {
  r += DPP_F(r, 0x111, 0xf, true); r += DPP_F(r, 0x112, 0xf, true); r += DPP_F(r, 0x114, 0xf, true); r += DPP_F(r, 0x118, 0xf, true);
  r += DPP_F(r, 0x142, 0xa, false); r += DPP_F(r, 0x143, 0xc, false);
  return r;
}
DI u32 wave_umax(u32 r) {
#define DPP_U(v, ctrl, rmask, bc) (u32)__builtin_amdgcn_update_dpp(0, (int)(v), ctrl, rmask, 0xf, bc)
  r = max(r, DPP_U(r, 0x111, 0xf, true)); r = max(r, DPP_U(r, 0x112, 0xf, true)); r = max(r, DPP_U(r, 0x114, 0xf, true));
  r = max(r, DPP_U(r, 0x118, 0xf, true)); r = max(r, DPP_U(r, 0x142, 0xa, false)); r = max(r, DPP_U(r, 0x143, 0xc, false));
  return (u32)__builtin_amdgcn_readlane((int)r, 63);
}
DI s16x4 tr_read(lptr p) { return __builtin_amdgcn_ds_read_tr16_b64_v4i16((LDS_AS s16x4*)p); }
DI s16x8 cat8(s16x4 a, s16x4 b) { return __builtin_shufflevector(a, b, 0, 1, 2, 3, 4, 5, 6, 7); }
typedef __amdgpu_buffer_rsrc_t rsrc_t;
DI rsrc_t mk_rsrc(const void* p, int bytes) { return __builtin_amdgcn_make_buffer_rsrc((void*)p, 0, bytes, 0x00020000); }
DI void store16(u32x4 v, rsrc_t r, int off) { __builtin_amdgcn_raw_buffer_store_b128(v, r, off, 0, ST_AUX); }
DI void blds16(rsrc_t r, lptr l, int voff, int soff) { __builtin_amdgcn_raw_ptr_buffer_load_lds(r, (LDS_AS void*)l, 16, voff, soff, 0, 0); }
#define CM_MUL(t, a, w) "v_pk_mul_f32 " t ", " a ", " w " op_sel:[1,1] op_sel_hi:[1,0]\n\t"
#define CM_FMA(r, a, w, t) "v_pk_fma_f32 " r ", " a ", " w ", " t " op_sel_hi:[0,1,1] neg_lo:[0,0,1]\n\t"
DI void cmul2(f32x2 (&r)[2], const f32x2 (&a)[2], const f32x2 (&w)[2]) {
  f32x2 r0, r1;
  asm(CM_MUL("%0", "%2", "%4") CM_MUL("%1", "%3", "%5") CM_FMA("%0", "%2", "%4", "%0") CM_FMA("%1", "%3", "%5", "%1")
      : "=&v"(r0), "=&v"(r1) : "v"(a[0]), "v"(a[1]), "v"(w[0]), "v"(w[1]));
  r[0] = r0; r[1] = r1;
}
DI void cmul4(f32x2 (&r)[2], const f32x2 (&a)[2], const f32x2 (&w)[2], f32x2 (&q)[2], const f32x2 (&b)[2], const f32x2 (&v)[2]) {
  f32x2 r0, r1, q0, q1;
  asm(CM_MUL("%0", "%4", "%6") CM_MUL("%1", "%5", "%7") CM_MUL("%2", "%8", "%10") CM_MUL("%3", "%9", "%11")
      CM_FMA("%0", "%4", "%6", "%0") CM_FMA("%1", "%5", "%7", "%1") CM_FMA("%2", "%8", "%10", "%2") CM_FMA("%3", "%9", "%11", "%3")
      : "=&v"(r0), "=&v"(r1), "=&v"(q0), "=&v"(q1)
      : "v"(a[0]), "v"(a[1]), "v"(w[0]), "v"(w[1]), "v"(b[0]), "v"(b[1]), "v"(v[0]), "v"(v[1]));
  r[0] = r0; r[1] = r1; q[0] = q0; q[1] = q1;
}
DI f32x2 cexp_p(float alpha, float theta, float p) {
  float m = __builtin_amdgcn_exp2f(alpha * p);
  float f = __builtin_amdgcn_fractf(theta * p);
  return (f32x2){m * __builtin_amdgcn_cosf(f), m * __builtin_amdgcn_sinf(f)};
}
#define MFMA16(a, b, c) __builtin_amdgcn_mfma_f32_16x16x32_bf16((a), (b), (c), 0, 0, 0)
#define MFMA16H(a, b, c) __builtin_amdgcn_mfma_f32_16x16x32_f16((a), (b), (c), 0, 0, 0)
#define MFMA32(a, b, c) __builtin_amdgcn_mfma_f32_32x32x16_bf16((a), (b), (c), 0, 0, 0)

static constexpr int STAGE = 40960;
static constexpr int WOQ = 17408;
static constexpr int P2_WO = 0, P2_KV = 4 * WOQ, P2_OK = P2_KV + 17408, P2_BT = P2_KV + 18432, P2_EX = P2_BT + 8192;

DI u64 load_epoch(char* ws) {
  int vzero;
  asm volatile("v_mov_b32 %0, 0" : "=v"(vzero));
  return __hip_atomic_load((gu64*)(ws + WS_SYNC + vzero), RLX_AGENT);
}
DI u32 touch_pages(const char* ws, const u16* out, int rt, int lane) {
  const char* p = lane < 4 ? ws + (size_t)((0xf871u >> (4 * lane)) & 15) * (512u << 10) : (const char*)out + (size_t)rt * 65536;
  return lane < 5 ? *(const u32*)p : 0u;
}
DI void fetch_wo(const u16* __restrict__ wo, int ct, int q, int part, int lane, lptr smem) {
  const rsrc_t rwo = mk_rsrc(wo, DM * DM * 2);
  const int sr = lane >> 3, sp = lane & 7, sc = (sp ^ (((sr >> 1) & 1) << 2)) << 4;
#pragma unroll
  for (int i = 4 * part; i < 4 * part + 4; ++i) blds16(rwo, smem + P2_WO + q * WOQ + 1024 * i, sr * 1024 + sc, (128 * q + 8 * i) * 1024 + 128 * ct);
}

DI void gemm_role(const u16* __restrict__ x, const u16* __restrict__ wqkv, const u16* __restrict__ wo, int ct, char* __restrict__ ws, const u16* out, int rt, int tt, int hg,
                  u64& epoch, lptr smem STAMP_ARG) {
  const int tid = threadIdx.x, lane = tid & 63;
  const int w = __builtin_amdgcn_readfirstlane(tid >> 6);
  const int wm = w >> 2, wh = w & 3, g = lane >> 4, l15 = lane & 15;
  const int sr = lane >> 3, sp = lane & 7;
  const rsrc_t rx = mk_rsrc(x, SEQ * DM * 2), rw = mk_rsrc(wqkv, DM * 1536 * 2);
  const int xv = sr * 1024 + ((sp ^ sr) << 4), xs0 = (tt * 128 + 16 * w) * 1024;
  const int fw = ((sr >> 1) & 1) | ((w & 1) << 1);
  const int wv = sr * 3072 + ((sp ^ (fw << 1)) << 4), ws0 = 8 * w * 3072 + hg * 128;
  const int ROT = 2 * (tt & 3) + ((tt >> 2) & 1);
  const rsrc_t rwo = mk_rsrc(wo, DM * DM * 2);
  const int ov = sr * 1024 + ((sp ^ (((sr >> 1) & 1) << 2)) << 4);
#define ISS(s, i) do { lptr sb = smem + ((s) & 3) * STAGE; \
    if ((s) >= 8) { if ((i) < 4) blds16(rwo, smem + P2_WO + (w >> 1) * WOQ + (64 * (w & 1) + 32 * ((s) - 8) + 8 * (i)) * 128, ov, (64 * w + 32 * ((s) - 8) + 8 * (i)) * 1024 + 128 * ct); } \
    else if ((i) < 2) blds16(rx, sb + w * 2048 + (i) * 1024, xv, xs0 + (((s) + ROT) & 7) * 128 + (i) * 8192); \
    else blds16(rw, sb + 16384 + ((i) - 2) * 8192 + w * 1024, wv, ws0 + (((s) + ROT) & 7) * 64 * 3072 + ((i) - 2) * 1024); } while (0)
#define ISSUE(s) do { ISS(s, 0); ISS(s, 1); ISS(s, 2); ISS(s, 3); ISS(s, 4); } while (0)
  gu64* sync_z = (gu64*)(ws + WS_SYNC + 128 + hg * 128);
  ISSUE(0);
  STAMP(19);
  SCHED(); RAW_BAR(); SCHED();
  u32 pv2 = 0;
  SCHED();
  ISSUE(1);
  SCHED();
  epoch = load_epoch(ws);
  const rsrc_t rws = mk_rsrc(ws, WS_USED);
  const bool clear = tt == 0 && w == 7;
  if (clear) {
    const u32x4 z4 = {0u, 0u, 0u, 0u};
#pragma unroll
    for (int i = 0; i < 4; ++i) store16(z4, rws, lane * 16 + (int)WS_KV + hg * 4352 + 1024 * i);
    store16(z4, rws, (lane & 15) * 16 + (int)WS_KV + hg * 4352 + 4096);
  }
  SCHED();
  const int xo = (64 * wm + l15) * 128 + ((g ^ (lane & 7)) << 4);
  const int q4 = l15 >> 2, p4 = lane & 3;
  const int wo_ = 16384 + (8 * g + q4) * 128 + ((wh ^ (((q4 >> 1) & 1) | ((g & 1) << 1))) << 5) + 8 * p4;
  f32x4 aq[4], ak[4], av[4];
#pragma unroll
  for (int i = 0; i < 4; ++i) { aq[i] = (f32x4){EPS, EPS, EPS, EPS}; ak[i] = aq[i]; av[i] = (f32x4){0, 0, 0, 0}; }
  s16x8 xa[4], wa[3], xb[4], wb[3];
#define LDWF(wf, s, ks, st) wf[st] = cat8(tr_read(smem + ((s) & 3) * STAGE + wo_ + (st) * 8192 + (ks) * 4096), tr_read(smem + ((s) & 3) * STAGE + wo_ + (st) * 8192 + (ks) * 4096 + 512))
#define LDXF(xf, s, ks, mt) xf[mt] = LD(s16x8, smem + ((s) & 3) * STAGE + ((xo ^ ((ks) * 64)) + (mt) * 2048))
#define LOADF(xf, wf, s, ks) do { LDWF(wf, s, ks, 0); LDWF(wf, s, ks, 1); LDWF(wf, s, ks, 2); \
    LDXF(xf, s, ks, 0); LDXF(xf, s, ks, 1); LDXF(xf, s, ks, 2); LDXF(xf, s, ks, 3); } while (0)
#define MM3(xf, wf, mt) do { aq[mt] = MFMA16(wf[0], xf[mt], aq[mt]); ak[mt] = MFMA16(xf[mt], wf[1], ak[mt]); av[mt] = MFMA16(xf[mt], wf[2], av[mt]); \
    asm volatile("" : "+v"(aq[mt]), "+v"(ak[mt]), "+v"(av[mt])); } while (0)
#define HALF(xf, wf, xn, wn, ls, lk, is) do { SCHED(); MM3(xf, wf, 0); SCHED(); \
    if ((ls) >= 0) { LDWF(wn, ls, lk, 0); LDXF(xn, ls, lk, 0); LDWF(wn, ls, lk, 1); } if ((is) >= 0) ISS(is, 0); SCHED(); MM3(xf, wf, 1); SCHED(); \
    if ((ls) >= 0) { LDWF(wn, ls, lk, 2); LDXF(xn, ls, lk, 1); LDXF(xn, ls, lk, 2); } if ((is) >= 0) ISS(is, 1); SCHED(); MM3(xf, wf, 2); SCHED(); \
    if ((ls) >= 0) LDXF(xn, ls, lk, 3); if ((is) >= 0) { ISS(is, 2); ISS(is, 3); } SCHED(); MM3(xf, wf, 3); SCHED(); \
    if ((is) >= 0) ISS(is, 4); SCHED(); } while (0)
#define STEP(s, vm, i1, i2) do { WAIT_L0(); HALF(xa, wa, xb, wb, s, 1, i1); \
    WAIT_L0(); WAIT_VM(vm); RAW_BAR(); HALF(xb, wb, xa, wa, (s) + 1, 0, i2); } while (0)
  STAMP(1);
  if (clear) WAIT_VM(11); else WAIT_VM(6);
  RAW_BAR();
  STAMP(2);
  LOADF(xa, wa, 0, 0);
  STEP(0, 5, 2, -1);
  if (clear && lane == 0) __hip_atomic_store(sync_z, next_tag(epoch), RLX_AGENT);
  STAMP(3);
  STEP(1, 5, 3, -1);
  STEP(2, 5, 4, 5);
  STEP(3, 5, -1, 6);
  STAMP(4); CYC(20);
  if (w == 0) pv2 = touch_pages(ws, out, rt, lane);
  STEP(4, 5, -1, 7);
  u64 ready = __hip_atomic_load(sync_z, RLX_AGENT);
  STEP(5, 5, -1, 8);
  if (w < 4) __builtin_amdgcn_s_setprio(2);
  STEP(6, 8, 9, -1);
  STAMP(5); CYC(21); CYCD(20, 21); CYC(21);
  {
    const u64 tag = next_tag(epoch);
#pragma unroll 1
    for (int spin = 0; ready != tag && spin < (1 << 22); ++spin) {
      __builtin_amdgcn_s_sleep(2); ready = __hip_atomic_load(sync_z, RLX_AGENT); __builtin_amdgcn_s_waitcnt(0x0F70);
    }
  }
  WAIT_L0(); HALF(xa, wa, xb, wb, 7, 1, -1); WAIT_L0(); SCHED();
#pragma unroll
  for (int mt = 0; mt < 4; ++mt) aq[mt] = MFMA16(wb[0], xb[mt], aq[mt]);
  asm volatile("" : "+v"(aq[0]), "+v"(aq[1]), "+v"(aq[2]), "+v"(aq[3]));
  SCHED();
#pragma unroll
  for (int mt = 0; mt < 4; ++mt) { ak[mt] = MFMA16(xb[mt], wb[1], ak[mt]); av[mt] = MFMA16(xb[mt], wb[2], av[mt]); }
  asm volatile("" : "+v"(ak[0]), "+v"(ak[1]), "+v"(ak[2]), "+v"(ak[3]), "+v"(av[0]), "+v"(av[1]), "+v"(av[2]), "+v"(av[3]));
  SCHED();
  asm volatile("" :: "v"(pv2));
  STAMP(6);
#undef ISS
#undef ISSUE
#undef LOADF
#undef MM3
#undef HALF
#undef LDWF
#undef LDXF
#undef STEP
  const int h = 4 * hg + wh;
  const int pqv = (g >> 1) * 512 + l15 * 16 + (g & 1) * 256, pqs = (int)WS_PQ + (tt * 4 + 2 * wm) * PQP + h * 1024;
#pragma unroll
  for (int pr = 0; pr < 2; ++pr) {
    const f32x4 q0 = aq[2 * pr], q1 = aq[2 * pr + 1];
    const auto s0 = __builtin_amdgcn_permlane16_swap(pk_bf16(phi_f(q0[0]), phi_f(q0[1])), pk_bf16(phi_f(q1[0]), phi_f(q1[1])), false, false);
    const auto s1 = __builtin_amdgcn_permlane16_swap(pk_bf16(phi_f(q0[2]), phi_f(q0[3])), pk_bf16(phi_f(q1[2]), phi_f(q1[3])), false, false);
    store16((u32x4){s0[0], s1[0], s0[1], s1[1]}, rws, pqv + pqs + pr * PQP);
  }
  SCHED();
  STAMP(7);
  f32x4 kvt = {0, 0, 0, 0}, zz = {0, 0, 0, 0};
  const u32x4 ones_u = {0x3f803f80u, 0x3f803f80u, 0x3f803f80u, 0x3f803f80u};
#pragma unroll
  for (int pr = 0; pr < 2; ++pr) {
    u32x4 pk, vv;
#pragma unroll
    for (int m = 0; m < 2; ++m) {
      const f32x4 kk = ak[2 * pr + m], v4 = av[2 * pr + m];
      pk[2 * m] = pk_bf16(phi_f(kk[0]), phi_f(kk[1])); pk[2 * m + 1] = pk_bf16(phi_f(kk[2]), phi_f(kk[3]));
      vv[2 * m] = pk_bf16(v4[0], v4[1]); vv[2 * m + 1] = pk_bf16(v4[2], v4[3]);
    }
    kvt = MFMA16(__builtin_bit_cast(s16x8, vv), __builtin_bit_cast(s16x8, pk), kvt);
    zz = MFMA16(__builtin_bit_cast(s16x8, ones_u), __builtin_bit_cast(s16x8, pk), zz);
  }
  lptr red = smem + 4 * WOQ;
#pragma unroll
  for (int r = 0; r < 4; ++r) ST(float, red + (wm * 4 + wh) * 1088 + l15 * 4 + (4 * g + r) * 64) = kvt[r];
  if (g == 0) ST(float, red + (wm * 4 + wh) * 1088 + l15 * 4 + 1024) = zz[0];
  LBAR();
  STAMP(8);
  float* kvacc = (float*)(ws + WS_KV + (tt % KVC) * KV_CP) + hg * 1088 + tid;
  atomicAdd(kvacc, LD(float, red + tid * 4) + LD(float, red + 4352 + tid * 4));
  atomicAdd(kvacc + 512, LD(float, red + 2048 + tid * 4) + LD(float, red + 4352 + 2048 + tid * 4));
  if (tid < 64) atomicAdd(kvacc + 1024, LD(float, red + 4096 + tid * 4) + LD(float, red + 4352 + 4096 + tid * 4));
  STAMP(9); CYC(22); CYCD(21, 22);
}

static constexpr int TB_ROW = 544, TB_BUF = 16 * TB_ROW, TB_CH = 2 * TB_BUF;
static constexpr int KR_OFF = 4 * TB_CH, KR_CP = 4160, KR_CH = 2 * KR_CP;
static constexpr int U_OFF = KR_OFF + 4 * KR_CH, U_CH = (2048 + 240) * 2;
static constexpr int EX_OFF = U_OFF + 4 * U_CH;
static_assert(EX_OFF + 32768 <= 163840 && TB_CH == WOQ && P2_EX + 65536 <= 163840, "LDS map");

DI void conv_role(const u16* __restrict__ x, u32 (&pr)[5], u32x2 (&xr)[8], u32 pf, const u16* __restrict__ dsk, const u16* __restrict__ gw,
                  const u16* __restrict__ wo, char* __restrict__ ws, const u16* out, int rt, int cg, int ct, u64& epoch, lptr smem STAMP_ARG) {
  const int tid = threadIdx.x, lane = tid & 63;
  const int w = __builtin_amdgcn_readfirstlane(tid >> 6);
  const int ch = w & 3, half = w >> 2, g = lane >> 4, l15 = lane & 15;
  const int c = 4 * cg + ch;
  const rsrc_t rx = mk_rsrc(x, SEQ * DM * 2);
  u32 dsk2, pv = 0;
  u32x4 gx[4], gwv;
  const rsrc_t rws = mk_rsrc(ws, WS_USED);
  f32x4 acc[8];
#pragma unroll
  for (int i = 0; i < 8; ++i) acc[i] = (f32x4){0, 0, 0, 0};
#define PINC() do { asm volatile("" : "+v"(acc[0]), "+v"(acc[1]), "+v"(acc[2]), "+v"(acc[3]), "+v"(acc[4]), "+v"(acc[5]), "+v"(acc[6]), "+v"(acc[7])); SCHED(); } while (0)
  if (w < 4) {
    const int cw = 4 * cg + w;
    if (lane < 8) ST(u32x4, smem + KR_OFF + w * KR_CH + (lane >> 2) * KR_CP + 4096 + 16 * (lane & 3)) = (u32x4){0u, 0u, 0u, 0u};
    __builtin_amdgcn_s_setprio(2);
    asm volatile("s_waitcnt vmcnt(0)" : "+v"(pr[0]), "+v"(pr[1]), "+v"(pr[2]), "+v"(pr[3]), "+v"(pr[4]));
    const u32 lar2 = pr[0], aim2 = pr[1], cre2 = pr[2], cim2 = pr[3], ldt_u = pr[4];
    const float dt = rbf(exp_f(bf_lo(ldt_u)));
    float al[2], th[2];
    al[0] = -rbf(exp_f(bf_lo(lar2))) * dt * LOG2E; al[1] = -rbf(exp_f(bf_hi(lar2))) * dt * LOG2E;
    th[0] = bf_lo(aim2) * dt * 0.15915494309189535f; th[1] = bf_hi(aim2) * dt * 0.15915494309189535f;
    STAMP(1);
    u32 ce = wave_umax(max(max(cre2 & 0x7f80u, (cre2 >> 16) & 0x7f80u), max(cim2 & 0x7f80u, (cim2 >> 16) & 0x7f80u))) >> 7;
    ce = ce < 1 ? 1 : (ce > 253 ? 253 : ce);
    const float cinv = __builtin_bit_cast(float, (254u - ce) << 23);
    const float kscale = 2.f * dt * __builtin_bit_cast(float, ce << 23);
    lptr tb = smem + w * TB_CH + lane * 8, tr = smem + w * TB_CH + 16 * g, kr = smem + KR_OFF + w * KR_CH;
#define PUT(buf, e, v) ST(u32x2, tb + (buf) * TB_BUF + (e) * TB_ROW) = (u32x2){pk_f16(v[0]), pk_f16(v[1])}
#define CONJ(v) ((f32x2){(v).x, -(v).y})
#define GETF(f, buf, row) do { lptr p_ = tr + (buf) * TB_BUF + (row) * TB_ROW; \
      _Pragma("unroll") for (int ks = 0; ks < 8; ++ks) f[ks] = LD(h16x8, p_ + 64 * ks); } while (0)
#define TILE(d, f) do { d = (f32x4){0, 0, 0, 0}; _Pragma("unroll") for (int ks = 0; ks < 8; ++ks) d = MFMA16H(f[ks], fx[ks], d); } while (0)
    f32x2 y[17][2], wB[2], w16[2], w32[2], xa[2], xb[2], aS[6][2], t[2], t2[2];
#pragma unroll
    for (int m = 0; m < 2; ++m) {
      y[1][m] = CONJ(cexp_p(al[m], th[m], 1.f)); w16[m] = cexp_p(al[m], th[m], 16.f); wB[m] = CONJ(cexp_p(al[m], th[m], 256.f));
      y[16][m] = CONJ(w16[m]);
    }
    xa[0] = (f32x2){bf_lo(cre2) * cinv, bf_lo(cim2) * cinv}; xa[1] = (f32x2){bf_hi(cre2) * cinv, bf_hi(cim2) * cinv};
    ST(u32x2, tb + TB_BUF) = (u32x2){0x3c00u, 0x3c00u};
    PUT(0, 0, xa); PUT(1, 1, y[1]);
    cmul4(xb, xa, w16, w32, w16, w16); cmul4(y[2], y[1], y[1], aS[0], wB, wB);
    PUT(0, 1, xb); PUT(1, 2, y[2]);
#pragma unroll
    for (int i = 1; i < 8; ++i) {
      cmul4(xa, xa, w32, xb, xb, w32);
      if (i < 7) cmul4(y[2 * i + 1], y[2 * i - 1], y[2], y[2 * i + 2], y[2 * i], y[2]); else cmul2(y[15], y[13], y[2]);
      PUT(0, 2 * i, xa); PUT(0, 2 * i + 1, xb); PUT(1, 2 * i + 1, y[2 * i + 1]);
      if (i < 7) PUT(1, 2 * i + 2, y[2 * i + 2]);
    }
    STAMP(2);
    h16x8 fx[8], fa[8], fb[8];
    f32x4 kd;
    const int gq = l15 >> 2, r3 = min(l15 & 3, 2), yk = 2044 - 16 * l15 - 4 * g;
    SCHED();
    GETF(fx, 0, l15); GETF(fa, 1, l15);
    SCHED();
    PUT(0, 0, wB);
#pragma unroll
    for (int m = 1; m < 9; m += 2) {
      cmul4(t, wB, y[2 * m], t2, wB, y[2 * m + 2]);
      if (m == 1) cmul4(aS[1], aS[0], wB, aS[2], aS[0], aS[0]);
      if (m == 3) cmul4(aS[3], aS[2], wB, aS[4], aS[2], aS[0]);
      if (m == 5) cmul2(aS[5], aS[4], wB);
      PUT(0, m, t); PUT(0, m + 1, t2);
    }
    SCHED();
    GETF(fb, 0, (int)((0x8766654443222100ull >> (4 * l15)) & 15));
    TILE(kd, fa);
    SCHED();
#pragma unroll
    for (int s = 0; s < 2; ++s) {
      PUT(1, 5 * s, aS[s]);
#pragma unroll
      for (int m = 1; m < 5; m += 2) { cmul4(t, aS[s], y[4 * m], t2, aS[s], y[4 * m + 4]); PUT(1, 5 * s + m, t); PUT(1, 5 * s + m + 1, t2); }
    }
    {
      const f32x4 k0 = kd * kscale;
      const u32 p32 = pk_bf16(k0[3], k0[2]), p10 = pk_bf16(k0[1], k0[0]);
      ST(u32x2, kr + 2 * yk) = (u32x2){p32, p10};
      ST(u16, kr + KR_CP + 2 * yk + 2) = (u16)p32;
      ST(u32, kr + KR_CP + 2 * yk + 4) = pk_bf16(k0[2], k0[1]);
      ST(u16, kr + KR_CP + 2 * yk + 8) = (u16)(p10 >> 16);
    }
    SCHED();
    GETF(fa, 1, 5 * (gq >> 1) + 2 * (gq & 1) + r3);
    TILE(kd, fb);
    SCHED();
#pragma unroll
    for (int s = 2; s < 6; ++s) {
      cmul4(t, aS[s], y[8], t2, aS[s], y[16]);
      PUT(0, 3 * (s - 2), aS[s]); PUT(0, 3 * (s - 2) + 1, t); PUT(0, 3 * (s - 2) + 2, t2);
    }
    {
      const f32x4 k1 = kd * kscale;
      const u32 q10 = pk_bf16(k1[1], k1[0]);
      ST(u32x2, kr + 2 * (yk - 256)) = (u32x2){pk_bf16(k1[3], k1[2]), q10};
      ST(u32x2, kr + KR_CP + 2 * (yk - 256)) = (u32x2){pk_bf16(k1[3], k1[3]), pk_bf16(k1[2], k1[1])};
      if (lane == 0) ST(u16, kr + KR_CP + 2 * 1792) = (u16)q10;
    }
    SCHED();
    GETF(fb, 0, 3 * gq + r3);
    TILE(kd, fa);
    STAMP(3);
    {
      const f32x4 k0 = kd * kscale;
      const int y2 = 2040 - (512 + 256 * (g >> 1) + 16 * l15 + 8 * (g & 1));
      const u32 b2 = pk_bf16(k0[2], k0[2]), b1 = pk_bf16(k0[1], k0[1]), b0 = pk_bf16(k0[0], k0[0]);
      ST(u32x4, kr + 2 * y2) = (u32x4){b2, b1, pk_bf16(k0[1], k0[0]), b0};
      ST(u32x4, kr + KR_CP + 2 * y2) = (u32x4){b2, pk_bf16(k0[2], k0[1]), b1, b0};
    }
    TILE(kd, fb);
    {
      const f32x4 k1 = kd * kscale;
      const int y3 = 2032 - (1024 + 256 * g + 16 * l15);
      const u32 a2 = pk_bf16(k1[2], k1[2]), a1 = pk_bf16(k1[1], k1[1]), a0 = pk_bf16(k1[0], k1[0]);
      ST(u32x4, kr + 2 * y3) = (u32x4){a2, a2, a1, a1};
      ST(u32x4, kr + 2 * y3 + 16) = (u32x4){a1, pk_bf16(k1[1], k1[0]), a0, a0};
      ST(u32x4, kr + KR_CP + 2 * y3) = (u32x4){a2, a2, pk_bf16(k1[2], k1[1]), a1};
      ST(u32x4, kr + KR_CP + 2 * y3 + 16) = (u32x4){a1, a1, a0, a0};
    }
#undef PUT
#undef GETF
#undef TILE
    SCHED();
    __builtin_amdgcn_s_setprio(0);
    WAIT_L0(); SCHED();
#pragma unroll
    for (int j = 0; j < WOE; ++j) fetch_wo(wo, ct, ch, j, lane, smem);
    STAMP(4);
  } else {
    const int t2 = (tid + 16 * cg) & 255;
    if (lane < 30) ST(u32x4, smem + U_OFF + ch * U_CH + lane * 16) = (u32x4){0u, 0u, 0u, 0u};
    STAMP(1);
#pragma unroll
    for (int hf = 0; hf < 2; ++hf) {
      lptr ub = smem + U_OFF + 480 + t2 * 8 + hf * 2048;
      if (hf == 0) asm volatile("s_waitcnt vmcnt(4)" : "+v"(xr[0]), "+v"(xr[1]), "+v"(xr[2]), "+v"(xr[3]) : "v"(pf));
      else asm volatile("s_waitcnt vmcnt(0)" : "+v"(xr[4]), "+v"(xr[5]), "+v"(xr[6]), "+v"(xr[7]));
      const u32x2 x0 = xr[4 * hf], x1 = xr[4 * hf + 1], x2 = xr[4 * hf + 2], x3 = xr[4 * hf + 3];
      ST(u32x2, ub + 0 * U_CH) = (u32x2){__builtin_amdgcn_perm(x1.x, x0.x, 0x05040100u), __builtin_amdgcn_perm(x3.x, x2.x, 0x05040100u)};
      ST(u32x2, ub + 1 * U_CH) = (u32x2){__builtin_amdgcn_perm(x1.x, x0.x, 0x07060302u), __builtin_amdgcn_perm(x3.x, x2.x, 0x07060302u)};
      ST(u32x2, ub + 2 * U_CH) = (u32x2){__builtin_amdgcn_perm(x1.y, x0.y, 0x05040100u), __builtin_amdgcn_perm(x3.y, x2.y, 0x05040100u)};
      ST(u32x2, ub + 3 * U_CH) = (u32x2){__builtin_amdgcn_perm(x1.y, x0.y, 0x07060302u), __builtin_amdgcn_perm(x3.y, x2.y, 0x07060302u)};
      SCHED();
    }
    STAMP(4);
  }
  lptr ka[8];
  {
    const int yb = 2031 - l15 + 8 * g - 128 * half - 96 - 1792;
    lptr k7 = smem + KR_OFF + ch * KR_CH + (yb & 1) * KR_CP + ((yb + 1) >> 1) * 4;
#pragma unroll
    for (int n = 0; n < 8; ++n) { ka[n] = k7 + 512 * (7 - n); asm volatile("" : "+v"(ka[n])); }
  }
  lptr ua = smem + U_OFF + ch * U_CH + (8 * g + 16 * l15 + 224 - 128 * half - 96) * 2;
  asm volatile("" : "+v"(ua));
  STAMP(6);
  LBAR();
  STAMP(7); CYC(20);
  {
    u32x4 A0[8], A1[8]; s16x8 B0[8], B1[8];
#define LOADC(A, B, rr) do { _Pragma("unroll") for (int n = 0; n < 8; ++n) { \
      lptr kp = ka[n] + 64 * (3 - (rr)); \
      A[n] = (u32x4){LD(u32, kp), LD(u32, kp + 4), LD(u32, kp + 8), LD(u32, kp + 12)}; \
      B[n] = LD(s16x8, ua + 64 * (3 - (rr)) + 512 * n); } } while (0)
#define MMC(A, B) do { _Pragma("unroll") for (int nu = 0; nu < 8; ++nu) _Pragma("unroll") for (int mu = 0; mu < 8 - nu; ++mu) \
      acc[nu + mu] = MFMA16(__builtin_bit_cast(s16x8, A[nu]), B[mu], acc[nu + mu]); } while (0)
#define MIXC() do { _Pragma("unroll") for (int q = 0; q < 12; ++q) { __builtin_amdgcn_sched_group_barrier(0x8, MIXM, 0); __builtin_amdgcn_sched_group_barrier(0x100, 2, 0); } \
      __builtin_amdgcn_sched_group_barrier(0x8, 36 - 12 * MIXM, 0); } while (0)
    LOADC(A0, B0, 0);
    SCHED();
    dsk2 = *(const u32*)(dsk + (c & ~1));
    epoch = load_epoch(ws);
    if (!half) {
#pragma unroll
      for (int r = 0; r < 4; ++r) gx[r] = __builtin_amdgcn_raw_buffer_load_b128(rx, lane * 16 + 1024 * r, (16 * cg + 4 * ch) * 1024, 0);
      gwv = *(const u32x4*)(gw + 8 * lane);
      pv = touch_pages(ws, out, rt, lane);
    }
    SCHED();
#define WOP(j) do { if (!half && (j) >= WOE) fetch_wo(wo, ct, ch, j, lane, smem); SCHED(); } while (0)
    WOP(0); WAIT_L0(); SCHED(); LOADC(A1, B1, 1); MMC(A0, B0); MIXC(); PINC();
    WOP(1); WAIT_L0(); SCHED(); LOADC(A0, B0, 2); MMC(A1, B1); MIXC(); PINC();
    WOP(2); WAIT_L0(); SCHED(); LOADC(A1, B1, 3); MMC(A0, B0); MIXC(); PINC();
    WOP(3); WAIT_L0(); SCHED(); MMC(A1, B1); PINC();
#undef WOP
#undef LOADC
#undef MMC
#undef MIXC
#undef PINC
  }
  STAMP(8); CYC(21); CYCD(20, 21);
  {
    lptr ex = smem + EX_OFF + w * 4096 + lane * 16, ei = smem + EX_OFF + (w ^ 4) * 4096 + lane * 16;
    f32x4 keep[4];
    if (half) {
#pragma unroll
      for (int i = 0; i < 4; ++i) { ST(f32x4, ex + 1024 * i) = acc[4 + i]; keep[i] = acc[i]; }
    } else {
#pragma unroll
      for (int i = 0; i < 4; ++i) { ST(f32x4, ex + 1024 * i) = acc[i]; keep[i] = acc[4 + i]; }
    }
    lptr up = smem + U_OFF + ch * U_CH + 480 + 2 * (16 * l15 + 4 * g) + 2048 * (1 - half);
    const float dsk_f = (c & 1) ? bf_hi(dsk2) : bf_lo(dsk2);
#pragma unroll
    for (int i = 0; i < 4; ++i) {
      const u32x2 uu = LD(u32x2, up + 512 * i);
      keep[i] += (f32x4){bf_lo(uu.x), bf_hi(uu.x), bf_lo(uu.y), bf_hi(uu.y)} * dsk_f;
    }
    SCHED();
    if (!half) {
      asm volatile("" :: "v"(pv));
      float sg[4];
#pragma unroll
      for (int r = 0; r < 4; ++r) {
        float s0 = 0.f;
#pragma unroll
        for (int i = 0; i < 4; ++i) s0 = __builtin_amdgcn_fdot2_f32_bf16(u2b(gx[r][i]), u2b(gwv[i]), s0, false);
        sg[r] = rbf(__builtin_amdgcn_rcpf(1.f + __builtin_amdgcn_exp2f(-LOG2E * rbf(wave_sum(s0)))));
      }
      if (lane == 63) store16(__builtin_bit_cast(u32x4, (f32x4){sg[0], sg[1], sg[2], sg[3]}), rws, (int)WS_G + (16 * cg + 4 * ch) * 4 + (lane - 63));
    }
    const int bo = (int)WS_BT + c * BTP + 2048 * (1 - half) + 32 * l15 + 16 * (g >> 1) + 512 * (g & 1);
    SCHED();
    LBAR();
    f32x4 o4[4];
#pragma unroll
    for (int i = 0; i < 4; ++i) o4[i] = LD(f32x4, ei + 1024 * i);
    SCHED();
#pragma unroll
    for (int i = 0; i < 2; ++i) {
      const f32x4 y0 = keep[2 * i] + o4[2 * i], y1 = keep[2 * i + 1] + o4[2 * i + 1];
      const auto sx = __builtin_amdgcn_permlane16_swap(pk_bf16(y0[0], y0[1]), pk_bf16(y1[0], y1[1]), false, false);
      const auto sy = __builtin_amdgcn_permlane16_swap(pk_bf16(y0[2], y0[3]), pk_bf16(y1[2], y1[3]), false, false);
      store16((u32x4){sx[0], sy[0], sx[1], sy[1]}, rws, bo + 1024 * i);
    }
    asm volatile(".Lfused_code_tail:");
  }
  STAMP(9);
}

struct Tags { u64 a, b; };
DI Tags poll_load(char* ws, int off, int lane) {
  gu64* p = (gu64*)(ws + WS_FLAG + off) + lane;
  return Tags{__hip_atomic_load(p, RLX_AGENT), __hip_atomic_load(p + 64, RLX_AGENT)};
}
DI void poll_wait(char* ws, int off, int lane, u64 tag, Tags& f, u64 ma, u64 mb) {
#pragma unroll 1
  for (int spin = 0; spin < (1 << 20); ++spin) {
    if ((__ballot(f.a != tag) & ma) == 0 && (__ballot(f.b != tag) & mb) == 0) break;
    __builtin_amdgcn_s_sleep(POLL_SLEEP);
    f = poll_load(ws, off, lane);
    __builtin_amdgcn_s_waitcnt(0x0F70);
  }
}

DI void out_role(char* __restrict__ ws, u16* __restrict__ out, int rt, int ct, u64 tag, Tags fl, lptr smem STAMP_ARG) {
  const int tid = threadIdx.x, lane = tid & 63;
  const int w = __builtin_amdgcn_readfirstlane(tid >> 6);
  const int th = w & 1, kq = w >> 1, hh = lane >> 5, l31 = lane & 31;
  const rsrc_t rws = mk_rsrc(ws, WS_USED);
  const int foff = (blockIdx.x & (NREP - 1)) * FPITCH;
  if (w == 0) poll_wait(ws, foff, lane, tag, fl, ~0ull, ~0ull);
  RAW_BAR();
  STAMP(11);
  if (w == 1) fl = poll_load(ws, foff + 1024, lane);
  f32x4 kvr[KVC][5];
#pragma unroll
  for (int cp = 0; cp < KVC; ++cp) {
#pragma unroll
    for (int i = 0; i < 4; ++i) kvr[cp][i] = __builtin_bit_cast(f32x4, __builtin_amdgcn_raw_buffer_load_b128(rws, tid * 16, (int)WS_KV + cp * KV_CP + 8192 * i, 16));
    kvr[cp][4] = __builtin_bit_cast(f32x4, __builtin_amdgcn_raw_buffer_load_b128(rws, (tid & 127) * 16, (int)WS_KV + cp * KV_CP + 32768, 16));
  }
  SCHED(); RAW_BAR(); SCHED();
  u32x4 pf[8];
#define LOAD_PQ(i0, i1) do { _Pragma("unroll") for (int i = i0; i < i1; ++i) \
    pf[i] = __builtin_amdgcn_raw_buffer_load_b128(rws, lane * 16, (int)WS_PQ + (2 * rt + th) * PQP + (8 * kq + i) * 1024, 16); } while (0)
  LOAD_PQ(0, 3);
  STAMP(12);
#pragma unroll
  for (int i = 0; i < 5; ++i) {
    f32x4 v = kvr[0][i];
#pragma unroll
    for (int cp = 1; cp < KVC; ++cp) v += kvr[cp][i];
    ST(u32x2, smem + P2_KV + (i < 4 ? tid + 512 * i : 2048 + (tid & 127)) * 8) = (u32x2){pk_bf16(v[0], v[1]), pk_bf16(v[2], v[3])};
  }
  f32x16 acc[2];
#pragma unroll
  for (int i = 0; i < 16; ++i) { acc[0][i] = 0.f; acc[1][i] = 0.f; }
  f32x16 cinit = acc[0];
  cinit[8] = EPS;
  lptr kva = smem + P2_KV + 8 * kq * 544 + min(l31, 16) * 32 + hh * 16;
  const int blk = (lane >> 4) & 1, q4 = (lane & 15) >> 2, p4 = lane & 3;
  const int sw = ((q4 >> 1) & 1) << 6;
  lptr wa0 = smem + P2_WO + kq * WOQ + (4 * hh + q4) * 128 + blk * 32 + p4 * 8 + sw;
  lptr wa1 = smem + P2_WO + kq * WOQ + (4 * hh + q4) * 128 + blk * 32 + p4 * 8 + (64 ^ sw);
  STAMP(13);
  const u64 mct = 0xffffull << (16 * ct & 63), mrt = 0xfull << (4 * rt & 63);
  const u64 ma = (ct < 4 ? mct : 0ull) | (rt < 16 ? mrt : 0ull), mb = (ct < 4 ? 0ull : mct) | (rt < 16 ? 0ull : mrt);
  if (w == 1) { const u32 ok = (__ballot(fl.a != tag) & ma) == 0 && (__ballot(fl.b != tag) & mb) == 0; if (lane == 0) ST(u32, smem + P2_OK) = ok; }
  LBAR();
  LOAD_PQ(3, 8);
#undef LOAD_PQ
  const bool early = __builtin_amdgcn_readfirstlane(LD(u32, smem + P2_OK)) != 0;
  u32x4 btr; float gt;
#define LOAD_B() do { \
    btr = __builtin_amdgcn_raw_buffer_load_b128(rws, (lane >> 2) * BTP + (lane & 3) * 16, (int)WS_BT + (64 * ct + 16 * kq) * BTP + 128 * rt + 64 * th, 16); \
    gt = u2f(__builtin_amdgcn_raw_buffer_load_b32(rws, l31 * 4, (int)WS_G + (64 * rt + 32 * th) * 4, 16)); } while (0)
  if (early) LOAD_B();
  u32x2 bb[2];
  lptr ba = smem + P2_BT + w * 1024 + (4 * hh + q4) * 64 + blk * 32 + p4 * 8;
#define TURN_B() do { ST(u32x4, smem + P2_BT + w * 1024 + lane * 16) = btr;        \
    bb[0] = __builtin_bit_cast(u32x2, tr_read(ba)); bb[1] = __builtin_bit_cast(u32x2, tr_read(ba + 512)); } while (0)
  STAMP(14);
  {
    f32x16 d[2]; s16x8 kf[8], wf[2][2]; u32x4 of[2];
#define OMM(i) do { d[(i) & 1] = MFMA32(kf[i], __builtin_bit_cast(s16x8, pf[i]), cinit); asm volatile("" : "+v"(d[(i) & 1])); } while (0)
#define NORM(i) do { const f32x16& dd = d[(i) & 1]; const float inv = __builtin_amdgcn_rcpf(dd[8]); \
      of[(i) & 1] = (u32x4){pk_bf16(dd[0] * inv, dd[1] * inv), pk_bf16(dd[2] * inv, dd[3] * inv), pk_bf16(dd[4] * inv, dd[5] * inv), pk_bf16(dd[6] * inv, dd[7] * inv)}; \
      asm volatile("" : "+v"(of[(i) & 1]) : "v"(dd)); } while (0)
#define LDW(i) do { wf[(i) & 1][0] = cat8(tr_read(wa0 + (i) * 2048), tr_read(wa0 + (i) * 2048 + 1024)); \
      wf[(i) & 1][1] = cat8(tr_read(wa1 + (i) * 2048), tr_read(wa1 + (i) * 2048 + 1024)); } while (0)
#define AMM(i) do { acc[0] = MFMA32(wf[(i) & 1][0], __builtin_bit_cast(s16x8, of[(i) & 1]), acc[0]); \
      acc[1] = MFMA32(wf[(i) & 1][1], __builtin_bit_cast(s16x8, of[(i) & 1]), acc[1]); asm volatile("" : "+v"(acc[0]), "+v"(acc[1])); } while (0)
#pragma unroll
    for (int i = 0; i < 8; ++i) kf[i] = LD(s16x8, kva + i * 544);
    LDW(0);
    SCHED();
    OMM(0); OMM(1); SCHED(); NORM(0); SCHED();
#pragma unroll
    for (int i = 0; i < 8; ++i) {
      if (i + 1 < 8) LDW(i + 1);
      SCHED();
      AMM(i);
      if (i + 2 < 8) OMM(i + 2);
      SCHED();
      if (i + 1 < 8) NORM(i + 1);
      SCHED();
      if (i == TURN_AT && early) { TURN_B(); SCHED(); }
    }
#undef OMM
#undef NORM
#undef LDW
#undef AMM
  }
  STAMP(15);
  f32x4 own[2];
#pragma unroll
  for (int q = 0; q < 4; ++q) {
    lptr ex = smem + P2_EX + ((2 * q + th) * 4 + kq) * 2048 + lane * 16;
    const f32x16& s = acc[q >> 1];
    const f32x4 s0 = {s[8 * (q & 1)], s[8 * (q & 1) + 1], s[8 * (q & 1) + 2], s[8 * (q & 1) + 3]};
    const f32x4 s1 = {s[8 * (q & 1) + 4], s[8 * (q & 1) + 5], s[8 * (q & 1) + 6], s[8 * (q & 1) + 7]};
    if (q == kq) { own[0] = s0; own[1] = s1; } else { ST(f32x4, ex) = s0; ST(f32x4, ex + 1024) = s1; }
  }
  if (!early && w == 1) poll_wait(ws, foff + 1024, lane, tag, fl, ma, mb);
  LBAR();
  if (!early) { LOAD_B(); TURN_B(); }
#undef TURN_B
#undef LOAD_B
  STAMP(16);
  lptr ei = smem + P2_EX + w * 8192 + lane * 16;
  const float g1 = rbf(1.f - gt);
  u32x2 res[2];
#pragma unroll
  for (int i = 0; i < 2; ++i) {
    const f32x4 o = (own[i] + LD(f32x4, ei + ((kq ^ 1) * 2048 + 1024 * i))) + (LD(f32x4, ei + ((kq ^ 2) * 2048 + 1024 * i)) + LD(f32x4, ei + ((kq ^ 3) * 2048 + 1024 * i)));
    res[i] = (u32x2){pk_bf16(gt * o[0] + g1 * bf_lo(bb[i].x), gt * o[1] + g1 * bf_hi(bb[i].x)), pk_bf16(gt * o[2] + g1 * bf_lo(bb[i].y), gt * o[3] + g1 * bf_hi(bb[i].y))};
  }
  const auto sx = __builtin_amdgcn_permlane32_swap(res[0].x, res[1].x, false, false);
  const auto sy = __builtin_amdgcn_permlane32_swap(res[0].y, res[1].y, false, false);
  *(u32x4*)(out + (size_t)(64 * rt + 32 * th + l31) * DM + 64 * ct + 16 * kq + 8 * hh) = (u32x4){sx[0], sy[0], sx[1], sy[1]};
  STAMP(17);
  if (blockIdx.x == 0 && w == 1) {
    poll_wait(ws, foff + 1024, lane, tag, fl, ~0ull, ~0ull);
    if (lane == 0) __hip_atomic_store((gu64*)(ws + WS_SYNC), tag, RLX_AGENT);
  }
}

__global__ __launch_bounds__(512, 2) void fused(const u16* __restrict__ x, const u16* __restrict__ wqkv, const u16* __restrict__ wo,
                                                const u16* __restrict__ lar, const u16* __restrict__ aim, const u16* __restrict__ cre,
                                                const u16* __restrict__ cim, const u16* __restrict__ ldt, const u16* __restrict__ dsk,
                                                const u16* __restrict__ gw, char* __restrict__ ws, u16* __restrict__ out) {
  extern __shared__ __attribute__((aligned(16))) char smem_[];
  lptr smem = (lptr)smem_;
  STAMP_DECL; STAMP(0);
  asm volatile("" :: "s"(x), "s"(wqkv), "s"(wo), "s"(lar), "s"(aim), "s"(cre), "s"(cim), "s"(ldt), "s"(dsk), "s"(gw), "s"(ws), "s"(out));
  STAMP(18);
  const int b = blockIdx.x, xcd = b & 7, slot = b >> 3, role = (b >> ROLEBIT) & 1;
  const int idx = ((b >> (ROLEBIT + 1)) << (ROLEBIT - 3)) | (slot & ((1 << (ROLEBIT - 3)) - 1));
  const int rt = 8 * (xcd >> 1) + (slot & 7), ct = 4 * (xcd & 1) + (slot >> 3);
  const int tt = 4 * (xcd >> 1) + (idx & 3), hg = 4 * (xcd & 1) + (idx >> 2);
  const int cg = xcd * 16 + idx;
  int pofs = (threadIdx.x & 63) * 8;
  asm volatile("" : "+v"(pofs));
  u64 epoch; u32 tmp0, tmp1, tmp2; u64 pc;
  u32 pr[5]; u32x2 xr[8];
  asm volatile(
      "s_bitcmp1_b32 %[b], %[rb]\n\t"
      "s_cbranch_scc0 .Lpre_end%=\n\t"
      "s_cmp_gt_u32 %[wv], 3\n\t"
      "s_cbranch_scc1 .Lpre_b%=\n\t"
      "v_lshlrev_b32 %[p4], 2, %[tid]\n\t"
      "v_lshl_add_u32 %[p4], %[cg], 10, %[p4]\n\t"
      "global_load_dword %[p0], %[p4], %[lar]\n\t"
      "global_load_dword %[p1], %[p4], %[aim]\n\t"
      "global_load_dword %[p2], %[p4], %[cre]\n\t"
      "global_load_dword %[p3], %[p4], %[cim]\n\t"
      "v_lshrrev_b32 %[t0], 7, %[p4]\n\t"
      "v_and_b32 %[t0], 0x3fe, %[t0]\n\t"
      "global_load_ushort %[p4], %[t0], %[ldt]\n\t"
      "s_barrier\n\t"
      "s_branch .Lpre_end%=\n"
      ".Lpre_b%=:\n\t"
      "s_getpc_b64 %[pc]\n\t"
      "v_and_b32 %[t0], 0xff, %[tid]\n\t"
      "v_lshlrev_b32 %[t0], 7, %[t0]\n\t"
      "v_mov_b32 %[t1], .Lfused_code_tail-.Lpre_b%=-4\n\t"
      "v_med3_i32 %[t0], 0, %[t0], %[t1]\n\t"
      "global_load_dword %[t2], %[t0], %[pc]\n\t"
      "v_lshl_add_u32 %[t0], %[cg], 4, %[tid]\n\t"
      "v_and_b32 %[t0], 0xff, %[t0]\n\t"
      "v_lshlrev_b32 %[t0], 12, %[t0]\n\t"
      "v_lshl_add_u32 %[t0], %[cg], 3, %[t0]\n\t"
      "v_add_u32 %[t1], 0x100000, %[t0]\n\t"
      "s_barrier\n\t"
      "global_load_dwordx2 %[x0], %[t0], %[x]\n\t"
      "global_load_dwordx2 %[x1], %[t0], %[x] offset:1024\n\t"
      "global_load_dwordx2 %[x2], %[t0], %[x] offset:2048\n\t"
      "global_load_dwordx2 %[x3], %[t0], %[x] offset:3072\n\t"
      "global_load_dwordx2 %[x4], %[t1], %[x]\n\t"
      "global_load_dwordx2 %[x5], %[t1], %[x] offset:1024\n\t"
      "global_load_dwordx2 %[x6], %[t1], %[x] offset:2048\n\t"
      "global_load_dwordx2 %[x7], %[t1], %[x] offset:3072\n"
      ".Lpre_end%=:"
      : [p0] "=&v"(pr[0]), [p1] "=&v"(pr[1]), [p2] "=&v"(pr[2]), [p3] "=&v"(pr[3]), [p4] "=&v"(pr[4]),
        [x0] "=&v"(xr[0]), [x1] "=&v"(xr[1]), [x2] "=&v"(xr[2]), [x3] "=&v"(xr[3]), [x4] "=&v"(xr[4]), [x5] "=&v"(xr[5]), [x6] "=&v"(xr[6]), [x7] "=&v"(xr[7]),
        [t0] "=&v"(tmp0), [t1] "=&v"(tmp1), [t2] "=&v"(tmp2), [pc] "=&s"(pc)
      : [b] "s"(b), [rb] "i"(ROLEBIT), [wv] "s"(__builtin_amdgcn_readfirstlane(threadIdx.x >> 6)), [tid] "v"(threadIdx.x), [cg] "s"(cg),
        [lar] "s"(lar), [aim] "s"(aim), [cre] "s"(cre), [cim] "s"(cim), [ldt] "s"(ldt), [x] "s"(x)
      : "scc", "memory");
  if (__builtin_expect(role, 0)) conv_role(x, pr, xr, tmp2, dsk, gw, wo, ws, out, rt, cg, ct, epoch, smem STAMP_PASS);
  else gemm_role(x, wqkv, wo, ct, ws, out, rt, tt, hg, epoch, smem STAMP_PASS);
  const int lane = threadIdx.x & 63, w = __builtin_amdgcn_readfirstlane(threadIdx.x >> 6);
  const u64 tag = next_tag(epoch);
  Tags fl = {0, 0};
  if (w == 0) {
    const rsrc_t rf = mk_rsrc(ws, WS_USED);
    fl.a = __builtin_bit_cast(u64, __builtin_amdgcn_raw_buffer_load_b64(rf, pofs, (int)WS_FLAG + (b & (NREP - 1)) * FPITCH, 16));
    fl.b = __builtin_bit_cast(u64, __builtin_amdgcn_raw_buffer_load_b64(rf, pofs, (int)WS_FLAG + (b & (NREP - 1)) * FPITCH + 512, 16));
  }
  SCHED();
  WAIT_VM(0);
  __builtin_amdgcn_s_setprio(0);
  STAMP(10);
  LBAR();
  if (w == 7 && lane < NREP) __hip_atomic_store((gu64*)(ws + WS_FLAG + lane * FPITCH) + (role ? 128 + cg : tt * 8 + hg), tag, RLX_AGENT);
  out_role(ws, out, rt, ct, tag, fl, smem STAMP_PASS);
  STAMP_FLUSH(ws);
}

extern "C" void kernel_launch(void* const* d_in, const int* in_sizes, int n_in, void* d_out, int out_size, void* d_ws,
                              size_t ws_size, hipStream_t stream) {
  (void)in_sizes; (void)n_in; (void)out_size; (void)ws_size;
  (void)hipFuncSetAttribute((const void*)fused, hipFuncAttributeMaxDynamicSharedMemorySize, 163840);
  fused<<<256, 512, 163840, stream>>>((const u16*)d_in[0], (const u16*)d_in[1], (const u16*)d_in[2], (const u16*)d_in[4],
                                      (const u16*)d_in[5], (const u16*)d_in[6], (const u16*)d_in[7], (const u16*)d_in[8],
                                      (const u16*)d_in[9], (const u16*)d_in[10], (char*)d_ws + WS_BASE, (u16*)d_out);
}
```

```cpp
#include <hip/hip_runtime.h>
#include <hip/hip_bf16.h>
#include <stdint.h>

typedef unsigned int u32;
typedef unsigned short u16;
typedef __attribute__((ext_vector_type(8))) short s16x8;
typedef __attribute__((ext_vector_type(4))) short s16x4;
typedef __attribute__((ext_vector_type(8))) _Float16 h16x8;
typedef __attribute__((ext_vector_type(2))) _Float16 h16x2;
typedef __attribute__((ext_vector_type(2))) __bf16 b16x2;
typedef __attribute__((ext_vector_type(2))) float f32x2;
typedef __attribute__((ext_vector_type(4))) float f32x4;
typedef __attribute__((ext_vector_type(16))) float f32x16;
typedef __attribute__((ext_vector_type(2))) u32 u32x2;
typedef __attribute__((ext_vector_type(4))) u32 u32x4;
#define LDS_AS __attribute__((address_space(3)))
#define DI __device__ __forceinline__

static constexpr int SEQ = 2048, DM = 512;
static constexpr size_t WS_KV = (6u << 20) + 131072;
static constexpr size_t WS_G = 65536;
static constexpr size_t WS_PQ = 1u << 20;
static constexpr size_t WS_BT = (4u << 20) + 4096;
static constexpr int WS_USED = 8 << 20;
static constexpr size_t WS_SYNC = 49152;
static constexpr size_t WS_FLAG = 131072;
static constexpr int BTP = 4224, PQP = 32768, NREP = 64, FPITCH = 2048;
typedef unsigned long long u64;
typedef __attribute__((address_space(1))) u64 gu64;
typedef __attribute__((address_space(1))) u32 gu32;
#define RLX_AGENT __ATOMIC_RELAXED, __HIP_MEMORY_SCOPE_AGENT
__device__ __forceinline__ u64 next_tag(u64 e) { return e * 6364136223846793005ull + 1442695040888963407ull; }

#define WAIT_VM(n) asm volatile("s_waitcnt vmcnt(" #n ")" ::: "memory")
#define WAIT_L0() __builtin_amdgcn_s_waitcnt(0xC07F)
#define RAW_BAR() __builtin_amdgcn_s_barrier()
#define SCHED() __builtin_amdgcn_sched_barrier(0)
#define LBAR() do { WAIT_L0(); asm volatile("" ::: "memory"); RAW_BAR(); asm volatile("" ::: "memory"); } while (0)
typedef LDS_AS char* lptr;
#define LD(T, p) (*(const LDS_AS T*)(p))
#define ST(T, p) (*(LDS_AS T*)(p))

DI u32 pk_bf16(float a, float b) { return __builtin_bit_cast(u32, __builtin_convertvector((f32x2){a, b}, b16x2)); }
DI u32 pk_f16(f32x2 v) { return __builtin_bit_cast(u32, __builtin_convertvector(v, h16x2)); }
DI float bf_lo(u32 v) { return __builtin_bit_cast(float, v << 16); }
DI float bf_hi(u32 v) { return __builtin_bit_cast(float, v & 0xffff0000u); }
DI float u2f(u32 v) { return __builtin_bit_cast(float, v); }
DI b16x2 u2b(u32 v) { return __builtin_bit_cast(b16x2, v); }
DI float rbf(float v) { return bf_lo(pk_bf16(v, v)); }
static constexpr float LOG2E = 1.44269504088896341f, EPS = 1e-6f;
DI float phi_f(float t) {
  const float hi = __builtin_bit_cast(float, max(__builtin_bit_cast(int, t), __builtin_bit_cast(int, EPS)));
  return hi + __builtin_amdgcn_fmed3f(__builtin_amdgcn_exp2f(__builtin_fmaf(t, LOG2E, -EPS * LOG2E)), 0.f, 1.f);
}
DI float exp_f(float v) {
  const float p = v * LOG2E;
  const float r = __builtin_fmaf(v, 1.925963033500011e-8f, __builtin_fmaf(v, LOG2E, -p));
  return __builtin_amdgcn_exp2f(p) * __builtin_fmaf(r, 0.6931471805599453f, 1.f);
}
#define DPP_F(v, ctrl, rmask, bc) __builtin_bit_cast(float, __builtin_amdgcn_update_dpp(0, __builtin_bit_cast(int, v), ctrl, rmask, 0xf, bc))
DI float wave_sum(float r) {
  r += DPP_F(r, 0x111, 0xf, true); r += DPP_F(r, 0x112, 0xf, true); r += DPP_F(r, 0x114, 0xf, true); r += DPP_F(r, 0x118, 0xf, true);
  r += DPP_F(r, 0x142, 0xa, false); r += DPP_F(r, 0x143, 0xc, false);
  return r;
}
DI u32 wave_umax(u32 r) {
#define DPP_U(v, ctrl, rmask, bc) (u32)__builtin_amdgcn_update_dpp(0, (int)(v), ctrl, rmask, 0xf, bc)
  r = max(r, DPP_U(r, 0x111, 0xf, true)); r = max(r, DPP_U(r, 0x112, 0xf, true)); r = max(r, DPP_U(r, 0x114, 0xf, true));
  r = max(r, DPP_U(r, 0x118, 0xf, true)); r = max(r, DPP_U(r, 0x142, 0xa, false)); r = max(r, DPP_U(r, 0x143, 0xc, false));
  return (u32)__builtin_amdgcn_readlane((int)r, 63);
}
DI s16x4 tr_read(lptr p) { return __builtin_amdgcn_ds_read_tr16_b64_v4i16((LDS_AS s16x4*)p); }
DI s16x8 cat8(s16x4 a, s16x4 b) { return __builtin_shufflevector(a, b, 0, 1, 2, 3, 4, 5, 6, 7); }
typedef __amdgpu_buffer_rsrc_t rsrc_t;
DI rsrc_t mk_rsrc(const void* p, int bytes) { return __builtin_amdgcn_make_buffer_rsrc((void*)p, 0, bytes, 0x00020000); }
DI void store16(u32x4 v, rsrc_t r, int off) { __builtin_amdgcn_raw_buffer_store_b128(v, r, off, 0, 16); }
DI void blds16(rsrc_t r, lptr l, int voff, int soff) { __builtin_amdgcn_raw_ptr_buffer_load_lds(r, (LDS_AS void*)l, 16, voff, soff, 0, 0); }
#define CM_MUL(t, a, w) "v_pk_mul_f32 " t ", " a ", " w " op_sel:[1,1] op_sel_hi:[1,0]\n\t"
#define CM_FMA(r, a, w, t) "v_pk_fma_f32 " r ", " a ", " w ", " t " op_sel_hi:[0,1,1] neg_lo:[0,0,1]\n\t"
DI void cmul2(f32x2 (&r)[2], const f32x2 (&a)[2], const f32x2 (&w)[2]) {
  f32x2 r0, r1;
  asm(CM_MUL("%0", "%2", "%4") CM_MUL("%1", "%3", "%5") CM_FMA("%0", "%2", "%4", "%0") CM_FMA("%1", "%3", "%5", "%1")
      : "=&v"(r0), "=&v"(r1) : "v"(a[0]), "v"(a[1]), "v"(w[0]), "v"(w[1]));
  r[0] = r0; r[1] = r1;
}
DI void cmul4(f32x2 (&r)[2], const f32x2 (&a)[2], const f32x2 (&w)[2], f32x2 (&q)[2], const f32x2 (&b)[2], const f32x2 (&v)[2]) {
  f32x2 r0, r1, q0, q1;
  asm(CM_MUL("%0", "%4", "%6") CM_MUL("%1", "%5", "%7") CM_MUL("%2", "%8", "%10") CM_MUL("%3", "%9", "%11")
      CM_FMA("%0", "%4", "%6", "%0") CM_FMA("%1", "%5", "%7", "%1") CM_FMA("%2", "%8", "%10", "%2") CM_FMA("%3", "%9", "%11", "%3")
      : "=&v"(r0), "=&v"(r1), "=&v"(q0), "=&v"(q1)
      : "v"(a[0]), "v"(a[1]), "v"(w[0]), "v"(w[1]), "v"(b[0]), "v"(b[1]), "v"(v[0]), "v"(v[1]));
  r[0] = r0; r[1] = r1; q[0] = q0; q[1] = q1;
}
DI f32x2 cexp_p(float alpha, float theta, float p) {
  float m = __builtin_amdgcn_exp2f(alpha * p);
  float f = __builtin_amdgcn_fractf(theta * p);
  return (f32x2){m * __builtin_amdgcn_cosf(f), m * __builtin_amdgcn_sinf(f)};
}
#define MFMA16(a, b, c) __builtin_amdgcn_mfma_f32_16x16x32_bf16((a), (b), (c), 0, 0, 0)
#define MFMA16H(a, b, c) __builtin_amdgcn_mfma_f32_16x16x32_f16((a), (b), (c), 0, 0, 0)
#define MFMA32(a, b, c) __builtin_amdgcn_mfma_f32_32x32x16_bf16((a), (b), (c), 0, 0, 0)

static constexpr int STAGE = 40960;
static constexpr int WOQ = 17408;
static constexpr int P2_WO = 0, P2_KV = 4 * WOQ, P2_OK = P2_KV + 17408, P2_BT = P2_KV + 18432, P2_EX = P2_BT + 8192;

DI u64 load_epoch(char* ws) {
  int vzero;
  asm volatile("v_mov_b32 %0, 0" : "=v"(vzero));
  return __hip_atomic_load((gu64*)(ws + WS_SYNC + vzero), RLX_AGENT);
}
DI u32 touch_pages(const char* ws, const u16* out, int rt, int lane) {
  const char* p = lane < 4 ? ws + (size_t)((0xf871u >> (4 * lane)) & 15) * (512u << 10) : (const char*)out + (size_t)rt * 65536;
  return lane < 5 ? *(const u32*)p : 0u;
}
DI void fetch_wo(const u16* __restrict__ wo, int ct, int q, int lane, lptr smem) {
  const rsrc_t rwo = mk_rsrc(wo, DM * DM * 2);
  const int sr = lane >> 3, sp = lane & 7, sc = (sp ^ (((sr >> 1) & 1) << 2)) << 4;
#pragma unroll
  for (int i = 0; i < 16; ++i) blds16(rwo, smem + P2_WO + q * WOQ + 1024 * i, sr * 1024 + sc, (128 * q + 8 * i) * 1024 + 128 * ct);
}

DI void gemm_role(const u16* __restrict__ x, const u16* __restrict__ wqkv, const u16* __restrict__ wo, int ct, char* __restrict__ ws, const u16* out, int rt, int tt, int hg,
                  u64& epoch, lptr smem) {
  const int tid = threadIdx.x, lane = tid & 63;
  const int w = __builtin_amdgcn_readfirstlane(tid >> 6);
  const int wm = w >> 2, wh = w & 3, g = lane >> 4, l15 = lane & 15;
  const int sr = lane >> 3, sp = lane & 7;
  const rsrc_t rx = mk_rsrc(x, SEQ * DM * 2), rw = mk_rsrc(wqkv, DM * 1536 * 2);
  const int xv = sr * 1024 + ((sp ^ sr) << 4), xs0 = (tt * 128 + 16 * w) * 1024;
  const int fw = ((sr >> 1) & 1) | ((w & 1) << 1);
  const int wv = sr * 3072 + ((sp ^ (fw << 1)) << 4), ws0 = 8 * w * 3072 + hg * 128;
  const int ROT = 2 * (tt & 3) + ((tt >> 2) & 1);
  const rsrc_t rwo = mk_rsrc(wo, DM * DM * 2);
  const int ov = sr * 1024 + ((sp ^ (((sr >> 1) & 1) << 2)) << 4);
#define ISS(s, i) do { lptr sb = smem + ((s) & 3) * STAGE; \
    if ((s) >= 8) { if ((i) < 4) blds16(rwo, smem + P2_WO + (w >> 1) * WOQ + (64 * (w & 1) + 32 * ((s) - 8) + 8 * (i)) * 128, ov, (64 * w + 32 * ((s) - 8) + 8 * (i)) * 1024 + 128 * ct); } \
    else if ((i) < 2) blds16(rx, sb + w * 2048 + (i) * 1024, xv, xs0 + (((s) + ROT) & 7) * 128 + (i) * 8192); \
    else blds16(rw, sb + 16384 + ((i) - 2) * 8192 + w * 1024, wv, ws0 + (((s) + ROT) & 7) * 64 * 3072 + ((i) - 2) * 1024); } while (0)
#define ISSUE(s) do { ISS(s, 0); ISS(s, 1); ISS(s, 2); ISS(s, 3); ISS(s, 4); } while (0)
  gu64* sync_z = (gu64*)(ws + WS_SYNC + 128 + hg * 128);
  ISSUE(0);
  SCHED(); RAW_BAR(); SCHED();
  u32 pv2 = 0;
  const rsrc_t rws = mk_rsrc(ws, WS_USED);
  const bool clear = tt == 0 && w == 7;
  if (clear) {
    const u32x4 z4 = {0u, 0u, 0u, 0u};
#pragma unroll
    for (int i = 0; i < 4; ++i) store16(z4, rws, lane * 16 + (int)WS_KV + hg * 4352 + 1024 * i);
    store16(z4, rws, (lane & 15) * 16 + (int)WS_KV + hg * 4352 + 4096);
  }
  SCHED();
  ISSUE(1);
  SCHED();
  epoch = load_epoch(ws);
  SCHED();
  const int xo = (64 * wm + l15) * 128 + ((g ^ (lane & 7)) << 4);
  const int q4 = l15 >> 2, p4 = lane & 3;
  const int wo_ = 16384 + (8 * g + q4) * 128 + ((wh ^ (((q4 >> 1) & 1) | ((g & 1) << 1))) << 5) + 8 * p4;
  f32x4 aq[4], ak[4], av[4];
#pragma unroll
  for (int i = 0; i < 4; ++i) { aq[i] = (f32x4){EPS, EPS, EPS, EPS}; ak[i] = aq[i]; av[i] = (f32x4){0, 0, 0, 0}; }
  s16x8 xa[4], wa[3], xb[4], wb[3];
#define LDWF(wf, s, ks, st) wf[st] = cat8(tr_read(smem + ((s) & 3) * STAGE + wo_ + (st) * 8192 + (ks) * 4096), tr_read(smem + ((s) & 3) * STAGE + wo_ + (st) * 8192 + (ks) * 4096 + 512))
#define LDXF(xf, s, ks, mt) xf[mt] = LD(s16x8, smem + ((s) & 3) * STAGE + ((xo ^ ((ks) * 64)) + (mt) * 2048))
#define LOADF(xf, wf, s, ks) do { LDWF(wf, s, ks, 0); LDWF(wf, s, ks, 1); LDWF(wf, s, ks, 2); \
    LDXF(xf, s, ks, 0); LDXF(xf, s, ks, 1); LDXF(xf, s, ks, 2); LDXF(xf, s, ks, 3); } while (0)
#define MM3(xf, wf, mt) do { aq[mt] = MFMA16(wf[0], xf[mt], aq[mt]); ak[mt] = MFMA16(xf[mt], wf[1], ak[mt]); av[mt] = MFMA16(xf[mt], wf[2], av[mt]); \
    asm volatile("" : "+v"(aq[mt]), "+v"(ak[mt]), "+v"(av[mt])); } while (0)
#define HALF(xf, wf, xn, wn, ls, lk, is) do { SCHED(); MM3(xf, wf, 0); SCHED(); \
    if ((ls) >= 0) { LDWF(wn, ls, lk, 0); LDXF(xn, ls, lk, 0); LDWF(wn, ls, lk, 1); } if ((is) >= 0) ISS(is, 0); SCHED(); MM3(xf, wf, 1); SCHED(); \
    if ((ls) >= 0) { LDWF(wn, ls, lk, 2); LDXF(xn, ls, lk, 1); LDXF(xn, ls, lk, 2); } if ((is) >= 0) ISS(is, 1); SCHED(); MM3(xf, wf, 2); SCHED(); \
    if ((ls) >= 0) LDXF(xn, ls, lk, 3); if ((is) >= 0) { ISS(is, 2); ISS(is, 3); } SCHED(); MM3(xf, wf, 3); SCHED(); \
    if ((is) >= 0) ISS(is, 4); SCHED(); } while (0)
#define STEP(s, vm, i1, i2) do { WAIT_L0(); HALF(xa, wa, xb, wb, s, 1, i1); \
    WAIT_L0(); WAIT_VM(vm); RAW_BAR(); HALF(xb, wb, xa, wa, (s) + 1, 0, i2); } while (0)
  WAIT_VM(5);
  RAW_BAR();
  LOADF(xa, wa, 0, 0);
  STEP(0, 5, 2, -1);
  if (clear && lane == 0) __hip_atomic_store(sync_z, next_tag(epoch), RLX_AGENT);
  STEP(1, 5, 3, -1);
  STEP(2, 5, 4, 5);
  STEP(3, 5, -1, 6);
  if (w == 0) pv2 = touch_pages(ws, out, rt, lane);
  STEP(4, 5, -1, 7);
  u64 ready = __hip_atomic_load(sync_z, RLX_AGENT);
  STEP(5, 5, -1, 8);
  if (w < 4) __builtin_amdgcn_s_setprio(2);
  STEP(6, 8, 9, -1);
  {
    const u64 tag = next_tag(epoch);
#pragma unroll 1
    for (int spin = 0; ready != tag && spin < (1 << 22); ++spin) {
      __builtin_amdgcn_s_sleep(2); ready = __hip_atomic_load(sync_z, RLX_AGENT); __builtin_amdgcn_s_waitcnt(0x0F70);
    }
  }
  WAIT_L0(); HALF(xa, wa, xb, wb, 7, 1, -1); WAIT_L0(); SCHED();
#pragma unroll
  for (int mt = 0; mt < 4; ++mt) aq[mt] = MFMA16(wb[0], xb[mt], aq[mt]);
  asm volatile("" : "+v"(aq[0]), "+v"(aq[1]), "+v"(aq[2]), "+v"(aq[3]));
  SCHED();
#pragma unroll
  for (int mt = 0; mt < 4; ++mt) { ak[mt] = MFMA16(xb[mt], wb[1], ak[mt]); av[mt] = MFMA16(xb[mt], wb[2], av[mt]); }
  asm volatile("" : "+v"(ak[0]), "+v"(ak[1]), "+v"(ak[2]), "+v"(ak[3]), "+v"(av[0]), "+v"(av[1]), "+v"(av[2]), "+v"(av[3]));
  SCHED();
  asm volatile("" :: "v"(pv2));
#undef ISS
#undef ISSUE
#undef LOADF
#undef MM3
#undef HALF
#undef LDWF
#undef LDXF
#undef STEP
  const int h = 4 * hg + wh;
  const int pqv = (g >> 1) * 512 + l15 * 16 + (g & 1) * 256, pqs = (int)WS_PQ + (tt * 4 + 2 * wm) * PQP + h * 1024;
#pragma unroll
  for (int pr = 0; pr < 2; ++pr) {
    const f32x4 q0 = aq[2 * pr], q1 = aq[2 * pr + 1];
    const auto s0 = __builtin_amdgcn_permlane16_swap(pk_bf16(phi_f(q0[0]), phi_f(q0[1])), pk_bf16(phi_f(q1[0]), phi_f(q1[1])), false, false);
    const auto s1 = __builtin_amdgcn_permlane16_swap(pk_bf16(phi_f(q0[2]), phi_f(q0[3])), pk_bf16(phi_f(q1[2]), phi_f(q1[3])), false, false);
    store16((u32x4){s0[0], s1[0], s0[1], s1[1]}, rws, pqv + pqs + pr * PQP);
  }
  SCHED();
  f32x4 kvt = {0, 0, 0, 0}, zz = {0, 0, 0, 0};
  const u32x4 ones_u = {0x3f803f80u, 0x3f803f80u, 0x3f803f80u, 0x3f803f80u};
#pragma unroll
  for (int pr = 0; pr < 2; ++pr) {
    u32x4 pk, vv;
#pragma unroll
    for (int m = 0; m < 2; ++m) {
      const f32x4 kk = ak[2 * pr + m], v4 = av[2 * pr + m];
      pk[2 * m] = pk_bf16(phi_f(kk[0]), phi_f(kk[1])); pk[2 * m + 1] = pk_bf16(phi_f(kk[2]), phi_f(kk[3]));
      vv[2 * m] = pk_bf16(v4[0], v4[1]); vv[2 * m + 1] = pk_bf16(v4[2], v4[3]);
    }
    kvt = MFMA16(__builtin_bit_cast(s16x8, vv), __builtin_bit_cast(s16x8, pk), kvt);
    zz = MFMA16(__builtin_bit_cast(s16x8, ones_u), __builtin_bit_cast(s16x8, pk), zz);
  }
  lptr red = smem + 4 * WOQ;
#pragma unroll
  for (int r = 0; r < 4; ++r) ST(float, red + (wm * 4 + wh) * 1088 + l15 * 4 + (4 * g + r) * 64) = kvt[r];
  if (g == 0) ST(float, red + (wm * 4 + wh) * 1088 + l15 * 4 + 1024) = zz[0];
  LBAR();
  float* kvacc = (float*)(ws + WS_KV) + hg * 1088 + tid;
  atomicAdd(kvacc, LD(float, red + tid * 4) + LD(float, red + 4352 + tid * 4));
  atomicAdd(kvacc + 512, LD(float, red + 2048 + tid * 4) + LD(float, red + 4352 + 2048 + tid * 4));
  if (tid < 64) atomicAdd(kvacc + 1024, LD(float, red + 4096 + tid * 4) + LD(float, red + 4352 + 4096 + tid * 4));
}

static constexpr int TB_ROW = 544, TB_BUF = 16 * TB_ROW, TB_CH = 2 * TB_BUF;
static constexpr int KR_OFF = 4 * TB_CH, KR_CP = 4160, KR_CH = 2 * KR_CP;
static constexpr int U_OFF = KR_OFF + 4 * KR_CH, U_CH = (2048 + 240) * 2;
static constexpr int EX_OFF = U_OFF + 4 * U_CH;
static_assert(EX_OFF + 32768 <= 163840 && TB_CH == WOQ && P2_EX + 65536 <= 163840, "LDS map");

DI void conv_role(const u16* __restrict__ x, u32 (&pr)[5], u32x2 (&xr)[8], u32 pf, const u16* __restrict__ dsk, const u16* __restrict__ gw,
                  const u16* __restrict__ wo, char* __restrict__ ws, const u16* out, int rt, int cg, int ct, u64& epoch, lptr smem) {
  const int tid = threadIdx.x, lane = tid & 63;
  const int w = __builtin_amdgcn_readfirstlane(tid >> 6);
  const int ch = w & 3, half = w >> 2, g = lane >> 4, l15 = lane & 15;
  const int c = 4 * cg + ch;
  const rsrc_t rx = mk_rsrc(x, SEQ * DM * 2);
  u32 dsk2, pv = 0;
  u32x4 gx[4], gwv;
  const rsrc_t rws = mk_rsrc(ws, WS_USED);
  f32x4 acc[8];
#pragma unroll
  for (int i = 0; i < 8; ++i) acc[i] = (f32x4){0, 0, 0, 0};
#define PINC() do { asm volatile("" : "+v"(acc[0]), "+v"(acc[1]), "+v"(acc[2]), "+v"(acc[3]), "+v"(acc[4]), "+v"(acc[5]), "+v"(acc[6]), "+v"(acc[7])); SCHED(); } while (0)
  if (w < 4) {
    const int cw = 4 * cg + w;
    if (lane < 8) ST(u32x4, smem + KR_OFF + w * KR_CH + (lane >> 2) * KR_CP + 4096 + 16 * (lane & 3)) = (u32x4){0u, 0u, 0u, 0u};
    __builtin_amdgcn_s_setprio(2);
    asm volatile("s_waitcnt vmcnt(0)" : "+v"(pr[0]), "+v"(pr[1]), "+v"(pr[2]), "+v"(pr[3]), "+v"(pr[4]));
    const u32 lar2 = pr[0], aim2 = pr[1], cre2 = pr[2], cim2 = pr[3], ldt_u = pr[4];
    const float dt = rbf(exp_f(bf_lo(ldt_u)));
    float al[2], th[2];
    al[0] = -rbf(exp_f(bf_lo(lar2))) * dt * LOG2E; al[1] = -rbf(exp_f(bf_hi(lar2))) * dt * LOG2E;
    th[0] = bf_lo(aim2) * dt * 0.15915494309189535f; th[1] = bf_hi(aim2) * dt * 0.15915494309189535f;
    u32 ce = wave_umax(max(max(cre2 & 0x7f80u, (cre2 >> 16) & 0x7f80u), max(cim2 & 0x7f80u, (cim2 >> 16) & 0x7f80u))) >> 7;
    ce = ce < 1 ? 1 : (ce > 253 ? 253 : ce);
    const float cinv = __builtin_bit_cast(float, (254u - ce) << 23);
    const float kscale = 2.f * dt * __builtin_bit_cast(float, ce << 23);
    lptr tb = smem + w * TB_CH + lane * 8, tr = smem + w * TB_CH + 16 * g, kr = smem + KR_OFF + w * KR_CH;
#define PUT(buf, e, v) ST(u32x2, tb + (buf) * TB_BUF + (e) * TB_ROW) = (u32x2){pk_f16(v[0]), pk_f16(v[1])}
#define CONJ(v) ((f32x2){(v).x, -(v).y})
#define GETF(f, buf, row) do { lptr p_ = tr + (buf) * TB_BUF + (row) * TB_ROW; \
      _Pragma("unroll") for (int ks = 0; ks < 8; ++ks) f[ks] = LD(h16x8, p_ + 64 * ks); } while (0)
#define TILE(d, f) do { d = (f32x4){0, 0, 0, 0}; _Pragma("unroll") for (int ks = 0; ks < 8; ++ks) d = MFMA16H(f[ks], fx[ks], d); } while (0)
    f32x2 y[17][2], wB[2], w16[2], w32[2], xa[2], xb[2], aS[6][2], t[2], t2[2];
#pragma unroll
    for (int m = 0; m < 2; ++m) {
      y[1][m] = CONJ(cexp_p(al[m], th[m], 1.f)); w16[m] = cexp_p(al[m], th[m], 16.f); wB[m] = CONJ(cexp_p(al[m], th[m], 256.f));
      y[16][m] = CONJ(w16[m]);
    }
    xa[0] = (f32x2){bf_lo(cre2) * cinv, bf_lo(cim2) * cinv}; xa[1] = (f32x2){bf_hi(cre2) * cinv, bf_hi(cim2) * cinv};
    ST(u32x2, tb + TB_BUF) = (u32x2){0x3c00u, 0x3c00u};
    PUT(0, 0, xa); PUT(1, 1, y[1]);
    cmul4(xb, xa, w16, w32, w16, w16); cmul4(y[2], y[1], y[1], aS[0], wB, wB);
    PUT(0, 1, xb); PUT(1, 2, y[2]);
#pragma unroll
    for (int i = 1; i < 8; ++i) {
      cmul4(xa, xa, w32, xb, xb, w32);
      if (i < 7) cmul4(y[2 * i + 1], y[2 * i - 1], y[2], y[2 * i + 2], y[2 * i], y[2]); else cmul2(y[15], y[13], y[2]);
      PUT(0, 2 * i, xa); PUT(0, 2 * i + 1, xb); PUT(1, 2 * i + 1, y[2 * i + 1]);
      if (i < 7) PUT(1, 2 * i + 2, y[2 * i + 2]);
    }
    h16x8 fx[8], fa[8], fb[8];
    f32x4 kd;
    const int gq = l15 >> 2, r3 = min(l15 & 3, 2), yk = 2044 - 16 * l15 - 4 * g;
    SCHED();
    GETF(fx, 0, l15); GETF(fa, 1, l15);
    SCHED();
    PUT(0, 0, wB);
#pragma unroll
    for (int m = 1; m < 9; m += 2) {
      cmul4(t, wB, y[2 * m], t2, wB, y[2 * m + 2]);
      if (m == 1) cmul4(aS[1], aS[0], wB, aS[2], aS[0], aS[0]);
      if (m == 3) cmul4(aS[3], aS[2], wB, aS[4], aS[2], aS[0]);
      if (m == 5) cmul2(aS[5], aS[4], wB);
      PUT(0, m, t); PUT(0, m + 1, t2);
    }
    SCHED();
    GETF(fb, 0, (int)((0x8766654443222100ull >> (4 * l15)) & 15));
    TILE(kd, fa);
    SCHED();
#pragma unroll
    for (int s = 0; s < 2; ++s) {
      PUT(1, 5 * s, aS[s]);
#pragma unroll
      for (int m = 1; m < 5; m += 2) { cmul4(t, aS[s], y[4 * m], t2, aS[s], y[4 * m + 4]); PUT(1, 5 * s + m, t); PUT(1, 5 * s + m + 1, t2); }
    }
    {
      const f32x4 k0 = kd * kscale;
      const u32 p32 = pk_bf16(k0[3], k0[2]), p10 = pk_bf16(k0[1], k0[0]);
      ST(u32x2, kr + 2 * yk) = (u32x2){p32, p10};
      ST(u16, kr + KR_CP + 2 * yk + 2) = (u16)p32;
      ST(u32, kr + KR_CP + 2 * yk + 4) = pk_bf16(k0[2], k0[1]);
      ST(u16, kr + KR_CP + 2 * yk + 8) = (u16)(p10 >> 16);
    }
    SCHED();
    GETF(fa, 1, 5 * (gq >> 1) + 2 * (gq & 1) + r3);
    TILE(kd, fb);
    SCHED();
#pragma unroll
    for (int s = 2; s < 6; ++s) {
      cmul4(t, aS[s], y[8], t2, aS[s], y[16]);
      PUT(0, 3 * (s - 2), aS[s]); PUT(0, 3 * (s - 2) + 1, t); PUT(0, 3 * (s - 2) + 2, t2);
    }
    {
      const f32x4 k1 = kd * kscale;
      const u32 q10 = pk_bf16(k1[1], k1[0]);
      ST(u32x2, kr + 2 * (yk - 256)) = (u32x2){pk_bf16(k1[3], k1[2]), q10};
      ST(u32x2, kr + KR_CP + 2 * (yk - 256)) = (u32x2){pk_bf16(k1[3], k1[3]), pk_bf16(k1[2], k1[1])};
      if (lane == 0) ST(u16, kr + KR_CP + 2 * 1792) = (u16)q10;
    }
    SCHED();
    GETF(fb, 0, 3 * gq + r3);
    TILE(kd, fa);
    {
      const f32x4 k0 = kd * kscale;
      const int y2 = 2040 - (512 + 256 * (g >> 1) + 16 * l15 + 8 * (g & 1));
      const u32 b2 = pk_bf16(k0[2], k0[2]), b1 = pk_bf16(k0[1], k0[1]), b0 = pk_bf16(k0[0], k0[0]);
      ST(u32x4, kr + 2 * y2) = (u32x4){b2, b1, pk_bf16(k0[1], k0[0]), b0};
      ST(u32x4, kr + KR_CP + 2 * y2) = (u32x4){b2, pk_bf16(k0[2], k0[1]), b1, b0};
    }
    TILE(kd, fb);
    {
      const f32x4 k1 = kd * kscale;
      const int y3 = 2032 - (1024 + 256 * g + 16 * l15);
      const u32 a2 = pk_bf16(k1[2], k1[2]), a1 = pk_bf16(k1[1], k1[1]), a0 = pk_bf16(k1[0], k1[0]);
      ST(u32x4, kr + 2 * y3) = (u32x4){a2, a2, a1, a1};
      ST(u32x4, kr + 2 * y3 + 16) = (u32x4){a1, pk_bf16(k1[1], k1[0]), a0, a0};
      ST(u32x4, kr + KR_CP + 2 * y3) = (u32x4){a2, a2, pk_bf16(k1[2], k1[1]), a1};
      ST(u32x4, kr + KR_CP + 2 * y3 + 16) = (u32x4){a1, a1, a0, a0};
    }
#undef PUT
#undef GETF
#undef TILE
    SCHED();
    __builtin_amdgcn_s_setprio(0);
  } else {
    const int t2 = (tid + 16 * cg) & 255;
    if (lane < 30) ST(u32x4, smem + U_OFF + ch * U_CH + lane * 16) = (u32x4){0u, 0u, 0u, 0u};
#pragma unroll
    for (int hf = 0; hf < 2; ++hf) {
      lptr ub = smem + U_OFF + 480 + t2 * 8 + hf * 2048;
      if (hf == 0) asm volatile("s_waitcnt vmcnt(4)" : "+v"(xr[0]), "+v"(xr[1]), "+v"(xr[2]), "+v"(xr[3]) : "v"(pf));
      else asm volatile("s_waitcnt vmcnt(0)" : "+v"(xr[4]), "+v"(xr[5]), "+v"(xr[6]), "+v"(xr[7]));
      const u32x2 x0 = xr[4 * hf], x1 = xr[4 * hf + 1], x2 = xr[4 * hf + 2], x3 = xr[4 * hf + 3];
      ST(u32x2, ub + 0 * U_CH) = (u32x2){__builtin_amdgcn_perm(x1.x, x0.x, 0x05040100u), __builtin_amdgcn_perm(x3.x, x2.x, 0x05040100u)};
      ST(u32x2, ub + 1 * U_CH) = (u32x2){__builtin_amdgcn_perm(x1.x, x0.x, 0x07060302u), __builtin_amdgcn_perm(x3.x, x2.x, 0x07060302u)};
      ST(u32x2, ub + 2 * U_CH) = (u32x2){__builtin_amdgcn_perm(x1.y, x0.y, 0x05040100u), __builtin_amdgcn_perm(x3.y, x2.y, 0x05040100u)};
      ST(u32x2, ub + 3 * U_CH) = (u32x2){__builtin_amdgcn_perm(x1.y, x0.y, 0x07060302u), __builtin_amdgcn_perm(x3.y, x2.y, 0x07060302u)};
      SCHED();
    }
  }
  lptr ka[8];
  {
    const int yb = 2031 - l15 + 8 * g - 128 * half - 96 - 1792;
    lptr k7 = smem + KR_OFF + ch * KR_CH + (yb & 1) * KR_CP + ((yb + 1) >> 1) * 4;
#pragma unroll
    for (int n = 0; n < 8; ++n) { ka[n] = k7 + 512 * (7 - n); asm volatile("" : "+v"(ka[n])); }
  }
  lptr ua = smem + U_OFF + ch * U_CH + (8 * g + 16 * l15 + 224 - 128 * half - 96) * 2;
  asm volatile("" : "+v"(ua));
  LBAR();
  {
    u32x4 A0[8], A1[8]; s16x8 B0[8], B1[8];
#define LOADC(A, B, rr) do { _Pragma("unroll") for (int n = 0; n < 8; ++n) { \
      lptr kp = ka[n] + 64 * (3 - (rr)); \
      A[n] = (u32x4){LD(u32, kp), LD(u32, kp + 4), LD(u32, kp + 8), LD(u32, kp + 12)}; \
      B[n] = LD(s16x8, ua + 64 * (3 - (rr)) + 512 * n); } } while (0)
#define MMC(A, B) do { _Pragma("unroll") for (int nu = 0; nu < 8; ++nu) _Pragma("unroll") for (int mu = 0; mu < 8 - nu; ++mu) \
      acc[nu + mu] = MFMA16(__builtin_bit_cast(s16x8, A[nu]), B[mu], acc[nu + mu]); } while (0)
#define MIXC() do { _Pragma("unroll") for (int q = 0; q < 12; ++q) { __builtin_amdgcn_sched_group_barrier(0x8, 2, 0); __builtin_amdgcn_sched_group_barrier(0x100, 2, 0); } \
      __builtin_amdgcn_sched_group_barrier(0x8, 12, 0); } while (0)
    LOADC(A0, B0, 0);
    SCHED();
    dsk2 = *(const u32*)(dsk + (c & ~1));
    epoch = load_epoch(ws);
    if (!half) {
#pragma unroll
      for (int r = 0; r < 4; ++r) gx[r] = __builtin_amdgcn_raw_buffer_load_b128(rx, lane * 16 + 1024 * r, (16 * cg + 4 * ch) * 1024, 0);
      gwv = *(const u32x4*)(gw + 8 * lane);
      pv = touch_pages(ws, out, rt, lane);
    }
    SCHED();
    if (half) fetch_wo(wo, ct, ch, lane, smem);
    SCHED();
    WAIT_L0(); SCHED(); LOADC(A1, B1, 1); MMC(A0, B0); MIXC(); PINC();
    WAIT_L0(); SCHED(); LOADC(A0, B0, 2); MMC(A1, B1); MIXC(); PINC();
    WAIT_L0(); SCHED(); LOADC(A1, B1, 3); MMC(A0, B0); MIXC(); PINC();
    WAIT_L0(); SCHED(); MMC(A1, B1); PINC();
#undef LOADC
#undef MMC
#undef MIXC
#undef PINC
  }
  {
    lptr ex = smem + EX_OFF + w * 4096 + lane * 16, ei = smem + EX_OFF + (w ^ 4) * 4096 + lane * 16;
    f32x4 keep[4];
    if (half) {
#pragma unroll
      for (int i = 0; i < 4; ++i) { ST(f32x4, ex + 1024 * i) = acc[4 + i]; keep[i] = acc[i]; }
    } else {
#pragma unroll
      for (int i = 0; i < 4; ++i) { ST(f32x4, ex + 1024 * i) = acc[i]; keep[i] = acc[4 + i]; }
    }
    lptr up = smem + U_OFF + ch * U_CH + 480 + 2 * (16 * l15 + 4 * g) + 2048 * (1 - half);
    const float dsk_f = (c & 1) ? bf_hi(dsk2) : bf_lo(dsk2);
#pragma unroll
    for (int i = 0; i < 4; ++i) {
      const u32x2 uu = LD(u32x2, up + 512 * i);
      keep[i] += (f32x4){bf_lo(uu.x), bf_hi(uu.x), bf_lo(uu.y), bf_hi(uu.y)} * dsk_f;
    }
    SCHED();
    if (!half) {
      asm volatile("" :: "v"(pv));
      float sg[4];
#pragma unroll
      for (int r = 0; r < 4; ++r) {
        float s0 = 0.f;
#pragma unroll
        for (int i = 0; i < 4; ++i) s0 = __builtin_amdgcn_fdot2_f32_bf16(u2b(gx[r][i]), u2b(gwv[i]), s0, false);
        sg[r] = rbf(__builtin_amdgcn_rcpf(1.f + __builtin_amdgcn_exp2f(-LOG2E * rbf(wave_sum(s0)))));
      }
      if (lane == 63) store16(__builtin_bit_cast(u32x4, (f32x4){sg[0], sg[1], sg[2], sg[3]}), rws, (int)WS_G + (16 * cg + 4 * ch) * 4 + (lane - 63));
    }
    const int bo = (int)WS_BT + c * BTP + 2048 * (1 - half) + 32 * l15 + 16 * (g >> 1) + 512 * (g & 1);
    SCHED();
    LBAR();
    f32x4 o4[4];
#pragma unroll
    for (int i = 0; i < 4; ++i) o4[i] = LD(f32x4, ei + 1024 * i);
    SCHED();
#pragma unroll
    for (int i = 0; i < 2; ++i) {
      const f32x4 y0 = keep[2 * i] + o4[2 * i], y1 = keep[2 * i + 1] + o4[2 * i + 1];
      const auto sx = __builtin_amdgcn_permlane16_swap(pk_bf16(y0[0], y0[1]), pk_bf16(y1[0], y1[1]), false, false);
      const auto sy = __builtin_amdgcn_permlane16_swap(pk_bf16(y0[2], y0[3]), pk_bf16(y1[2], y1[3]), false, false);
      store16((u32x4){sx[0], sy[0], sx[1], sy[1]}, rws, bo + 1024 * i);
    }
    asm volatile(".Lfused_code_tail:");
  }
}

struct Tags { u64 a, b; };
DI Tags poll_load(char* ws, int off, int lane) {
  gu64* p = (gu64*)(ws + WS_FLAG + off) + lane;
  return Tags{__hip_atomic_load(p, RLX_AGENT), __hip_atomic_load(p + 64, RLX_AGENT)};
}
DI void poll_wait(char* ws, int off, int lane, u64 tag, Tags& f, u64 ma, u64 mb) {
#pragma unroll 1
  for (int spin = 0; spin < (1 << 20); ++spin) {
    if ((__ballot(f.a != tag) & ma) == 0 && (__ballot(f.b != tag) & mb) == 0) break;
    __builtin_amdgcn_s_sleep(2);
    f = poll_load(ws, off, lane);
    __builtin_amdgcn_s_waitcnt(0x0F70);
  }
}

DI void out_role(char* __restrict__ ws, u16* __restrict__ out, int rt, int ct, u64 tag, Tags fl, lptr smem) {
  const int tid = threadIdx.x, lane = tid & 63;
  const int w = __builtin_amdgcn_readfirstlane(tid >> 6);
  const int th = w & 1, kq = w >> 1, hh = lane >> 5, l31 = lane & 31;
  const rsrc_t rws = mk_rsrc(ws, WS_USED);
  const int foff = (blockIdx.x & (NREP - 1)) * FPITCH;
  if (w == 0) poll_wait(ws, foff, lane, tag, fl, ~0ull, ~0ull);
  RAW_BAR();
  if (w == 1) fl = poll_load(ws, foff + 1024, lane);
  f32x4 kvr[5];
#pragma unroll
  for (int i = 0; i < 4; ++i) kvr[i] = __builtin_bit_cast(f32x4, __builtin_amdgcn_raw_buffer_load_b128(rws, tid * 16, (int)WS_KV + 8192 * i, 16));
  kvr[4] = __builtin_bit_cast(f32x4, __builtin_amdgcn_raw_buffer_load_b128(rws, (tid & 127) * 16, (int)WS_KV + 32768, 16));
  SCHED(); RAW_BAR(); SCHED();
  u32x4 pf[8];
#define LOAD_PQ(i0, i1) do { _Pragma("unroll") for (int i = i0; i < i1; ++i) \
    pf[i] = __builtin_amdgcn_raw_buffer_load_b128(rws, lane * 16, (int)WS_PQ + (2 * rt + th) * PQP + (8 * kq + i) * 1024, 16); } while (0)
  LOAD_PQ(0, 3);
#pragma unroll
  for (int i = 0; i < 5; ++i) {
    const f32x4 v = kvr[i];
    ST(u32x2, smem + P2_KV + (i < 4 ? tid + 512 * i : 2048 + (tid & 127)) * 8) = (u32x2){pk_bf16(v[0], v[1]), pk_bf16(v[2], v[3])};
  }
  f32x16 acc[2];
#pragma unroll
  for (int i = 0; i < 16; ++i) { acc[0][i] = 0.f; acc[1][i] = 0.f; }
  f32x16 cinit = acc[0];
  cinit[8] = EPS;
  lptr kva = smem + P2_KV + 8 * kq * 544 + min(l31, 16) * 32 + hh * 16;
  const int blk = (lane >> 4) & 1, q4 = (lane & 15) >> 2, p4 = lane & 3;
  const int sw = ((q4 >> 1) & 1) << 6;
  lptr wa0 = smem + P2_WO + kq * WOQ + (4 * hh + q4) * 128 + blk * 32 + p4 * 8 + sw;
  lptr wa1 = smem + P2_WO + kq * WOQ + (4 * hh + q4) * 128 + blk * 32 + p4 * 8 + (64 ^ sw);
  const u64 mct = 0xffffull << (16 * ct & 63), mrt = 0xfull << (4 * rt & 63);
  const u64 ma = (ct < 4 ? mct : 0ull) | (rt < 16 ? mrt : 0ull), mb = (ct < 4 ? 0ull : mct) | (rt < 16 ? 0ull : mrt);
  if (w == 1) { const u32 ok = (__ballot(fl.a != tag) & ma) == 0 && (__ballot(fl.b != tag) & mb) == 0; if (lane == 0) ST(u32, smem + P2_OK) = ok; }
  LBAR();
  LOAD_PQ(3, 8);
#undef LOAD_PQ
  const bool early = __builtin_amdgcn_readfirstlane(LD(u32, smem + P2_OK)) != 0;
  u32x4 btr; float gt;
#define LOAD_B() do { \
    btr = __builtin_amdgcn_raw_buffer_load_b128(rws, (lane >> 2) * BTP + (lane & 3) * 16, (int)WS_BT + (64 * ct + 16 * kq) * BTP + 128 * rt + 64 * th, 16); \
    gt = u2f(__builtin_amdgcn_raw_buffer_load_b32(rws, l31 * 4, (int)WS_G + (64 * rt + 32 * th) * 4, 16)); } while (0)
  if (early) LOAD_B();
  u32x2 bb[2];
  lptr ba = smem + P2_BT + w * 1024 + (4 * hh + q4) * 64 + blk * 32 + p4 * 8;
#define TURN_B() do { ST(u32x4, smem + P2_BT + w * 1024 + lane * 16) = btr;        \
    bb[0] = __builtin_bit_cast(u32x2, tr_read(ba)); bb[1] = __builtin_bit_cast(u32x2, tr_read(ba + 512)); } while (0)
  {
    f32x16 d[2]; s16x8 kf[8], wf[2][2]; u32x4 of[2];
#define OMM(i) do { d[(i) & 1] = MFMA32(kf[i], __builtin_bit_cast(s16x8, pf[i]), cinit); asm volatile("" : "+v"(d[(i) & 1])); } while (0)
#define NORM(i) do { const f32x16& dd = d[(i) & 1]; const float inv = __builtin_amdgcn_rcpf(dd[8]); \
      of[(i) & 1] = (u32x4){pk_bf16(dd[0] * inv, dd[1] * inv), pk_bf16(dd[2] * inv, dd[3] * inv), pk_bf16(dd[4] * inv, dd[5] * inv), pk_bf16(dd[6] * inv, dd[7] * inv)}; \
      asm volatile("" : "+v"(of[(i) & 1]) : "v"(dd)); } while (0)
#define LDW(i) do { wf[(i) & 1][0] = cat8(tr_read(wa0 + (i) * 2048), tr_read(wa0 + (i) * 2048 + 1024)); \
      wf[(i) & 1][1] = cat8(tr_read(wa1 + (i) * 2048), tr_read(wa1 + (i) * 2048 + 1024)); } while (0)
#define AMM(i) do { acc[0] = MFMA32(wf[(i) & 1][0], __builtin_bit_cast(s16x8, of[(i) & 1]), acc[0]); \
      acc[1] = MFMA32(wf[(i) & 1][1], __builtin_bit_cast(s16x8, of[(i) & 1]), acc[1]); asm volatile("" : "+v"(acc[0]), "+v"(acc[1])); } while (0)
#pragma unroll
    for (int i = 0; i < 8; ++i) kf[i] = LD(s16x8, kva + i * 544);
    LDW(0);
    SCHED();
    OMM(0); OMM(1); SCHED(); NORM(0); SCHED();
#pragma unroll
    for (int i = 0; i < 8; ++i) {
      if (i + 1 < 8) LDW(i + 1);
      SCHED();
      AMM(i);
      if (i + 2 < 8) OMM(i + 2);
      SCHED();
      if (i + 1 < 8) NORM(i + 1);
      SCHED();
      if (i == 5 && early) { TURN_B(); SCHED(); }
    }
#undef OMM
#undef NORM
#undef LDW
#undef AMM
  }
  f32x4 own[2];
#pragma unroll
  for (int q = 0; q < 4; ++q) {
    lptr ex = smem + P2_EX + ((2 * q + th) * 4 + kq) * 2048 + lane * 16;
    const f32x16& s = acc[q >> 1];
    const f32x4 s0 = {s[8 * (q & 1)], s[8 * (q & 1) + 1], s[8 * (q & 1) + 2], s[8 * (q & 1) + 3]};
    const f32x4 s1 = {s[8 * (q & 1) + 4], s[8 * (q & 1) + 5], s[8 * (q & 1) + 6], s[8 * (q & 1) + 7]};
    if (q == kq) { own[0] = s0; own[1] = s1; } else { ST(f32x4, ex) = s0; ST(f32x4, ex + 1024) = s1; }
  }
  if (!early && w == 1) poll_wait(ws, foff + 1024, lane, tag, fl, ma, mb);
  LBAR();
  if (!early) { LOAD_B(); TURN_B(); }
#undef TURN_B
#undef LOAD_B
  lptr ei = smem + P2_EX + w * 8192 + lane * 16;
  const float g1 = rbf(1.f - gt);
  u32x2 res[2];
#pragma unroll
  for (int i = 0; i < 2; ++i) {
    const f32x4 o = (own[i] + LD(f32x4, ei + ((kq ^ 1) * 2048 + 1024 * i))) + (LD(f32x4, ei + ((kq ^ 2) * 2048 + 1024 * i)) + LD(f32x4, ei + ((kq ^ 3) * 2048 + 1024 * i)));
    res[i] = (u32x2){pk_bf16(gt * o[0] + g1 * bf_lo(bb[i].x), gt * o[1] + g1 * bf_hi(bb[i].x)), pk_bf16(gt * o[2] + g1 * bf_lo(bb[i].y), gt * o[3] + g1 * bf_hi(bb[i].y))};
  }
  const auto sx = __builtin_amdgcn_permlane32_swap(res[0].x, res[1].x, false, false);
  const auto sy = __builtin_amdgcn_permlane32_swap(res[0].y, res[1].y, false, false);
  *(u32x4*)(out + (size_t)(64 * rt + 32 * th + l31) * DM + 64 * ct + 16 * kq + 8 * hh) = (u32x4){sx[0], sy[0], sx[1], sy[1]};
  if (blockIdx.x == 0 && w == 1) {
    poll_wait(ws, foff + 1024, lane, tag, fl, ~0ull, ~0ull);
    if (lane == 0) __hip_atomic_store((gu64*)(ws + WS_SYNC), tag, RLX_AGENT);
  }
}

__global__ __launch_bounds__(512, 2) void fused(const u16* __restrict__ x, const u16* __restrict__ wqkv, const u16* __restrict__ wo,
                                                const u16* __restrict__ lar, const u16* __restrict__ aim, const u16* __restrict__ cre,
                                                const u16* __restrict__ cim, const u16* __restrict__ ldt, const u16* __restrict__ dsk,
                                                const u16* __restrict__ gw, char* __restrict__ ws, u16* __restrict__ out) {
  extern __shared__ __attribute__((aligned(16))) char smem_[];
  lptr smem = (lptr)smem_;
  asm volatile("" :: "s"(x), "s"(wqkv), "s"(wo), "s"(lar), "s"(aim), "s"(cre), "s"(cim), "s"(ldt), "s"(dsk), "s"(gw), "s"(ws), "s"(out));
  const int b = blockIdx.x, xcd = b & 7, slot = b >> 3, role = slot & 1, idx = slot >> 1;
  const int rt = 8 * (xcd >> 1) + (slot & 7), ct = 4 * (xcd & 1) + (slot >> 3);
  const int tt = 4 * (xcd >> 1) + (idx & 3), hg = 4 * (xcd & 1) + (idx >> 2);
  const int cg = xcd * 16 + idx;
  int pofs = (threadIdx.x & 63) * 8;
  asm volatile("" : "+v"(pofs));
  u64 epoch; u32 tmp0, tmp1, tmp2; u64 pc;
  u32 pr[5]; u32x2 xr[8];
  asm volatile(
      "s_bitcmp1_b32 %[b], 3\n\t"
      "s_cbranch_scc0 .Lpre_end%=\n\t"
      "s_cmp_gt_u32 %[wv], 3\n\t"
      "s_cbranch_scc1 .Lpre_b%=\n\t"
      "v_lshlrev_b32 %[p4], 2, %[tid]\n\t"
      "v_lshl_add_u32 %[p4], %[cg], 10, %[p4]\n\t"
      "global_load_dword %[p0], %[p4], %[lar]\n\t"
      "global_load_dword %[p1], %[p4], %[aim]\n\t"
      "global_load_dword %[p2], %[p4], %[cre]\n\t"
      "global_load_dword %[p3], %[p4], %[cim]\n\t"
      "v_lshrrev_b32 %[t0], 7, %[p4]\n\t"
      "v_and_b32 %[t0], 0x3fe, %[t0]\n\t"
      "global_load_ushort %[p4], %[t0], %[ldt]\n\t"
      "s_barrier\n\t"
      "s_branch .Lpre_end%=\n"
      ".Lpre_b%=:\n\t"
      "s_getpc_b64 %[pc]\n\t"
      "v_and_b32 %[t0], 0xff, %[tid]\n\t"
      "v_lshlrev_b32 %[t0], 7, %[t0]\n\t"
      "v_mov_b32 %[t1], .Lfused_code_tail-.Lpre_b%=-4\n\t"
      "v_med3_i32 %[t0], 0, %[t0], %[t1]\n\t"
      "global_load_dword %[t2], %[t0], %[pc]\n\t"
      "v_lshl_add_u32 %[t0], %[cg], 4, %[tid]\n\t"
      "v_and_b32 %[t0], 0xff, %[t0]\n\t"
      "v_lshlrev_b32 %[t0], 12, %[t0]\n\t"
      "v_lshl_add_u32 %[t0], %[cg], 3, %[t0]\n\t"
      "v_add_u32 %[t1], 0x100000, %[t0]\n\t"
      "s_barrier\n\t"
      "global_load_dwordx2 %[x0], %[t0], %[x]\n\t"
      "global_load_dwordx2 %[x1], %[t0], %[x] offset:1024\n\t"
      "global_load_dwordx2 %[x2], %[t0], %[x] offset:2048\n\t"
      "global_load_dwordx2 %[x3], %[t0], %[x] offset:3072\n\t"
      "global_load_dwordx2 %[x4], %[t1], %[x]\n\t"
      "global_load_dwordx2 %[x5], %[t1], %[x] offset:1024\n\t"
      "global_load_dwordx2 %[x6], %[t1], %[x] offset:2048\n\t"
      "global_load_dwordx2 %[x7], %[t1], %[x] offset:3072\n"
      ".Lpre_end%=:"
      : [p0] "=&v"(pr[0]), [p1] "=&v"(pr[1]), [p2] "=&v"(pr[2]), [p3] "=&v"(pr[3]), [p4] "=&v"(pr[4]),
        [x0] "=&v"(xr[0]), [x1] "=&v"(xr[1]), [x2] "=&v"(xr[2]), [x3] "=&v"(xr[3]), [x4] "=&v"(xr[4]), [x5] "=&v"(xr[5]), [x6] "=&v"(xr[6]), [x7] "=&v"(xr[7]),
        [t0] "=&v"(tmp0), [t1] "=&v"(tmp1), [t2] "=&v"(tmp2), [pc] "=&s"(pc)
      : [b] "s"(b), [wv] "s"(__builtin_amdgcn_readfirstlane(threadIdx.x >> 6)), [tid] "v"(threadIdx.x), [cg] "s"(cg),
        [lar] "s"(lar), [aim] "s"(aim), [cre] "s"(cre), [cim] "s"(cim), [ldt] "s"(ldt), [x] "s"(x)
      : "scc", "memory");
  if (__builtin_expect(role, 0)) conv_role(x, pr, xr, tmp2, dsk, gw, wo, ws, out, rt, cg, ct, epoch, smem);
  else gemm_role(x, wqkv, wo, ct, ws, out, rt, tt, hg, epoch, smem);
  const int lane = threadIdx.x & 63, w = __builtin_amdgcn_readfirstlane(threadIdx.x >> 6);
  const u64 tag = next_tag(epoch);
  Tags fl = {0, 0};
  if (w == 0) {
    const rsrc_t rf = mk_rsrc(ws, WS_USED);
    fl.a = __builtin_bit_cast(u64, __builtin_amdgcn_raw_buffer_load_b64(rf, pofs, (int)WS_FLAG + (b & (NREP - 1)) * FPITCH, 16));
    fl.b = __builtin_bit_cast(u64, __builtin_amdgcn_raw_buffer_load_b64(rf, pofs, (int)WS_FLAG + (b & (NREP - 1)) * FPITCH + 512, 16));
  }
  SCHED();
  WAIT_VM(0);
  __builtin_amdgcn_s_setprio(0);
  LBAR();
  if (w == 7) __hip_atomic_store((gu64*)(ws + WS_FLAG + lane * FPITCH) + (role ? 128 + cg : tt * 8 + hg), tag, RLX_AGENT);
  out_role(ws, out, rt, ct, tag, fl, smem);
}

extern "C" void kernel_launch(void* const* d_in, const int* in_sizes, int n_in, void* d_out, int out_size, void* d_ws,
                              size_t ws_size, hipStream_t stream) {
  (void)in_sizes; (void)n_in; (void)out_size; (void)ws_size;
  (void)hipFuncSetAttribute((const void*)fused, hipFuncAttributeMaxDynamicSharedMemorySize, 163840);
  fused<<<256, 512, 163840, stream>>>((const u16*)d_in[0], (const u16*)d_in[1], (const u16*)d_in[2], (const u16*)d_in[4],
                                      (const u16*)d_in[5], (const u16*)d_in[6], (const u16*)d_in[7], (const u16*)d_in[8],
                                      (const u16*)d_in[9], (const u16*)d_in[10], (char*)d_ws, (u16*)d_out);
}
```
